```python
import torch

def _softmax(x, axis=-1):
    return torch.softmax(x, dim=axis)
def _gelu(x):
    return torch.nn.functional.gelu(x, approximate="tanh")
def _silu(x):
    return torch.nn.functional.silu(x)
def _sigmoid(x):
    return torch.sigmoid(x)
def _layernorm(x, axis=-1, eps=1e-5):
    m = x.mean(dim=axis, keepdim=True)
    v = x.var(dim=axis, keepdim=True, unbiased=False)
    return (x - m) / torch.sqrt(v + eps)
def _scatter_add(tgt, idx, src, axis=0):
    return tgt.clone().index_add_(axis, idx, src)
def _segment_sum(src, seg, n):
    out = torch.zeros((n,) + src.shape[1:], dtype=src.dtype)
    return out.index_add_(0, seg, src)
def _pad(x, pw):
    flat = tuple(v for pair in reversed(pw) for v in pair)
    return torch.nn.functional.pad(x, flat)

kernel_name = 'standalone__mha_softmax__near_ridge'

def setup_inputs():

    _w = (lambda a: (lambda t: t.to(torch.bfloat16) if t.is_floating_point() else t)(torch.as_tensor(a)))
    B, H, Sq, Sk, D = 16, 16, 512, 512, 256
    return {
        "q": _w((lambda *s: torch.randn(*s, dtype=torch.bfloat16) * 0.8)(B, H, Sq, D)),
        "k": _w((lambda *s: torch.randn(*s, dtype=torch.bfloat16) * 0.8)(B, H, Sk, D)),
        "v": _w((lambda *s: torch.randn(*s, dtype=torch.bfloat16) * 0.05)(B, H, Sk, D)),
    }

def reference(q, k, v):
    scale = q.shape[-1] ** -0.5
    scores = torch.einsum("bhqd,bhkd->bhqk", q, k) * scale
    p = _softmax(scores, axis=-1)
    return torch.einsum("bhqk,bhkd->bhqd", p, v)
```

```cpp
#include <hip/hip_runtime.h>
#include <hip/hip_bf16.h>
typedef short bf16x8 __attribute__((ext_vector_type(8)));
typedef short s16x4 __attribute__((ext_vector_type(4)));
typedef float f32x16 __attribute__((ext_vector_type(16)));
typedef unsigned u32x4 __attribute__((ext_vector_type(4)));
typedef unsigned u32x2 __attribute__((ext_vector_type(2)));
typedef short v4i16_t __attribute__((ext_vector_type(4)));
#define MFMA(a,b,c) __builtin_amdgcn_mfma_f32_32x32x16_bf16(a,b,c,0,0,0)
__device__ __forceinline__ unsigned cvtpk(float lo, float hi){ unsigned r; asm("v_cvt_pk_bf16_f32 %0, %1, %2":"=v"(r):"v"(lo),"v"(hi)); return r;}
constexpr int S=512, D=256;
__global__ __launch_bounds__(256) void attn_base(const unsigned short* __restrict__ Q, const unsigned short* __restrict__ K, const unsigned short* __restrict__ V, unsigned short* __restrict__ O){
  __shared__ __attribute__((aligned(16))) char lds[32768];
  const int tid=threadIdx.x, lane=tid&63, wid=tid>>6, r=lane&31, h=lane>>5;
  const int bid=blockIdx.x; const int xcd=bid&7, j=bid>>3; const int head=xcd*32+(j>>2), qb=j&3;
  const size_t hb=(size_t)head*S*D;
  const unsigned short* q=Q+hb+(size_t)(qb*128+wid*32+r)*D+h*8;
  bf16x8 qf[16];
#pragma unroll
  for(int s=0;s<16;++s) qf[s]=*(const bf16x8*)(q+s*16);
  f32x16 o[8];
#pragma unroll
  for(int i=0;i<8;++i) o[i]=f32x16{};
  float m=-1e30f,l=0.f; const float c=1.4426950408889634f/16.f;
  char* kl=lds; char* vl=lds+16384;
  for(int t=0;t<16;++t){
    __syncthreads();
#pragma unroll
    for(int i=0;i<4;++i){
      int idx=tid+256*i; int row=idx>>5, ch=idx&31;
      u32x4 kv=*(const u32x4*)(K+hb+(size_t)(t*32+row)*D+ch*8);
      u32x4 vv=*(const u32x4*)(V+hb+(size_t)(t*32+row)*D+ch*8);
      *(u32x4*)(kl+row*512+((ch^(row&15))<<4))=kv;
      *(u32x4*)(vl+((row>>3)*8+(ch>>2))*512+(row&7)*64+(ch&3)*16)=vv;
    }
    __syncthreads();
    f32x16 s=f32x16{};
#pragma unroll
    for(int ks=0;ks<16;++ks){ bf16x8 a=*(const bf16x8*)(kl+r*512+(((2*ks+h)^(r&15))<<4)); s=MFMA(a,qf[ks],s);}
    float mx=s[0];
#pragma unroll
    for(int i=1;i<16;++i) mx=fmaxf(mx,s[i]);
    mx=fmaxf(mx,__shfl_xor(mx,32));
    float mn=fmaxf(m,mx); float al=exp2f((m-mn)*c); m=mn; float ps=0;
#pragma unroll
    for(int i=0;i<16;++i){ s[i]=__builtin_amdgcn_exp2f((s[i]-mn)*c); ps+=s[i];}
    l=l*al+ps;
#pragma unroll
    for(int db=0;db<8;++db)
#pragma unroll
      for(int i=0;i<16;++i) o[db][i]*=al;
    u32x4 p0={cvtpk(s[0],s[1]),cvtpk(s[2],s[3]),cvtpk(s[4],s[5]),cvtpk(s[6],s[7])};
    u32x4 p1={cvtpk(s[8],s[9]),cvtpk(s[10],s[11]),cvtpk(s[12],s[13]),cvtpk(s[14],s[15])};
    const int i16=lane&15, qq=i16>>2, pp=i16&3, blk=(lane>>4)&1;
#pragma unroll
    for(int db=0;db<8;++db){
#pragma unroll
      for(int s2=0;s2<2;++s2){
        typedef __attribute__((address_space(3))) v4i16_t* lp;
        int k0=16*s2+4*h+qq, k1=k0+8;
        char* a0=vl+((k0>>3)*8+db)*512+(k0&7)*64+blk*32+pp*8;
        char* a1=vl+((k1>>3)*8+db)*512+(k1&7)*64+blk*32+pp*8;
        v4i16_t lo=__builtin_amdgcn_ds_read_tr16_b64_v4i16((lp)a0);
        v4i16_t hi=__builtin_amdgcn_ds_read_tr16_b64_v4i16((lp)a1);
        bf16x8 a=__builtin_shufflevector(lo,hi,0,1,2,3,4,5,6,7);
        o[db]=MFMA(a,__builtin_bit_cast(bf16x8,s2?p1:p0),o[db]);
      }
    }
  }
  l+=__shfl_xor(l,32);
  float il=1.f/l;
  unsigned short* op=O+hb+(size_t)(qb*128+wid*32+r)*D+4*h;
#pragma unroll
  for(int db=0;db<8;++db)
#pragma unroll
    for(int g=0;g<4;++g){
      u32x2 w={cvtpk(o[db][4*g]*il,o[db][4*g+1]*il),cvtpk(o[db][4*g+2]*il,o[db][4*g+3]*il)};
      *(u32x2*)(op+db*32+8*g)=w;
    }
}
extern "C" void kernel_launch(void* const* d_in, const int* in_sizes, int n_in, void* d_out, int out_size, void* d_ws, size_t ws_size, hipStream_t stream){
  attn_base<<<1024,256,0,stream>>>((const unsigned short*)d_in[0],(const unsigned short*)d_in[1],(const unsigned short*)d_in[2],(unsigned short*)d_out);
}
```

```cpp
#define RQ 0
#define RK 0
#define RV 0
#define WO 1
#define USEMARK 1
#include <hip/hip_runtime.h>
#include <hip/hip_bf16.h>
typedef short bf16x8 __attribute__((ext_vector_type(8)));
typedef short s16x4 __attribute__((ext_vector_type(4)));
typedef float f32x16 __attribute__((ext_vector_type(16)));
typedef unsigned u32x4 __attribute__((ext_vector_type(4)));
typedef unsigned u32x2 __attribute__((ext_vector_type(2)));
typedef short v4i16_t __attribute__((ext_vector_type(4)));
#define MFMA(a,b,c) __builtin_amdgcn_mfma_f32_32x32x16_bf16(a,b,c,0,0,0)
__device__ __forceinline__ unsigned cvtpk(float lo, float hi){ unsigned r; asm("v_cvt_pk_bf16_f32 %0, %1, %2":"=v"(r):"v"(lo),"v"(hi)); return r;}
constexpr int S=512, D=256;
__global__ __launch_bounds__(256) void attn_base(const unsigned short* __restrict__ Q, const unsigned short* __restrict__ K, const unsigned short* __restrict__ V, unsigned short* __restrict__ O){
  __shared__ __attribute__((aligned(16))) char lds[32768];
  const int tid=threadIdx.x, lane=tid&63, wid=tid>>6, r=lane&31, h=lane>>5;
  const int bid=blockIdx.x; const int xcd=bid&7, j=bid>>3; const int head=xcd*32+(j>>2), qb=j&3;
  const size_t hb=(size_t)head*S*D;
  const unsigned short* q=Q+hb+(size_t)(qb*128+wid*32+r)*D+h*8;
  bf16x8 qf[16];
#pragma unroll
  for(int s=0;s<16;++s) qf[s]=*(const bf16x8*)(q+s*16);
  f32x16 o[8];
#pragma unroll
  for(int i=0;i<8;++i) o[i]=f32x16{};
  float m=-1e30f,l=0.f; const float c=1.4426950408889634f/16.f;
  char* kl=lds; char* vl=lds+16384;
  for(int t=0;t<16;++t){
    __syncthreads();
#pragma unroll
    for(int i=0;i<4;++i){
      int idx=tid+256*i; int row=idx>>5, ch=idx&31;
      u32x4 kv=*(const u32x4*)(K+hb+(size_t)(t*32+row)*D+ch*8);
      u32x4 vv=*(const u32x4*)(V+hb+(size_t)(t*32+row)*D+ch*8);
      *(u32x4*)(kl+row*512+((ch^(row&15))<<4))=kv;
      *(u32x4*)(vl+((row>>3)*8+(ch>>2))*512+(row&7)*64+(ch&3)*16)=vv;
    }
    __syncthreads();
    f32x16 s=f32x16{};
#pragma unroll
    for(int ks=0;ks<16;++ks){ bf16x8 a=*(const bf16x8*)(kl+r*512+(((2*ks+h)^(r&15))<<4)); s=MFMA(a,qf[ks],s);}
    float mx=s[0];
#pragma unroll
    for(int i=1;i<16;++i) mx=fmaxf(mx,s[i]);
    mx=fmaxf(mx,__shfl_xor(mx,32));
    float mn=fmaxf(m,mx); float al=exp2f((m-mn)*c); m=mn; float ps=0;
#pragma unroll
    for(int i=0;i<16;++i){ s[i]=__builtin_amdgcn_exp2f((s[i]-mn)*c); ps+=s[i];}
    l=l*al+ps;
#pragma unroll
    for(int db=0;db<8;++db)
#pragma unroll
      for(int i=0;i<16;++i) o[db][i]*=al;
    u32x4 p0={cvtpk(s[0],s[1]),cvtpk(s[2],s[3]),cvtpk(s[4],s[5]),cvtpk(s[6],s[7])};
    u32x4 p1={cvtpk(s[8],s[9]),cvtpk(s[10],s[11]),cvtpk(s[12],s[13]),cvtpk(s[14],s[15])};
    const int i16=lane&15, qq=i16>>2, pp=i16&3, blk=(lane>>4)&1;
#pragma unroll
    for(int db=0;db<8;++db){
#pragma unroll
      for(int s2=0;s2<2;++s2){
        typedef __attribute__((address_space(3))) v4i16_t* lp;
        int k0=16*s2+4*h+qq, k1=k0+8;
        char* a0=vl+((k0>>3)*8+db)*512+(k0&7)*64+blk*32+pp*8;
        char* a1=vl+((k1>>3)*8+db)*512+(k1&7)*64+blk*32+pp*8;
        v4i16_t lo=__builtin_amdgcn_ds_read_tr16_b64_v4i16((lp)a0);
        v4i16_t hi=__builtin_amdgcn_ds_read_tr16_b64_v4i16((lp)a1);
        bf16x8 a=__builtin_shufflevector(lo,hi,0,1,2,3,4,5,6,7);
        o[db]=MFMA(a,__builtin_bit_cast(bf16x8,s2?p1:p0),o[db]);
      }
    }
  }
  l+=__shfl_xor(l,32);
  float il=1.f/l;
  unsigned short* op=O+hb+(size_t)(qb*128+wid*32+r)*D+4*h;
#pragma unroll
  for(int db=0;db<8;++db)
#pragma unroll
    for(int g=0;g<4;++g){
      u32x2 w={cvtpk(o[db][4*g]*il,o[db][4*g+1]*il),cvtpk(o[db][4*g+2]*il,o[db][4*g+3]*il)};
      *(u32x2*)(op+db*32+8*g)=w;
    }
}

__global__ __launch_bounds__(256) void stream_k(const u32x4* __restrict__ Q, const u32x4* __restrict__ K, const u32x4* __restrict__ V, u32x4* __restrict__ O, int n, unsigned long long* ts){
  unsigned long long t0=__builtin_amdgcn_s_memrealtime();
  u32x4 acc={0,0,0,0};
  for(int i=blockIdx.x*256+threadIdx.x;i<n;i+=2048*256){
    u32x4 a={1,2,3,4};
    if(RQ) a^=Q[i];
    if(RK) a^=K[i];
    if(RV) a^=V[i];
    if(WO) O[i]=a; else acc^=a;
  }
  if(!WO && acc[0]==0x12345678 && acc[1]==0x9abcdef0 && acc[2]==acc[3]) O[0]=acc;
  asm volatile("s_waitcnt vmcnt(0)":::"memory");
  unsigned long long t1=__builtin_amdgcn_s_memrealtime();
  if(threadIdx.x==0){ atomicMax(&ts[0], ~t0); atomicMax(&ts[1], t1); }
}
__global__ void mark_k(unsigned long long* ts){ if(threadIdx.x==0) ts[2]=__builtin_amdgcn_s_memrealtime(); }
__global__ void spin_k(unsigned long long* ts){
  unsigned long long t0=~ts[0], t1=USEMARK?ts[2]:ts[1];
  unsigned long long d=t1-t0; if(d>20000) d=20000;
  unsigned long long tgt=t0+50000+10*d;
  for(int i=0;i<2000000;++i){ if(__builtin_amdgcn_s_memrealtime()>=tgt) break; __builtin_amdgcn_s_sleep(2);}
}
extern "C" void kernel_launch(void* const* d_in, const int* in_sizes, int n_in, void* d_out, int out_size, void* d_ws, size_t ws_size, hipStream_t stream){
  unsigned long long* ts=(unsigned long long*)d_ws;
  hipMemsetAsync(ts,0,64,stream);
  stream_k<<<2048,256,0,stream>>>((const u32x4*)d_in[0],(const u32x4*)d_in[1],(const u32x4*)d_in[2],(u32x4*)d_out, out_size/8, ts);
  mark_k<<<1,64,0,stream>>>(ts);
  attn_base<<<1024,256,0,stream>>>((const unsigned short*)d_in[0],(const unsigned short*)d_in[1],(const unsigned short*)d_in[2],(unsigned short*)d_out);
  spin_k<<<1,64,0,stream>>>(ts);
}
```

```cpp
#define RQ 1
#define RK 0
#define RV 0
#define WO 0
#define USEMARK 0
#include <hip/hip_runtime.h>
#include <hip/hip_bf16.h>
typedef short bf16x8 __attribute__((ext_vector_type(8)));
typedef short s16x4 __attribute__((ext_vector_type(4)));
typedef float f32x16 __attribute__((ext_vector_type(16)));
typedef unsigned u32x4 __attribute__((ext_vector_type(4)));
typedef unsigned u32x2 __attribute__((ext_vector_type(2)));
typedef short v4i16_t __attribute__((ext_vector_type(4)));
#define MFMA(a,b,c) __builtin_amdgcn_mfma_f32_32x32x16_bf16(a,b,c,0,0,0)
__device__ __forceinline__ unsigned cvtpk(float lo, float hi){ unsigned r; asm("v_cvt_pk_bf16_f32 %0, %1, %2":"=v"(r):"v"(lo),"v"(hi)); return r;}
constexpr int S=512, D=256;
__global__ __launch_bounds__(256) void attn_base(const unsigned short* __restrict__ Q, const unsigned short* __restrict__ K, const unsigned short* __restrict__ V, unsigned short* __restrict__ O){
  __shared__ __attribute__((aligned(16))) char lds[32768];
  const int tid=threadIdx.x, lane=tid&63, wid=tid>>6, r=lane&31, h=lane>>5;
  const int bid=blockIdx.x; const int xcd=bid&7, j=bid>>3; const int head=xcd*32+(j>>2), qb=j&3;
  const size_t hb=(size_t)head*S*D;
  const unsigned short* q=Q+hb+(size_t)(qb*128+wid*32+r)*D+h*8;
  bf16x8 qf[16];
#pragma unroll
  for(int s=0;s<16;++s) qf[s]=*(const bf16x8*)(q+s*16);
  f32x16 o[8];
#pragma unroll
  for(int i=0;i<8;++i) o[i]=f32x16{};
  float m=-1e30f,l=0.f; const float c=1.4426950408889634f/16.f;
  char* kl=lds; char* vl=lds+16384;
  for(int t=0;t<16;++t){
    __syncthreads();
#pragma unroll
    for(int i=0;i<4;++i){
      int idx=tid+256*i; int row=idx>>5, ch=idx&31;
      u32x4 kv=*(const u32x4*)(K+hb+(size_t)(t*32+row)*D+ch*8);
      u32x4 vv=*(const u32x4*)(V+hb+(size_t)(t*32+row)*D+ch*8);
      *(u32x4*)(kl+row*512+((ch^(row&15))<<4))=kv;
      *(u32x4*)(vl+((row>>3)*8+(ch>>2))*512+(row&7)*64+(ch&3)*16)=vv;
    }
    __syncthreads();
    f32x16 s=f32x16{};
#pragma unroll
    for(int ks=0;ks<16;++ks){ bf16x8 a=*(const bf16x8*)(kl+r*512+(((2*ks+h)^(r&15))<<4)); s=MFMA(a,qf[ks],s);}
    float mx=s[0];
#pragma unroll
    for(int i=1;i<16;++i) mx=fmaxf(mx,s[i]);
    mx=fmaxf(mx,__shfl_xor(mx,32));
    float mn=fmaxf(m,mx); float al=exp2f((m-mn)*c); m=mn; float ps=0;
#pragma unroll
    for(int i=0;i<16;++i){ s[i]=__builtin_amdgcn_exp2f((s[i]-mn)*c); ps+=s[i];}
    l=l*al+ps;
#pragma unroll
    for(int db=0;db<8;++db)
#pragma unroll
      for(int i=0;i<16;++i) o[db][i]*=al;
    u32x4 p0={cvtpk(s[0],s[1]),cvtpk(s[2],s[3]),cvtpk(s[4],s[5]),cvtpk(s[6],s[7])};
    u32x4 p1={cvtpk(s[8],s[9]),cvtpk(s[10],s[11]),cvtpk(s[12],s[13]),cvtpk(s[14],s[15])};
    const int i16=lane&15, qq=i16>>2, pp=i16&3, blk=(lane>>4)&1;
#pragma unroll
    for(int db=0;db<8;++db){
#pragma unroll
      for(int s2=0;s2<2;++s2){
        typedef __attribute__((address_space(3))) v4i16_t* lp;
        int k0=16*s2+4*h+qq, k1=k0+8;
        char* a0=vl+((k0>>3)*8+db)*512+(k0&7)*64+blk*32+pp*8;
        char* a1=vl+((k1>>3)*8+db)*512+(k1&7)*64+blk*32+pp*8;
        v4i16_t lo=__builtin_amdgcn_ds_read_tr16_b64_v4i16((lp)a0);
        v4i16_t hi=__builtin_amdgcn_ds_read_tr16_b64_v4i16((lp)a1);
        bf16x8 a=__builtin_shufflevector(lo,hi,0,1,2,3,4,5,6,7);
        o[db]=MFMA(a,__builtin_bit_cast(bf16x8,s2?p1:p0),o[db]);
      }
    }
  }
  l+=__shfl_xor(l,32);
  float il=1.f/l;
  unsigned short* op=O+hb+(size_t)(qb*128+wid*32+r)*D+4*h;
#pragma unroll
  for(int db=0;db<8;++db)
#pragma unroll
    for(int g=0;g<4;++g){
      u32x2 w={cvtpk(o[db][4*g]*il,o[db][4*g+1]*il),cvtpk(o[db][4*g+2]*il,o[db][4*g+3]*il)};
      *(u32x2*)(op+db*32+8*g)=w;
    }
}

__global__ __launch_bounds__(256) void stream_k(const u32x4* __restrict__ Q, const u32x4* __restrict__ K, const u32x4* __restrict__ V, u32x4* __restrict__ O, int n, unsigned long long* ts){
  unsigned long long t0=__builtin_amdgcn_s_memrealtime();
  u32x4 acc={0,0,0,0};
  for(int i=blockIdx.x*256+threadIdx.x;i<n;i+=2048*256){
    u32x4 a={1,2,3,4};
    if(RQ) a^=Q[i];
    if(RK) a^=K[i];
    if(RV) a^=V[i];
    if(WO) O[i]=a; else acc^=a;
  }
  if(!WO && acc[0]==0x12345678 && acc[1]==0x9abcdef0 && acc[2]==acc[3]) O[0]=acc;
  asm volatile("s_waitcnt vmcnt(0)":::"memory");
  unsigned long long t1=__builtin_amdgcn_s_memrealtime();
  if(threadIdx.x==0){ ts[16+2*blockIdx.x]=t0; ts[17+2*blockIdx.x]=t1; }
}
__global__ void mark_k(unsigned long long* ts){ if(threadIdx.x==0) ts[2]=__builtin_amdgcn_s_memrealtime(); }
__global__ void spin_k(unsigned long long* ts){
  unsigned long long t0=~0ull, t1=0;
  for(int i=threadIdx.x;i<2048;i+=64){ unsigned long long a=ts[16+2*i], b=ts[17+2*i]; t0=a<t0?a:t0; t1=b>t1?b:t1; }
  for(int o=32;o;o>>=1){ unsigned long long a=__shfl_xor(t0,o), b=__shfl_xor(t1,o); t0=a<t0?a:t0; t1=b>t1?b:t1; }
  if(USEMARK) t1=ts[2];
  unsigned long long d=t1-t0; if(d>20000) d=20000;
  unsigned long long tgt=t0+50000+10*d;
  for(int i=0;i<2000000;++i){ if(__builtin_amdgcn_s_memrealtime()>=tgt) break; __builtin_amdgcn_s_sleep(2);}
}
extern "C" void kernel_launch(void* const* d_in, const int* in_sizes, int n_in, void* d_out, int out_size, void* d_ws, size_t ws_size, hipStream_t stream){
  unsigned long long* ts=(unsigned long long*)d_ws;
  stream_k<<<2048,256,0,stream>>>((const u32x4*)d_in[0],(const u32x4*)d_in[1],(const u32x4*)d_in[2],(u32x4*)d_out, out_size/8, ts);
  mark_k<<<1,64,0,stream>>>(ts);
  attn_base<<<1024,256,0,stream>>>((const unsigned short*)d_in[0],(const unsigned short*)d_in[1],(const unsigned short*)d_in[2],(unsigned short*)d_out);
  spin_k<<<1,64,0,stream>>>(ts);
}
```

```cpp
#include <hip/hip_runtime.h>
#include <hip/hip_bf16.h>
typedef short bf16x8 __attribute__((ext_vector_type(8)));
typedef short v4i16_t __attribute__((ext_vector_type(4)));
typedef float f32x4 __attribute__((ext_vector_type(4)));
typedef float f32x2 __attribute__((ext_vector_type(2)));
typedef __bf16 bf16x2 __attribute__((ext_vector_type(2)));
typedef unsigned u32x4 __attribute__((ext_vector_type(4)));
typedef unsigned u32x2 __attribute__((ext_vector_type(2)));

__device__ __forceinline__ unsigned pk2(float lo, float hi) { f32x2 v = {lo, hi}; return __builtin_bit_cast(unsigned, __builtin_convertvector(v, bf16x2)); }
__device__ __forceinline__ float xmax4(float m) { m = fmaxf(m, __shfl_xor(m, 16)); return fmaxf(m, __shfl_xor(m, 32)); }

__device__ __forceinline__ void slow_rows(const char* q, const char* k, const char* v, char* o, char* l0, char* l1, unsigned lane) {
  const unsigned m = lane & 15u, g = lane >> 4, qq = m >> 2, p = lane & 3u;
  const unsigned rd = (2u * g + (qq >> 1)) * 1056u + (qq & 1u) * 512u + p * 8u;
  const float c = 0.09016844005556021f;
  for (int qp = 0; qp < 2; ++qp) {
    bf16x8 qf[2][8];
    f32x4 acc[2][16];
    float mrun[2], l[2];
#pragma unroll
    for (int u = 0; u < 2; ++u) {
#pragma unroll
      for (int ks = 0; ks < 8; ++ks) qf[u][ks] = *(const bf16x8*)(q + (32 * qp + 16 * u + m) * 512 + ks * 64 + g * 16);
#pragma unroll
      for (int i = 0; i < 16; ++i) acc[u][i] = f32x4{0.f, 0.f, 0.f, 0.f};
      mrun[u] = -INFINITY; l[u] = 0.f;
    }
    for (int s = 0; s < 16; ++s) {
      typedef __attribute__((address_space(1))) const void* gp;
      typedef __attribute__((address_space(3))) void* dp;
#pragma unroll
      for (int i = 0; i < 8; ++i) {
        __builtin_amdgcn_global_load_lds((gp)(v + (32 * s) * 512 + i * 1024 + lane * 16), (dp)(l0 + i * 1056), 16, 0, 0);
        __builtin_amdgcn_global_load_lds((gp)(v + (32 * s + 16) * 512 + i * 1024 + lane * 16), (dp)(l1 + i * 1056), 16, 0, 0);
      }
      f32x4 sc[2][2];
#pragma unroll
      for (int h = 0; h < 2; ++h) {
        bf16x8 kf[8];
#pragma unroll
        for (int ks = 0; ks < 8; ++ks) kf[ks] = *(const bf16x8*)(k + (32 * s + 16 * h + m) * 512 + ks * 64 + g * 16);
        __builtin_amdgcn_sched_barrier(0);
#pragma unroll
        for (int u = 0; u < 2; ++u) {
          sc[u][h] = f32x4{0.f, 0.f, 0.f, 0.f};
#pragma unroll
          for (int ks = 0; ks < 8; ++ks) sc[u][h] = __builtin_amdgcn_mfma_f32_16x16x32_bf16(kf[ks], qf[u][ks], sc[u][h], 0, 0, 0);
        }
      }
      u32x4 pw[2];
      float al[2];
#pragma unroll
      for (int u = 0; u < 2; ++u) {
        f32x4 &s0 = sc[u][0], &s1 = sc[u][1];
        const float mx = fmaxf(fmaxf(fmaxf(s0[0], s0[1]), fmaxf(s0[2], s0[3])), fmaxf(fmaxf(s1[0], s1[1]), fmaxf(s1[2], s1[3])));
        const float mn = fmaxf(mrun[u], xmax4(mx));
        al[u] = (mrun[u] == mn) ? 1.f : __builtin_amdgcn_exp2f((mrun[u] - mn) * c);
        mrun[u] = mn;
        const float mref = (mn == -INFINITY) ? 0.f : mn;
        float ps = 0.f;
#pragma unroll
        for (int i = 0; i < 4; ++i) {
          s0[i] = __builtin_amdgcn_exp2f((s0[i] - mref) * c); s1[i] = __builtin_amdgcn_exp2f((s1[i] - mref) * c);
          ps += s0[i] + s1[i];
        }
        l[u] = l[u] * al[u] + ps;
        pw[u] = u32x4{pk2(s0[0], s0[1]), pk2(s0[2], s0[3]), pk2(s1[0], s1[1]), pk2(s1[2], s1[3])};
      }
      asm volatile("s_waitcnt vmcnt(0)" ::: "memory");
      typedef __attribute__((address_space(3))) v4i16_t* lp;
#pragma unroll
      for (int dt = 0; dt < 16; ++dt) {
        const v4i16_t lo = __builtin_amdgcn_ds_read_tr16_b64_v4i16((lp)(l0 + rd + dt * 32));
        const v4i16_t hi = __builtin_amdgcn_ds_read_tr16_b64_v4i16((lp)(l1 + rd + dt * 32));
        const bf16x8 vf = __builtin_shufflevector(lo, hi, 0, 1, 2, 3, 4, 5, 6, 7);
#pragma unroll
        for (int u = 0; u < 2; ++u)
          acc[u][dt] = __builtin_amdgcn_mfma_f32_16x16x32_bf16(vf, __builtin_bit_cast(bf16x8, pw[u]), acc[u][dt] * al[u], 0, 0, 0);
      }
      asm volatile("s_waitcnt lgkmcnt(0)" ::: "memory");
    }
#pragma unroll
    for (int u = 0; u < 2; ++u) {
      float ls = l[u];
      ls += __shfl_xor(ls, 16); ls += __shfl_xor(ls, 32);
      const float il = 1.f / ls;
#pragma unroll
      for (int dt = 0; dt < 16; ++dt) {
        const u32x2 w = {pk2(acc[u][dt][0] * il, acc[u][dt][1] * il), pk2(acc[u][dt][2] * il, acc[u][dt][3] * il)};
        *(u32x2*)(o + (32 * qp + 16 * u + m) * 512 + dt * 32 + g * 8) = w;
      }
    }
  }
}

__global__ __attribute__((aligned(4096))) __launch_bounds__(256, 1) void mha_tp(const char* __restrict__ Q, const char* __restrict__ K, const char* __restrict__ V, char* __restrict__ O) {
  __shared__ __attribute__((aligned(16))) char lds[163840];
  const unsigned w = __builtin_amdgcn_readfirstlane(threadIdx.x >> 6);
  const unsigned b = blockIdx.x;
  unsigned fl;
  asm volatile(
R"(
s_load_dwordx8 s[20:27], %[ka], 0x0
v_mbcnt_lo_u32_b32 v0, -1, 0
v_mbcnt_hi_u32_b32 v0, -1, v0
s_lshr_b32 s66, %[b], 3
s_getpc_b64 s[68:69]
.Lpc%=:
s_lshl_b32 s63, s66, 2
s_add_u32 s63, s63, %[w]
s_lshl_b32 s63, s63, 3
v_and_b32 v1, 7, v0
v_add_u32 v1, s63, v1
v_lshlrev_b32 v1, 7, v1
v_min_u32 v1, .Lend%=-.Lpc%=-4, v1
global_load_dword v2, v1, s[68:69]
v_lshlrev_b32 v241, 4, v0
s_and_b32 s67, %[b], 7
s_lshl_b32 s67, s67, 4
s_lshr_b32 s63, s66, 1
s_add_u32 s67, s67, s63
s_lshl_b32 s67, s67, 18
s_and_b32 s63, s66, 1
s_lshl_b32 s63, s63, 2
s_add_u32 s63, s63, %[w]
s_lshl_b32 s63, s63, 15
s_add_u32 s64, s63, s67
s_mul_i32 s62, %[w], 9216
s_mul_i32 s60, %[w], 4160
s_mul_i32 s61, %[w], 2112
s_mov_b32 s54, 32768
s_mov_b32 s50, 32768
s_mov_b32 s55, 0x20000
s_mov_b32 s51, 0x20000
s_mov_b32 s39, 0x20000
s_mov_b32 s43, 0x20000
s_lshl_b32 s63, %[w], 12
s_sub_u32 s38, 0x40000, s63
s_add_u32 s36, s63, s67
s_lshl_b32 s63, %[w], 11
s_sub_u32 s42, 0x40000, s63
s_add_u32 s40, s63, s67
s_mov_b32 s72, 4096
s_mov_b32 s73, 8192
s_mov_b32 s74, 12288
s_mov_b32 s75, 16384
s_mov_b32 s76, 20480
s_mov_b32 s77, 24576
s_mov_b32 s78, 28672
s_mov_b32 s79, 32768
s_mov_b32 s80, 49152
s_mov_b32 s81, 65536
s_mov_b32 s82, 81920
s_mov_b32 s83, 98304
s_mov_b32 s84, 114688
s_mov_b32 s85, 131072
s_mov_b32 s86, 147456
s_mov_b32 s87, 163840
s_mov_b32 s88, 180224
s_mov_b32 s89, 196608
s_mov_b32 s90, 212992
s_mov_b32 s91, 229376
s_mov_b32 s92, 245760
s_getreg_b32 s63, hwreg(HW_REG_XCC_ID, 0, 3)
s_lshl_b32 s63, s63, 2
s_lshr_b32 s63, 0x7070707, s63
s_waitcnt lgkmcnt(0)
s_add_u32 s52, s20, s64
s_addc_u32 s53, s21, 0
s_and_b32 s63, s63, 15
s_cbranch_scc0 .Lnd%=
s_add_u32 m0, s62, 0
s_nop 0
buffer_load_dwordx4 v241, s[52:55], 0 offen offset:0 nt lds
s_sleep 9
s_add_u32 m0, s62, 32
s_nop 0
buffer_load_dwordx4 v241, s[52:55], 0 offen offset:1024 nt lds
s_sleep 9
s_add_u32 m0, s62, 64
s_nop 0
buffer_load_dwordx4 v241, s[52:55], 0 offen offset:2048 nt lds
s_sleep 9
s_add_u32 m0, s62, 96
s_nop 0
buffer_load_dwordx4 v241, s[52:55], 0 offen offset:3072 nt lds
s_sleep 9
s_add_u32 m0, s62, 4224
s_nop 0
buffer_load_dwordx4 v241, s[52:55], s72 offen offset:0 nt lds
s_sleep 9
s_add_u32 m0, s62, 4256
s_nop 0
buffer_load_dwordx4 v241, s[52:55], s72 offen offset:1024 nt lds
s_sleep 9
s_add_u32 m0, s62, 4288
s_nop 0
buffer_load_dwordx4 v241, s[52:55], s72 offen offset:2048 nt lds
s_sleep 9
s_add_u32 m0, s62, 4320
s_nop 0
buffer_load_dwordx4 v241, s[52:55], s72 offen offset:3072 nt lds
s_sleep 9
s_branch .Lj%=
.Lnd%=:
s_add_u32 m0, s62, 0
s_nop 0
buffer_load_dwordx4 v241, s[52:55], 0 offen offset:0 nt lds
s_add_u32 m0, s62, 32
s_nop 0
buffer_load_dwordx4 v241, s[52:55], 0 offen offset:1024 nt lds
s_add_u32 m0, s62, 64
s_nop 0
buffer_load_dwordx4 v241, s[52:55], 0 offen offset:2048 nt lds
s_add_u32 m0, s62, 96
s_nop 0
buffer_load_dwordx4 v241, s[52:55], 0 offen offset:3072 nt lds
s_add_u32 m0, s62, 4224
s_nop 0
buffer_load_dwordx4 v241, s[52:55], s72 offen offset:0 nt lds
s_add_u32 m0, s62, 4256
s_nop 0
buffer_load_dwordx4 v241, s[52:55], s72 offen offset:1024 nt lds
s_add_u32 m0, s62, 4288
s_nop 0
buffer_load_dwordx4 v241, s[52:55], s72 offen offset:2048 nt lds
s_add_u32 m0, s62, 4320
s_nop 0
buffer_load_dwordx4 v241, s[52:55], s72 offen offset:3072 nt lds
.Lj%=:
s_add_u32 m0, s62, 36864
s_nop 0
buffer_load_dwordx4 v241, s[52:55], s73 offen offset:0 nt lds
s_add_u32 m0, s62, 36896
s_nop 0
buffer_load_dwordx4 v241, s[52:55], s73 offen offset:1024 nt lds
s_add_u32 s36, s36, s22
s_addc_u32 s37, s23, 0
s_add_u32 s40, s40, s24
s_addc_u32 s41, s25, 0
s_add_u32 s48, s26, s64
s_addc_u32 s49, s27, 0
s_add_u32 s63, s64, 33529856
s_add_u32 s34, s24, s63
s_addc_u32 s35, s25, 0
s_add_u32 s44, s52, 0x2000000
s_addc_u32 s45, s53, 0
s_mov_b32 s46, 32768
s_mov_b32 s47, 0x20000
s_add_u32 s20, s52, 0x2000000
s_addc_u32 s21, s53, 0
s_mov_b32 s22, 32768
s_mov_b32 s23, 0x20000
s_add_u32 s24, s52, 0x2000000
s_addc_u32 s25, s53, 0
s_mov_b32 s26, 32768
s_mov_b32 s27, 0x20000
s_add_u32 s28, s52, 0x2000000
s_addc_u32 s29, s53, 0
s_mov_b32 s30, 32768
s_mov_b32 s31, 0x20000
s_mov_b32 s71, 0x3f803f80
v_mov_b32 v254, v241
s_mov_b32 s56, 0x3db8aa3b
s_mov_b32 s57, 0xbdb8aa3b
s_mov_b32 s58, 0x5d800000
s_mov_b32 s59, 0
v_and_b32 v3, 15, v0
v_lshrrev_b32 v4, 4, v0
v_and_b32 v5, 1, v4
v_lshlrev_b32 v5, 8, v5
v_lshrrev_b32 v6, 1, v4
v_lshl_add_u32 v5, v6, 7, v5
v_mul_u32_u24 v7, 1040, v3
v_add_u32 v7, v7, v5
v_add_u32 v236, 73728, v7
v_add_u32 v237, 141312, v7
v_lshrrev_b32 v7, 1, v3
v_mul_u32_u24 v7, 1056, v7
v_and_b32 v8, 1, v3
v_lshl_add_u32 v7, v8, 9, v7
v_add_u32 v7, v7, v5
v_add_u32 v240, s62, v7
v_and_b32 v7, 1, v4
v_lshrrev_b32 v8, 2, v3
v_lshl_add_u32 v7, v7, 2, v8
v_mul_u32_u24 v7, 1056, v7
v_lshl_add_u32 v7, v6, 8, v7
v_and_b32 v8, 1, v8
v_lshlrev_b32 v8, 4, v8
v_sub_u32 v7, v7, v8
v_bfe_u32 v8, v0, 1, 1
v_lshl_add_u32 v7, v8, 4, v7
v_and_b32 v8, 3, v0
v_lshl_add_u32 v7, v8, 3, v7
v_add_u32 v238, 73728, v7
v_add_u32 v239, 141312, v7
v_and_b32 v10, 7, v0
v_lshlrev_b32 v10, 4, v10
v_lshrrev_b32 v11, 3, v0
v_and_b32 v7, 1, v11
v_lshl_add_u32 v7, v4, 4, v7
v_lshl_add_u32 v242, v7, 9, v10
v_bfe_u32 v7, v0, 1, 1
v_lshl_add_u32 v7, v4, 1, v7
v_lshlrev_b32 v7, 9, v7
v_bfe_u32 v9, v0, 3, 1
v_lshl_add_u32 v7, v9, 6, v7
v_and_b32 v9, 1, v0
v_lshl_add_u32 v7, v9, 4, v7
v_bfe_u32 v9, v0, 2, 1
v_lshl_add_u32 v243, v9, 5, v7
v_lshrrev_b32 v9, 2, v3
v_lshl_add_u32 v7, v8, 3, v9
v_mul_u32_u24 v7, 264, v7
v_lshl_add_u32 v7, v4, 3, v7
v_add_u32 v245, s62, v7
v_mul_u32_u24 v7, 264, v3
v_lshl_add_u32 v7, v4, 4, v7
v_add_u32 v244, s62, v7
.p2align 6
s_mov_b64 vcc, -1
s_cmp_eq_u32 %[w], 1
s_cbranch_scc1 .Lpad1_%=
s_cmp_eq_u32 %[w], 2
s_cbranch_scc1 .Lpad2_%=
s_cmp_eq_u32 %[w], 3
s_cbranch_scc1 .Lpad3_%=
s_branch .Lpad0_%=
.Lret0_%=:
s_add_u32 m0, s62, 36928
s_nop 0
buffer_load_dwordx4 v241, s[52:55], s73 offen offset:2048 nt lds
s_add_u32 m0, s62, 36960
s_nop 0
buffer_load_dwordx4 v241, s[52:55], s73 offen offset:3072 nt lds
s_add_u32 m0, s62, 41088
s_nop 0
buffer_load_dwordx4 v241, s[52:55], s74 offen offset:0 nt lds
s_add_u32 m0, s62, 41120
s_nop 0
buffer_load_dwordx4 v241, s[52:55], s74 offen offset:1024 nt lds
s_add_u32 m0, s62, 41152
s_nop 0
buffer_load_dwordx4 v241, s[52:55], s74 offen offset:2048 nt lds
s_add_u32 m0, s62, 41184
s_nop 0
buffer_load_dwordx4 v241, s[52:55], s74 offen offset:3072 nt lds
s_cmp_eq_u32 %[w], 1
s_cbranch_scc1 .Lpad5_%=
s_cmp_eq_u32 %[w], 2
s_cbranch_scc1 .Lpad6_%=
s_cmp_eq_u32 %[w], 3
s_cbranch_scc1 .Lpad7_%=
s_branch .Lpad4_%=
.Lret1_%=:
s_add_u32 m0, s60, 73728
s_nop 0
buffer_load_dwordx4 v241, s[36:39], 0 offen offset:0 nt lds
s_add_u32 m0, s60, 73744
s_nop 0
buffer_load_dwordx4 v241, s[36:39], 0 offen offset:1024 nt lds
s_add_u32 m0, s60, 73760
s_nop 0
buffer_load_dwordx4 v241, s[36:39], 0 offen offset:2048 nt lds
s_add_u32 m0, s60, 73776
s_nop 0
buffer_load_dwordx4 v241, s[36:39], 0 offen offset:3072 nt lds
s_cmp_eq_u32 %[w], 1
s_cbranch_scc1 .Lpad9_%=
s_cmp_eq_u32 %[w], 2
s_cbranch_scc1 .Lpad10_%=
s_cmp_eq_u32 %[w], 3
s_cbranch_scc1 .Lpad11_%=
s_branch .Lpad8_%=
.Lret2_%=:
s_waitcnt vmcnt(12)
ds_read_b128 a[0:3], v240 offset:0
ds_read_b128 a[4:7], v240 offset:16
ds_read_b128 a[8:11], v240 offset:32
ds_read_b128 a[12:15], v240 offset:48
ds_read_b128 a[16:19], v240 offset:64
ds_read_b128 a[20:23], v240 offset:80
ds_read_b128 a[24:27], v240 offset:96
ds_read_b128 a[28:31], v240 offset:112
s_add_u32 m0, s60, 90624
s_nop 0
buffer_load_dwordx4 v241, s[36:39], s75 offen offset:0 nt lds
s_add_u32 m0, s60, 90640
s_nop 0
buffer_load_dwordx4 v241, s[36:39], s75 offen offset:1024 nt lds
s_add_u32 m0, s60, 90656
s_nop 0
buffer_load_dwordx4 v241, s[36:39], s75 offen offset:2048 nt lds
s_add_u32 m0, s60, 90672
s_nop 0
buffer_load_dwordx4 v241, s[36:39], s75 offen offset:3072 nt lds
s_cmp_eq_u32 %[w], 1
s_cbranch_scc1 .Lpad13_%=
s_cmp_eq_u32 %[w], 2
s_cbranch_scc1 .Lret3_%=
s_cmp_eq_u32 %[w], 3
s_cbranch_scc1 .Lret3_%=
s_branch .Lpad12_%=
.Lret3_%=:
s_mov_b64 vcc, 0
s_add_u32 m0, s60, 107520
s_nop 0
buffer_load_dwordx4 v241, s[36:39], s79 offen offset:0 nt lds
s_add_u32 m0, s60, 107536
s_nop 0
buffer_load_dwordx4 v241, s[36:39], s79 offen offset:1024 nt lds
s_add_u32 m0, s60, 107552
s_nop 0
buffer_load_dwordx4 v241, s[36:39], s79 offen offset:2048 nt lds
s_add_u32 m0, s60, 107568
s_nop 0
buffer_load_dwordx4 v241, s[36:39], s79 offen offset:3072 nt lds
s_waitcnt vmcnt(12)
ds_read_b128 a[32:35], v240 offset:36864
ds_read_b128 a[36:39], v240 offset:36880
ds_read_b128 a[40:43], v240 offset:36896
ds_read_b128 a[44:47], v240 offset:36912
ds_read_b128 a[48:51], v240 offset:36928
ds_read_b128 a[52:55], v240 offset:36944
ds_read_b128 a[56:59], v240 offset:36960
ds_read_b128 a[60:63], v240 offset:36976
s_waitcnt lgkmcnt(8)
s_waitcnt vmcnt(8)
s_barrier
ds_read_b128 a[128:131], v236 offset:0
ds_read_b128 a[132:135], v236 offset:16
ds_read_b128 a[136:139], v236 offset:32
ds_read_b128 a[140:143], v236 offset:48
ds_read_b128 a[144:147], v236 offset:64
ds_read_b128 a[148:151], v236 offset:80
ds_read_b128 v[204:207], v236 offset:96
ds_read_b128 v[208:211], v236 offset:112
s_waitcnt lgkmcnt(6)
v_mfma_f32_16x16x32_bf16 v[164:167], a[128:131], a[0:3], 0
ds_read_b128 v[212:215], v236 offset:512
v_mfma_f32_16x16x32_bf16 v[164:167], a[132:135], a[4:7], v[164:167]
s_waitcnt lgkmcnt(5)
v_mfma_f32_16x16x32_bf16 v[164:167], a[136:139], a[8:11], v[164:167]
s_add_u32 m0, s60, 124416
ds_read_b128 v[216:219], v236 offset:528
v_mfma_f32_16x16x32_bf16 v[164:167], a[140:143], a[12:15], v[164:167]
s_waitcnt lgkmcnt(4)
buffer_load_dwordx4 v241, s[36:39], s80 offen offset:0 nt lds
v_mfma_f32_16x16x32_bf16 v[164:167], a[144:147], a[16:19], v[164:167]
ds_read_b128 v[220:223], v236 offset:544
v_mfma_f32_16x16x32_bf16 v[164:167], a[148:151], a[20:23], v[164:167]
s_waitcnt lgkmcnt(3)
v_mfma_f32_16x16x32_bf16 v[164:167], v[204:207], a[24:27], v[164:167]
ds_read_b128 v[224:227], v236 offset:560
v_mfma_f32_16x16x32_bf16 v[164:167], v[208:211], a[28:31], v[164:167]
v_mfma_f32_16x16x32_bf16 v[168:171], v[208:211], a[60:63], 0
ds_read_b128 v[228:231], v236 offset:576
v_mfma_f32_16x16x32_bf16 v[168:171], v[204:207], a[56:59], v[168:171]
v_mfma_f32_16x16x32_bf16 v[168:171], a[148:151], a[52:55], v[168:171]
s_add_u32 m0, s60, 124432
ds_read_b128 v[232:235], v236 offset:592
v_mfma_f32_16x16x32_bf16 v[168:171], a[144:147], a[48:51], v[168:171]
buffer_load_dwordx4 v241, s[36:39], s80 offen offset:1024 nt lds
v_max3_f32 v246, v164, v165, v166
v_max_f32 v246, v246, v167
v_mfma_f32_16x16x32_bf16 v[168:171], a[140:143], a[44:47], v[168:171]
ds_read_b128 v[156:159], v236 offset:608
v_mov_b32 v250, v246
v_mfma_f32_16x16x32_bf16 v[168:171], a[136:139], a[40:43], v[168:171]
s_nop 0
v_permlane32_swap_b32 v246, v250
v_mfma_f32_16x16x32_bf16 v[168:171], a[132:135], a[36:39], v[168:171]
ds_read_b128 v[160:163], v236 offset:624
v_max_f32 v246, v246, v250
v_mov_b32 v250, v246
v_mfma_f32_16x16x32_bf16 v[168:171], a[128:131], a[32:35], v[168:171]
s_waitcnt vmcnt(6) lgkmcnt(6)
s_barrier
v_mfma_f32_16x16x32_bf16 v[172:175], v[212:215], a[0:3], 0
ds_read_b128 a[220:223], v236 offset:16896
v_permlane16_swap_b32 v246, v250
v_mfma_f32_16x16x32_bf16 v[172:175], v[216:219], a[4:7], v[172:175]
s_waitcnt lgkmcnt(5)
v_max_f32 v246, v246, v250
v_mul_f32 v246, s57, v246
v_add_f32 v246, 0xc0400000, v246
v_mfma_f32_16x16x32_bf16 v[172:175], v[220:223], a[8:11], v[172:175]
s_add_u32 m0, s60, 124448
ds_read_b128 v[0:3], v236 offset:16912
v_max3_f32 v247, v168, v169, v170
v_max_f32 v247, v247, v171
v_mov_b32 v251, v247
v_mfma_f32_16x16x32_bf16 v[172:175], v[224:227], a[12:15], v[172:175]
s_waitcnt lgkmcnt(4)
buffer_load_dwordx4 v241, s[36:39], s80 offen offset:2048 nt lds
v_permlane32_swap_b32 v247, v251
v_fma_f32 v164, v164, s56, v246
v_mfma_f32_16x16x32_bf16 v[172:175], v[228:231], a[16:19], v[172:175]
ds_read_b128 v[4:7], v236 offset:16928
v_max_f32 v247, v247, v251
v_mov_b32 v251, v247
v_mfma_f32_16x16x32_bf16 v[172:175], v[232:235], a[20:23], v[172:175]
s_waitcnt lgkmcnt(3)
v_permlane16_swap_b32 v247, v251
v_fma_f32 v165, v165, s56, v246
v_mfma_f32_16x16x32_bf16 v[172:175], v[156:159], a[24:27], v[172:175]
)"
R"(
ds_read_b128 v[8:11], v236 offset:16944
v_max_f32 v247, v247, v251
v_mfma_f32_16x16x32_bf16 v[172:175], v[160:163], a[28:31], v[172:175]
v_mul_f32 v247, s57, v247
v_add_f32 v247, 0xc0400000, v247
v_mfma_f32_16x16x32_bf16 v[176:179], v[160:163], a[60:63], 0
ds_read_b128 a[224:227], v236 offset:16960
v_fma_f32 v166, v166, s56, v246
v_mfma_f32_16x16x32_bf16 v[176:179], v[156:159], a[56:59], v[176:179]
v_exp_f32 v164, v164
v_mfma_f32_16x16x32_bf16 v[176:179], v[232:235], a[52:55], v[176:179]
s_add_u32 m0, s60, 124464
ds_read_b128 v[12:15], v236 offset:16976
v_fma_f32 v167, v167, s56, v246
v_mfma_f32_16x16x32_bf16 v[176:179], v[228:231], a[48:51], v[176:179]
buffer_load_dwordx4 v241, s[36:39], s80 offen offset:3072 nt lds
v_mfma_f32_16x16x32_bf16 v[176:179], v[224:227], a[44:47], v[176:179]
ds_read_b128 v[16:19], v236 offset:16992
v_fma_f32 v168, v168, s56, v247
v_mfma_f32_16x16x32_bf16 v[176:179], v[220:223], a[40:43], v[176:179]
v_exp_f32 v165, v165
v_mfma_f32_16x16x32_bf16 v[176:179], v[216:219], a[36:39], v[176:179]
ds_read_b128 v[20:23], v236 offset:17008
v_cvt_pk_bf16_f32 v164, v164, v165
v_mfma_f32_16x16x32_bf16 v[176:179], v[212:215], a[32:35], v[176:179]
s_waitcnt lgkmcnt(6)
v_exp_f32 v166, v166
v_mfma_f32_16x16x32_bf16 v[180:183], a[220:223], a[0:3], 0
ds_read_b128 v[24:27], v236 offset:17408
v_accvgpr_write_b32 a156, v164
v_mfma_f32_16x16x32_bf16 v[180:183], v[0:3], a[4:7], v[180:183]
s_waitcnt lgkmcnt(5)
v_exp_f32 v167, v167
v_mfma_f32_16x16x32_bf16 v[180:183], v[4:7], a[8:11], v[180:183]
s_add_u32 m0, s62, 0
ds_read_b128 a[228:231], v236 offset:17424
v_cvt_pk_bf16_f32 v166, v166, v167
v_mfma_f32_16x16x32_bf16 v[180:183], v[8:11], a[12:15], v[180:183]
s_waitcnt lgkmcnt(4)
buffer_load_dwordx4 v241, s[52:55], s75 offen offset:0 nt lds
v_mfma_f32_16x16x32_bf16 v[180:183], a[224:227], a[16:19], v[180:183]
ds_read_b128 v[28:31], v236 offset:17440
v_accvgpr_write_b32 a157, v166
v_mfma_f32_16x16x32_bf16 v[180:183], v[12:15], a[20:23], v[180:183]
s_waitcnt lgkmcnt(3)
v_exp_f32 v168, v168
v_mfma_f32_16x16x32_bf16 v[180:183], v[16:19], a[24:27], v[180:183]
ds_read_b128 v[32:35], v236 offset:17456
v_fma_f32 v169, v169, s56, v247
v_mfma_f32_16x16x32_bf16 v[180:183], v[20:23], a[28:31], v[180:183]
v_exp_f32 v169, v169
v_mfma_f32_16x16x32_bf16 v[184:187], v[20:23], a[60:63], 0
ds_read_b128 v[36:39], v236 offset:17472
v_fma_f32 v170, v170, s56, v247
v_mfma_f32_16x16x32_bf16 v[184:187], v[16:19], a[56:59], v[184:187]
v_exp_f32 v170, v170
v_mfma_f32_16x16x32_bf16 v[184:187], v[12:15], a[52:55], v[184:187]
s_add_u32 m0, s62, 32
ds_read_b128 v[40:43], v236 offset:17488
v_fma_f32 v171, v171, s56, v247
v_mfma_f32_16x16x32_bf16 v[184:187], a[224:227], a[48:51], v[184:187]
buffer_load_dwordx4 v241, s[52:55], s75 offen offset:1024 nt lds
v_mfma_f32_16x16x32_bf16 v[184:187], v[8:11], a[44:47], v[184:187]
ds_read_b128 a[232:235], v236 offset:17504
v_cvt_pk_bf16_f32 v168, v168, v169
v_mfma_f32_16x16x32_bf16 v[184:187], v[4:7], a[40:43], v[184:187]
v_exp_f32 v171, v171
v_mfma_f32_16x16x32_bf16 v[184:187], v[0:3], a[36:39], v[184:187]
ds_read_b128 v[44:47], v236 offset:17520
v_accvgpr_write_b32 a160, v168
v_mfma_f32_16x16x32_bf16 v[184:187], a[220:223], a[32:35], v[184:187]
s_waitcnt vmcnt(6) lgkmcnt(6)
s_barrier
v_mfma_f32_16x16x32_bf16 v[188:191], v[24:27], a[0:3], 0
ds_read_b128 v[48:51], v236 offset:33792
v_cvt_pk_bf16_f32 v170, v170, v171
v_mfma_f32_16x16x32_bf16 v[188:191], a[228:231], a[4:7], v[188:191]
s_waitcnt lgkmcnt(5)
v_accvgpr_write_b32 a161, v170
v_fma_f32 v172, v172, s56, v246
v_mfma_f32_16x16x32_bf16 v[188:191], v[28:31], a[8:11], v[188:191]
s_add_u32 m0, s62, 64
ds_read_b128 v[52:55], v236 offset:33808
v_fma_f32 v173, v173, s56, v246
v_mfma_f32_16x16x32_bf16 v[188:191], v[32:35], a[12:15], v[188:191]
s_waitcnt lgkmcnt(4)
buffer_load_dwordx4 v241, s[52:55], s75 offen offset:2048 nt lds
v_mfma_f32_16x16x32_bf16 v[188:191], v[36:39], a[16:19], v[188:191]
ds_read_b128 v[56:59], v236 offset:33824
v_fma_f32 v174, v174, s56, v246
v_mfma_f32_16x16x32_bf16 v[188:191], v[40:43], a[20:23], v[188:191]
s_waitcnt lgkmcnt(3)
v_exp_f32 v172, v172
v_mfma_f32_16x16x32_bf16 v[188:191], a[232:235], a[24:27], v[188:191]
ds_read_b128 a[236:239], v236 offset:33840
v_fma_f32 v175, v175, s56, v246
v_mfma_f32_16x16x32_bf16 v[188:191], v[44:47], a[28:31], v[188:191]
v_exp_f32 v173, v173
v_mfma_f32_16x16x32_bf16 v[192:195], v[44:47], a[60:63], 0
ds_read_b128 v[60:63], v236 offset:33856
v_cvt_pk_bf16_f32 v172, v172, v173
v_mfma_f32_16x16x32_bf16 v[192:195], a[232:235], a[56:59], v[192:195]
v_exp_f32 v174, v174
v_mfma_f32_16x16x32_bf16 v[192:195], v[40:43], a[52:55], v[192:195]
s_add_u32 m0, s62, 96
ds_read_b128 v[64:67], v236 offset:33872
v_accvgpr_write_b32 a158, v172
v_mfma_f32_16x16x32_bf16 v[192:195], v[36:39], a[48:51], v[192:195]
buffer_load_dwordx4 v241, s[52:55], s75 offen offset:3072 nt lds
v_mfma_f32_16x16x32_bf16 v[192:195], v[32:35], a[44:47], v[192:195]
ds_read_b128 v[68:71], v236 offset:33888
v_fma_f32 v176, v176, s56, v247
v_mfma_f32_16x16x32_bf16 v[192:195], v[28:31], a[40:43], v[192:195]
v_exp_f32 v175, v175
v_mfma_f32_16x16x32_bf16 v[192:195], a[228:231], a[36:39], v[192:195]
ds_read_b128 v[72:75], v236 offset:33904
v_cvt_pk_bf16_f32 v174, v174, v175
v_mfma_f32_16x16x32_bf16 v[192:195], v[24:27], a[32:35], v[192:195]
s_waitcnt lgkmcnt(6)
v_exp_f32 v176, v176
v_mfma_f32_16x16x32_bf16 v[196:199], v[48:51], a[0:3], 0
ds_read_b128 a[240:243], v236 offset:34304
v_accvgpr_write_b32 a159, v174
v_mfma_f32_16x16x32_bf16 v[196:199], v[52:55], a[4:7], v[196:199]
s_waitcnt lgkmcnt(5)
v_fma_f32 v177, v177, s56, v247
v_fma_f32 v178, v178, s56, v247
v_mfma_f32_16x16x32_bf16 v[196:199], v[56:59], a[8:11], v[196:199]
s_add_u32 m0, s62, 4224
ds_read_b128 v[76:79], v236 offset:34320
v_fma_f32 v179, v179, s56, v247
v_mfma_f32_16x16x32_bf16 v[196:199], a[236:239], a[12:15], v[196:199]
s_waitcnt lgkmcnt(4)
buffer_load_dwordx4 v241, s[52:55], s76 offen offset:0 nt lds
v_mfma_f32_16x16x32_bf16 v[196:199], v[60:63], a[16:19], v[196:199]
ds_read_b128 v[80:83], v236 offset:34336
v_fma_f32 v180, v180, s56, v246
v_mfma_f32_16x16x32_bf16 v[196:199], v[64:67], a[20:23], v[196:199]
s_waitcnt lgkmcnt(3)
v_exp_f32 v177, v177
v_mfma_f32_16x16x32_bf16 v[196:199], v[68:71], a[24:27], v[196:199]
ds_read_b128 v[84:87], v236 offset:34352
v_cvt_pk_bf16_f32 v176, v176, v177
v_mfma_f32_16x16x32_bf16 v[196:199], v[72:75], a[28:31], v[196:199]
v_exp_f32 v178, v178
v_mfma_f32_16x16x32_bf16 v[200:203], v[72:75], a[60:63], 0
ds_read_b128 v[88:91], v236 offset:34368
v_accvgpr_write_b32 a162, v176
v_mfma_f32_16x16x32_bf16 v[200:203], v[68:71], a[56:59], v[200:203]
v_exp_f32 v179, v179
v_mfma_f32_16x16x32_bf16 v[200:203], v[64:67], a[52:55], v[200:203]
s_add_u32 m0, s62, 4256
ds_read_b128 a[244:247], v236 offset:34384
v_cvt_pk_bf16_f32 v178, v178, v179
v_mfma_f32_16x16x32_bf16 v[200:203], v[60:63], a[48:51], v[200:203]
buffer_load_dwordx4 v241, s[52:55], s76 offen offset:1024 nt lds
v_mfma_f32_16x16x32_bf16 v[200:203], a[236:239], a[44:47], v[200:203]
ds_read_b128 v[92:95], v236 offset:34400
v_accvgpr_write_b32 a163, v178
v_mfma_f32_16x16x32_bf16 v[200:203], v[56:59], a[40:43], v[200:203]
v_exp_f32 v180, v180
v_mfma_f32_16x16x32_bf16 v[200:203], v[52:55], a[36:39], v[200:203]
ds_read_b128 v[96:99], v236 offset:34416
v_fma_f32 v181, v181, s56, v246
v_mfma_f32_16x16x32_bf16 v[200:203], v[48:51], a[32:35], v[200:203]
s_waitcnt vmcnt(6) lgkmcnt(6)
s_barrier
v_mfma_f32_16x16x32_bf16 v[164:167], a[240:243], a[0:3], 0
ds_read_b128 v[100:103], v236 offset:50688
v_fma_f32 v182, v182, s56, v246
v_mfma_f32_16x16x32_bf16 v[164:167], v[76:79], a[4:7], v[164:167]
s_waitcnt lgkmcnt(5)
v_exp_f32 v181, v181
v_mfma_f32_16x16x32_bf16 v[164:167], v[80:83], a[8:11], v[164:167]
s_add_u32 m0, s62, 4288
ds_read_b128 v[104:107], v236 offset:50704
v_fma_f32 v183, v183, s56, v246
v_mfma_f32_16x16x32_bf16 v[164:167], v[84:87], a[12:15], v[164:167]
s_waitcnt lgkmcnt(4)
buffer_load_dwordx4 v241, s[52:55], s76 offen offset:2048 nt lds
v_mfma_f32_16x16x32_bf16 v[164:167], v[88:91], a[16:19], v[164:167]
ds_read_b128 a[248:251], v236 offset:50720
v_cvt_pk_bf16_f32 v180, v180, v181
v_mfma_f32_16x16x32_bf16 v[164:167], a[244:247], a[20:23], v[164:167]
s_waitcnt lgkmcnt(3)
v_exp_f32 v182, v182
v_mfma_f32_16x16x32_bf16 v[164:167], v[92:95], a[24:27], v[164:167]
ds_read_b128 v[108:111], v236 offset:50736
v_accvgpr_write_b32 a172, v180
v_mfma_f32_16x16x32_bf16 v[164:167], v[96:99], a[28:31], v[164:167]
v_exp_f32 v183, v183
v_mfma_f32_16x16x32_bf16 v[168:171], v[96:99], a[60:63], 0
ds_read_b128 v[112:115], v236 offset:50752
v_cvt_pk_bf16_f32 v182, v182, v183
v_mfma_f32_16x16x32_bf16 v[168:171], v[92:95], a[56:59], v[168:171]
v_accvgpr_write_b32 a173, v182
v_fma_f32 v184, v184, s56, v247
v_mfma_f32_16x16x32_bf16 v[168:171], a[244:247], a[52:55], v[168:171]
s_add_u32 m0, s62, 4320
ds_read_b128 v[116:119], v236 offset:50768
v_fma_f32 v185, v185, s56, v247
v_mfma_f32_16x16x32_bf16 v[168:171], v[88:91], a[48:51], v[168:171]
buffer_load_dwordx4 v241, s[52:55], s76 offen offset:3072 nt lds
v_mfma_f32_16x16x32_bf16 v[168:171], v[84:87], a[44:47], v[168:171]
ds_read_b128 v[120:123], v236 offset:50784
v_fma_f32 v186, v186, s56, v247
v_mfma_f32_16x16x32_bf16 v[168:171], v[80:83], a[40:43], v[168:171]
v_exp_f32 v184, v184
v_mfma_f32_16x16x32_bf16 v[168:171], v[76:79], a[36:39], v[168:171]
ds_read_b128 a[252:255], v236 offset:50800
v_fma_f32 v187, v187, s56, v247
v_mfma_f32_16x16x32_bf16 v[168:171], a[240:243], a[32:35], v[168:171]
s_waitcnt lgkmcnt(6)
v_exp_f32 v185, v185
v_mfma_f32_16x16x32_bf16 v[172:175], v[100:103], a[0:3], 0
ds_read_b128 v[124:127], v236 offset:51200
v_cvt_pk_bf16_f32 v184, v184, v185
v_mfma_f32_16x16x32_bf16 v[172:175], v[104:107], a[4:7], v[172:175]
s_waitcnt lgkmcnt(5)
v_exp_f32 v186, v186
v_mfma_f32_16x16x32_bf16 v[172:175], a[248:251], a[8:11], v[172:175]
s_add_u32 m0, s62, 36864
ds_read_b128 v[128:131], v236 offset:51216
v_accvgpr_write_b32 a176, v184
v_mfma_f32_16x16x32_bf16 v[172:175], v[108:111], a[12:15], v[172:175]
s_waitcnt lgkmcnt(4)
buffer_load_dwordx4 v241, s[52:55], s77 offen offset:0 nt lds
v_mfma_f32_16x16x32_bf16 v[172:175], v[112:115], a[16:19], v[172:175]
ds_read_b128 v[132:135], v236 offset:51232
v_fma_f32 v188, v188, s56, v246
v_mfma_f32_16x16x32_bf16 v[172:175], v[116:119], a[20:23], v[172:175]
.Lpad0_%=:
s_cbranch_vccnz .Lret0_%=
s_waitcnt lgkmcnt(3)
v_exp_f32 v187, v187
v_mfma_f32_16x16x32_bf16 v[172:175], v[120:123], a[24:27], v[172:175]
ds_read_b128 v[136:139], v236 offset:51248
v_cvt_pk_bf16_f32 v186, v186, v187
v_mfma_f32_16x16x32_bf16 v[172:175], a[252:255], a[28:31], v[172:175]
v_accvgpr_write_b32 a177, v186
v_fma_f32 v189, v189, s56, v246
v_mfma_f32_16x16x32_bf16 v[176:179], a[252:255], a[60:63], 0
ds_read_b128 v[140:143], v236 offset:51264
v_fma_f32 v190, v190, s56, v246
v_mfma_f32_16x16x32_bf16 v[176:179], v[120:123], a[56:59], v[176:179]
v_exp_f32 v188, v188
v_mfma_f32_16x16x32_bf16 v[176:179], v[116:119], a[52:55], v[176:179]
s_add_u32 m0, s62, 36896
ds_read_b128 v[144:147], v236 offset:51280
v_fma_f32 v191, v191, s56, v246
v_mfma_f32_16x16x32_bf16 v[176:179], v[112:115], a[48:51], v[176:179]
buffer_load_dwordx4 v241, s[52:55], s77 offen offset:1024 nt lds
v_mfma_f32_16x16x32_bf16 v[176:179], v[108:111], a[44:47], v[176:179]
ds_read_b128 v[148:151], v236 offset:51296
v_fma_f32 v192, v192, s56, v247
v_mfma_f32_16x16x32_bf16 v[176:179], a[248:251], a[40:43], v[176:179]
v_exp_f32 v189, v189
v_mfma_f32_16x16x32_bf16 v[176:179], v[104:107], a[36:39], v[176:179]
ds_read_b128 v[152:155], v236 offset:51312
v_fma_f32 v193, v193, s56, v247
v_mfma_f32_16x16x32_bf16 v[176:179], v[100:103], a[32:35], v[176:179]
s_waitcnt lgkmcnt(6)
v_exp_f32 v190, v190
v_mfma_f32_16x16x32_bf16 v[180:183], v[124:127], a[0:3], 0
v_exp_f32 v191, v191
v_mfma_f32_16x16x32_bf16 v[180:183], v[128:131], a[4:7], v[180:183]
s_waitcnt lgkmcnt(4)
v_exp_f32 v192, v192
v_mfma_f32_16x16x32_bf16 v[180:183], v[132:135], a[8:11], v[180:183]
s_add_u32 m0, s62, 36928
v_exp_f32 v193, v193
)"
R"(
v_mfma_f32_16x16x32_bf16 v[180:183], v[136:139], a[12:15], v[180:183]
s_waitcnt lgkmcnt(2)
buffer_load_dwordx4 v241, s[52:55], s77 offen offset:2048 nt lds
v_mfma_f32_16x16x32_bf16 v[180:183], v[140:143], a[16:19], v[180:183]
v_fma_f32 v194, v194, s56, v247
v_fma_f32 v195, v195, s56, v247
v_mfma_f32_16x16x32_bf16 v[180:183], v[144:147], a[20:23], v[180:183]
s_waitcnt lgkmcnt(0)
v_exp_f32 v194, v194
v_mfma_f32_16x16x32_bf16 v[180:183], v[148:151], a[24:27], v[180:183]
v_exp_f32 v195, v195
v_mfma_f32_16x16x32_bf16 v[180:183], v[152:155], a[28:31], v[180:183]
v_fma_f32 v196, v196, s56, v246
v_fma_f32 v197, v197, s56, v246
v_mfma_f32_16x16x32_bf16 v[184:187], v[152:155], a[60:63], 0
v_exp_f32 v196, v196
v_mfma_f32_16x16x32_bf16 v[184:187], v[148:151], a[56:59], v[184:187]
v_exp_f32 v197, v197
v_mfma_f32_16x16x32_bf16 v[184:187], v[144:147], a[52:55], v[184:187]
s_add_u32 m0, s62, 36960
v_fma_f32 v198, v198, s56, v246
v_fma_f32 v199, v199, s56, v246
v_mfma_f32_16x16x32_bf16 v[184:187], v[140:143], a[48:51], v[184:187]
buffer_load_dwordx4 v241, s[52:55], s77 offen offset:3072 nt lds
v_mfma_f32_16x16x32_bf16 v[184:187], v[136:139], a[44:47], v[184:187]
v_exp_f32 v198, v198
v_mfma_f32_16x16x32_bf16 v[184:187], v[132:135], a[40:43], v[184:187]
v_exp_f32 v199, v199
v_mfma_f32_16x16x32_bf16 v[184:187], v[128:131], a[36:39], v[184:187]
v_fma_f32 v200, v200, s56, v247
v_fma_f32 v201, v201, s56, v247
v_mfma_f32_16x16x32_bf16 v[184:187], v[124:127], a[32:35], v[184:187]
v_exp_f32 v200, v200
v_exp_f32 v201, v201
v_fma_f32 v202, v202, s56, v247
v_fma_f32 v203, v203, s56, v247
v_exp_f32 v202, v202
v_exp_f32 v203, v203
v_fma_f32 v164, v164, s56, v246
v_fma_f32 v165, v165, s56, v246
v_exp_f32 v164, v164
v_exp_f32 v165, v165
v_fma_f32 v166, v166, s56, v246
v_fma_f32 v167, v167, s56, v246
v_exp_f32 v166, v166
v_exp_f32 v167, v167
v_fma_f32 v168, v168, s56, v247
v_fma_f32 v169, v169, s56, v247
v_exp_f32 v168, v168
v_exp_f32 v169, v169
v_fma_f32 v170, v170, s56, v247
v_fma_f32 v171, v171, s56, v247
v_exp_f32 v170, v170
v_exp_f32 v171, v171
v_fma_f32 v172, v172, s56, v246
v_fma_f32 v173, v173, s56, v246
v_exp_f32 v172, v172
v_exp_f32 v173, v173
v_fma_f32 v174, v174, s56, v246
v_fma_f32 v175, v175, s56, v246
v_exp_f32 v174, v174
v_exp_f32 v175, v175
v_fma_f32 v176, v176, s56, v247
v_fma_f32 v177, v177, s56, v247
v_exp_f32 v176, v176
v_exp_f32 v177, v177
v_fma_f32 v178, v178, s56, v247
v_fma_f32 v179, v179, s56, v247
v_exp_f32 v178, v178
v_exp_f32 v179, v179
v_fma_f32 v180, v180, s56, v246
v_fma_f32 v181, v181, s56, v246
v_exp_f32 v180, v180
v_exp_f32 v181, v181
v_fma_f32 v182, v182, s56, v246
v_fma_f32 v183, v183, s56, v246
v_exp_f32 v182, v182
v_exp_f32 v183, v183
v_fma_f32 v184, v184, s56, v247
v_fma_f32 v185, v185, s56, v247
v_exp_f32 v184, v184
v_exp_f32 v185, v185
v_fma_f32 v186, v186, s56, v247
v_fma_f32 v187, v187, s56, v247
v_exp_f32 v186, v186
v_exp_f32 v187, v187
v_cvt_pk_bf16_f32 v188, v188, v189
v_accvgpr_write_b32 a174, v188
v_cvt_pk_bf16_f32 v190, v190, v191
v_accvgpr_write_b32 a175, v190
v_cvt_pk_bf16_f32 v192, v192, v193
v_accvgpr_write_b32 a178, v192
v_cvt_pk_bf16_f32 v194, v194, v195
v_accvgpr_write_b32 a179, v194
v_cvt_pk_bf16_f32 v196, v196, v197
v_accvgpr_write_b32 a188, v196
v_cvt_pk_bf16_f32 v198, v198, v199
v_accvgpr_write_b32 a189, v198
v_cvt_pk_bf16_f32 v200, v200, v201
v_accvgpr_write_b32 a192, v200
v_cvt_pk_bf16_f32 v202, v202, v203
v_accvgpr_write_b32 a193, v202
v_cvt_pk_bf16_f32 v164, v164, v165
v_accvgpr_write_b32 a190, v164
v_cvt_pk_bf16_f32 v166, v166, v167
v_accvgpr_write_b32 a191, v166
v_cvt_pk_bf16_f32 v168, v168, v169
v_accvgpr_write_b32 a194, v168
v_cvt_pk_bf16_f32 v170, v170, v171
v_accvgpr_write_b32 a195, v170
v_cvt_pk_bf16_f32 v172, v172, v173
v_accvgpr_write_b32 a204, v172
v_cvt_pk_bf16_f32 v174, v174, v175
v_accvgpr_write_b32 a205, v174
v_cvt_pk_bf16_f32 v176, v176, v177
v_accvgpr_write_b32 a208, v176
v_cvt_pk_bf16_f32 v178, v178, v179
v_accvgpr_write_b32 a209, v178
v_cvt_pk_bf16_f32 v180, v180, v181
v_accvgpr_write_b32 a206, v180
v_cvt_pk_bf16_f32 v182, v182, v183
v_accvgpr_write_b32 a207, v182
v_cvt_pk_bf16_f32 v184, v184, v185
v_accvgpr_write_b32 a210, v184
v_cvt_pk_bf16_f32 v186, v186, v187
v_accvgpr_write_b32 a211, v186
s_add_u32 m0, s62, 41088
s_nop 0
buffer_load_dwordx4 v241, s[52:55], s78 offen offset:0 nt lds
s_add_u32 m0, s62, 41120
s_nop 0
buffer_load_dwordx4 v241, s[52:55], s78 offen offset:1024 nt lds
s_add_u32 m0, s62, 41152
s_nop 0
buffer_load_dwordx4 v241, s[52:55], s78 offen offset:2048 nt lds
s_add_u32 m0, s62, 41184
s_nop 0
buffer_load_dwordx4 v241, s[52:55], s78 offen offset:3072 nt lds
s_waitcnt vmcnt(8)
ds_read_b128 a[64:67], v240 offset:0
ds_read_b128 a[68:71], v240 offset:16
ds_read_b128 a[72:75], v240 offset:32
ds_read_b128 a[76:79], v240 offset:48
ds_read_b128 a[80:83], v240 offset:64
ds_read_b128 a[84:87], v240 offset:80
ds_read_b128 a[88:91], v240 offset:96
ds_read_b128 a[92:95], v240 offset:112
s_waitcnt lgkmcnt(6)
v_mfma_f32_16x16x32_bf16 v[188:191], a[128:131], a[64:67], 0
s_add_u32 m0, s60, 73728
v_mfma_f32_16x16x32_bf16 v[188:191], a[132:135], a[68:71], v[188:191]
s_waitcnt lgkmcnt(4)
buffer_load_dwordx4 v241, s[36:39], s81 offen offset:0 nt lds
s_nop 5
v_mfma_f32_16x16x32_bf16 v[188:191], a[136:139], a[72:75], v[188:191]
s_add_u32 m0, s60, 73744
v_mfma_f32_16x16x32_bf16 v[188:191], a[140:143], a[76:79], v[188:191]
s_waitcnt lgkmcnt(2)
buffer_load_dwordx4 v241, s[36:39], s81 offen offset:1024 nt lds
s_nop 5
v_mfma_f32_16x16x32_bf16 v[188:191], a[144:147], a[80:83], v[188:191]
s_add_u32 m0, s60, 73760
v_mfma_f32_16x16x32_bf16 v[188:191], a[148:151], a[84:87], v[188:191]
s_waitcnt lgkmcnt(0)
buffer_load_dwordx4 v241, s[36:39], s81 offen offset:2048 nt lds
s_nop 5
v_mfma_f32_16x16x32_bf16 v[188:191], v[204:207], a[88:91], v[188:191]
s_add_u32 m0, s60, 73776
v_mfma_f32_16x16x32_bf16 v[188:191], v[208:211], a[92:95], v[188:191]
buffer_load_dwordx4 v241, s[36:39], s81 offen offset:3072 nt lds
s_nop 5
v_mfma_f32_16x16x32_bf16 v[192:195], v[212:215], a[64:67], 0
v_mfma_f32_16x16x32_bf16 v[192:195], v[216:219], a[68:71], v[192:195]
s_nop 5
v_mfma_f32_16x16x32_bf16 v[192:195], v[220:223], a[72:75], v[192:195]
v_mfma_f32_16x16x32_bf16 v[192:195], v[224:227], a[76:79], v[192:195]
s_nop 5
v_mfma_f32_16x16x32_bf16 v[192:195], v[228:231], a[80:83], v[192:195]
v_mfma_f32_16x16x32_bf16 v[192:195], v[232:235], a[84:87], v[192:195]
s_nop 5
v_mfma_f32_16x16x32_bf16 v[192:195], v[156:159], a[88:91], v[192:195]
v_max3_f32 v248, v188, v189, v190
v_max_f32 v248, v248, v191
v_mfma_f32_16x16x32_bf16 v[192:195], v[160:163], a[92:95], v[192:195]
v_mov_b32 v252, v248
s_nop 5
v_mfma_f32_16x16x32_bf16 v[196:199], a[220:223], a[64:67], 0
v_permlane32_swap_b32 v248, v252
v_mfma_f32_16x16x32_bf16 v[196:199], v[0:3], a[68:71], v[196:199]
v_max_f32 v248, v248, v252
v_mov_b32 v252, v248
s_nop 5
v_mfma_f32_16x16x32_bf16 v[196:199], v[4:7], a[72:75], v[196:199]
v_permlane16_swap_b32 v248, v252
v_mfma_f32_16x16x32_bf16 v[196:199], v[8:11], a[76:79], v[196:199]
v_max_f32 v248, v248, v252
v_mul_f32 v248, s57, v248
s_nop 5
v_mfma_f32_16x16x32_bf16 v[196:199], a[224:227], a[80:83], v[196:199]
v_add_f32 v248, 0xc0400000, v248
v_mfma_f32_16x16x32_bf16 v[196:199], v[12:15], a[84:87], v[196:199]
v_fma_f32 v188, v188, s56, v248
v_fma_f32 v189, v189, s56, v248
s_nop 5
v_mfma_f32_16x16x32_bf16 v[196:199], v[16:19], a[88:91], v[196:199]
v_exp_f32 v188, v188
v_mfma_f32_16x16x32_bf16 v[196:199], v[20:23], a[92:95], v[196:199]
v_exp_f32 v189, v189
s_nop 5
v_mfma_f32_16x16x32_bf16 v[200:203], v[24:27], a[64:67], 0
v_fma_f32 v190, v190, s56, v248
v_fma_f32 v191, v191, s56, v248
v_mfma_f32_16x16x32_bf16 v[200:203], a[228:231], a[68:71], v[200:203]
s_add_u32 m0, s60, 90624
v_exp_f32 v190, v190
s_nop 5
v_mfma_f32_16x16x32_bf16 v[200:203], v[28:31], a[72:75], v[200:203]
buffer_load_dwordx4 v241, s[36:39], s82 offen offset:0 nt lds
v_mfma_f32_16x16x32_bf16 v[200:203], v[32:35], a[76:79], v[200:203]
v_exp_f32 v191, v191
s_nop 5
v_mfma_f32_16x16x32_bf16 v[200:203], v[36:39], a[80:83], v[200:203]
v_cvt_pk_bf16_f32 v188, v188, v189
v_accvgpr_write_b32 a164, v188
v_mfma_f32_16x16x32_bf16 v[200:203], v[40:43], a[84:87], v[200:203]
v_cvt_pk_bf16_f32 v190, v190, v191
v_accvgpr_write_b32 a165, v190
s_nop 5
v_mfma_f32_16x16x32_bf16 v[200:203], a[232:235], a[88:91], v[200:203]
v_fma_f32 v192, v192, s56, v248
v_fma_f32 v193, v193, s56, v248
v_mfma_f32_16x16x32_bf16 v[200:203], v[44:47], a[92:95], v[200:203]
v_exp_f32 v192, v192
s_nop 5
v_mfma_f32_16x16x32_bf16 v[164:167], v[48:51], a[64:67], 0
v_exp_f32 v193, v193
v_mfma_f32_16x16x32_bf16 v[164:167], v[52:55], a[68:71], v[164:167]
v_fma_f32 v194, v194, s56, v248
v_fma_f32 v195, v195, s56, v248
s_nop 5
v_mfma_f32_16x16x32_bf16 v[164:167], v[56:59], a[72:75], v[164:167]
v_exp_f32 v194, v194
v_mfma_f32_16x16x32_bf16 v[164:167], a[236:239], a[76:79], v[164:167]
v_exp_f32 v195, v195
s_nop 5
v_mfma_f32_16x16x32_bf16 v[164:167], v[60:63], a[80:83], v[164:167]
v_cvt_pk_bf16_f32 v192, v192, v193
v_accvgpr_write_b32 a166, v192
v_mfma_f32_16x16x32_bf16 v[164:167], v[64:67], a[84:87], v[164:167]
v_cvt_pk_bf16_f32 v194, v194, v195
v_accvgpr_write_b32 a167, v194
s_nop 5
v_mfma_f32_16x16x32_bf16 v[164:167], v[68:71], a[88:91], v[164:167]
v_fma_f32 v196, v196, s56, v248
v_fma_f32 v197, v197, s56, v248
v_mfma_f32_16x16x32_bf16 v[164:167], v[72:75], a[92:95], v[164:167]
v_exp_f32 v196, v196
s_nop 5
v_mfma_f32_16x16x32_bf16 v[168:171], a[240:243], a[64:67], 0
v_exp_f32 v197, v197
v_mfma_f32_16x16x32_bf16 v[168:171], v[76:79], a[68:71], v[168:171]
s_add_u32 m0, s60, 90640
v_fma_f32 v198, v198, s56, v248
v_fma_f32 v199, v199, s56, v248
s_nop 5
v_mfma_f32_16x16x32_bf16 v[168:171], v[80:83], a[72:75], v[168:171]
buffer_load_dwordx4 v241, s[36:39], s82 offen offset:1024 nt lds
v_mfma_f32_16x16x32_bf16 v[168:171], v[84:87], a[76:79], v[168:171]
v_exp_f32 v198, v198
s_nop 5
v_mfma_f32_16x16x32_bf16 v[168:171], v[88:91], a[80:83], v[168:171]
v_exp_f32 v199, v199
v_mfma_f32_16x16x32_bf16 v[168:171], a[244:247], a[84:87], v[168:171]
v_cvt_pk_bf16_f32 v196, v196, v197
v_accvgpr_write_b32 a180, v196
s_nop 5
v_mfma_f32_16x16x32_bf16 v[168:171], v[92:95], a[88:91], v[168:171]
v_cvt_pk_bf16_f32 v198, v198, v199
v_accvgpr_write_b32 a181, v198
v_mfma_f32_16x16x32_bf16 v[168:171], v[96:99], a[92:95], v[168:171]
v_fma_f32 v200, v200, s56, v248
v_fma_f32 v201, v201, s56, v248
s_nop 5
v_mfma_f32_16x16x32_bf16 v[172:175], v[100:103], a[64:67], 0
v_exp_f32 v200, v200
v_mfma_f32_16x16x32_bf16 v[172:175], v[104:107], a[68:71], v[172:175]
v_exp_f32 v201, v201
s_nop 5
v_mfma_f32_16x16x32_bf16 v[172:175], a[248:251], a[72:75], v[172:175]
v_fma_f32 v202, v202, s56, v248
v_fma_f32 v203, v203, s56, v248
v_mfma_f32_16x16x32_bf16 v[172:175], v[108:111], a[76:79], v[172:175]
v_exp_f32 v202, v202
s_nop 5
v_mfma_f32_16x16x32_bf16 v[172:175], v[112:115], a[80:83], v[172:175]
v_exp_f32 v203, v203
v_mfma_f32_16x16x32_bf16 v[172:175], v[116:119], a[84:87], v[172:175]
v_cvt_pk_bf16_f32 v200, v200, v201
v_accvgpr_write_b32 a182, v200
s_nop 5
v_mfma_f32_16x16x32_bf16 v[172:175], v[120:123], a[88:91], v[172:175]
s_waitcnt vmcnt(6)
ds_read_b128 a[96:99], v240 offset:36864
v_cvt_pk_bf16_f32 v202, v202, v203
v_mfma_f32_16x16x32_bf16 v[172:175], a[252:255], a[92:95], v[172:175]
ds_read_b128 a[100:103], v240 offset:36880
v_accvgpr_write_b32 a183, v202
v_mfma_f32_16x16x32_bf16 v[176:179], v[124:127], a[64:67], 0
ds_read_b128 a[104:107], v240 offset:36896
v_fma_f32 v164, v164, s56, v248
v_mfma_f32_16x16x32_bf16 v[176:179], v[128:131], a[68:71], v[176:179]
ds_read_b128 a[108:111], v240 offset:36912
v_fma_f32 v165, v165, s56, v248
v_mfma_f32_16x16x32_bf16 v[176:179], v[132:135], a[72:75], v[176:179]
ds_read_b128 a[112:115], v240 offset:36928
v_fma_f32 v166, v166, s56, v248
v_mfma_f32_16x16x32_bf16 v[176:179], v[136:139], a[76:79], v[176:179]
ds_read_b128 a[116:119], v240 offset:36944
v_fma_f32 v167, v167, s56, v248
v_mfma_f32_16x16x32_bf16 v[176:179], v[140:143], a[80:83], v[176:179]
ds_read_b128 a[120:123], v240 offset:36960
)"
R"(
v_fma_f32 v168, v168, s56, v248
v_mfma_f32_16x16x32_bf16 v[176:179], v[144:147], a[84:87], v[176:179]
ds_read_b128 a[124:127], v240 offset:36976
v_fma_f32 v169, v169, s56, v248
v_mfma_f32_16x16x32_bf16 v[176:179], v[148:151], a[88:91], v[176:179]
v_exp_f32 v164, v164
v_mfma_f32_16x16x32_bf16 v[176:179], v[152:155], a[92:95], v[176:179]
v_exp_f32 v165, v165
s_add_u32 m0, s60, 90656
s_nop 0
buffer_load_dwordx4 v241, s[36:39], s82 offen offset:2048 nt lds
s_add_u32 m0, s60, 90672
s_nop 0
buffer_load_dwordx4 v241, s[36:39], s82 offen offset:3072 nt lds
s_waitcnt lgkmcnt(6)
v_mfma_f32_16x16x32_bf16 v[180:183], a[128:131], a[96:99], 0
v_exp_f32 v166, v166
v_mfma_f32_16x16x32_bf16 v[180:183], a[132:135], a[100:103], v[180:183]
s_waitcnt lgkmcnt(4)
v_exp_f32 v167, v167
v_mfma_f32_16x16x32_bf16 v[180:183], a[136:139], a[104:107], v[180:183]
v_exp_f32 v168, v168
v_mfma_f32_16x16x32_bf16 v[180:183], a[140:143], a[108:111], v[180:183]
s_waitcnt lgkmcnt(2)
v_exp_f32 v169, v169
v_mfma_f32_16x16x32_bf16 v[180:183], a[144:147], a[112:115], v[180:183]
v_cvt_pk_bf16_f32 v164, v164, v165
v_accvgpr_write_b32 a196, v164
v_mfma_f32_16x16x32_bf16 v[180:183], a[148:151], a[116:119], v[180:183]
s_waitcnt lgkmcnt(0)
v_cvt_pk_bf16_f32 v166, v166, v167
v_accvgpr_write_b32 a197, v166
v_mfma_f32_16x16x32_bf16 v[180:183], v[204:207], a[120:123], v[180:183]
v_fma_f32 v170, v170, s56, v248
v_fma_f32 v171, v171, s56, v248
v_mfma_f32_16x16x32_bf16 v[180:183], v[208:211], a[124:127], v[180:183]
v_exp_f32 v170, v170
v_mfma_f32_16x16x32_bf16 v[184:187], v[212:215], a[96:99], 0
v_exp_f32 v171, v171
v_mfma_f32_16x16x32_bf16 v[184:187], v[216:219], a[100:103], v[184:187]
v_cvt_pk_bf16_f32 v168, v168, v169
v_accvgpr_write_b32 a198, v168
v_mfma_f32_16x16x32_bf16 v[184:187], v[220:223], a[104:107], v[184:187]
v_cvt_pk_bf16_f32 v170, v170, v171
v_max3_f32 v249, v180, v181, v182
v_mfma_f32_16x16x32_bf16 v[184:187], v[224:227], a[108:111], v[184:187]
v_max_f32 v249, v249, v183
v_mov_b32 v253, v249
v_mfma_f32_16x16x32_bf16 v[184:187], v[228:231], a[112:115], v[184:187]
s_nop 0
v_permlane32_swap_b32 v249, v253
v_accvgpr_write_b32 a199, v170
v_mfma_f32_16x16x32_bf16 v[184:187], v[232:235], a[116:119], v[184:187]
v_max_f32 v249, v249, v253
v_mov_b32 v253, v249
v_mfma_f32_16x16x32_bf16 v[184:187], v[156:159], a[120:123], v[184:187]
s_nop 0
v_permlane16_swap_b32 v249, v253
v_fma_f32 v172, v172, s56, v248
v_mfma_f32_16x16x32_bf16 v[184:187], v[160:163], a[124:127], v[184:187]
v_max_f32 v249, v249, v253
v_mul_f32 v249, s57, v249
v_mfma_f32_16x16x32_bf16 v[188:191], a[220:223], a[96:99], 0
v_add_f32 v249, 0xc0400000, v249
v_fma_f32 v173, v173, s56, v248
v_mfma_f32_16x16x32_bf16 v[188:191], v[0:3], a[100:103], v[188:191]
v_exp_f32 v172, v172
v_mfma_f32_16x16x32_bf16 v[188:191], v[4:7], a[104:107], v[188:191]
v_exp_f32 v173, v173
v_mfma_f32_16x16x32_bf16 v[188:191], v[8:11], a[108:111], v[188:191]
v_fma_f32 v180, v180, s56, v249
v_fma_f32 v181, v181, s56, v249
v_mfma_f32_16x16x32_bf16 v[188:191], a[224:227], a[112:115], v[188:191]
v_exp_f32 v180, v180
v_mfma_f32_16x16x32_bf16 v[188:191], v[12:15], a[116:119], v[188:191]
v_exp_f32 v181, v181
v_mfma_f32_16x16x32_bf16 v[188:191], v[16:19], a[120:123], v[188:191]
v_fma_f32 v182, v182, s56, v249
v_fma_f32 v183, v183, s56, v249
v_mfma_f32_16x16x32_bf16 v[188:191], v[20:23], a[124:127], v[188:191]
v_exp_f32 v182, v182
v_mfma_f32_16x16x32_bf16 v[192:195], v[24:27], a[96:99], 0
v_exp_f32 v183, v183
v_mfma_f32_16x16x32_bf16 v[192:195], a[228:231], a[100:103], v[192:195]
v_fma_f32 v184, v184, s56, v249
v_fma_f32 v185, v185, s56, v249
v_mfma_f32_16x16x32_bf16 v[192:195], v[28:31], a[104:107], v[192:195]
v_exp_f32 v184, v184
v_mfma_f32_16x16x32_bf16 v[192:195], v[32:35], a[108:111], v[192:195]
v_exp_f32 v185, v185
v_mfma_f32_16x16x32_bf16 v[192:195], v[36:39], a[112:115], v[192:195]
v_fma_f32 v186, v186, s56, v249
v_fma_f32 v187, v187, s56, v249
v_mfma_f32_16x16x32_bf16 v[192:195], v[40:43], a[116:119], v[192:195]
v_exp_f32 v186, v186
v_mfma_f32_16x16x32_bf16 v[192:195], a[232:235], a[120:123], v[192:195]
v_exp_f32 v187, v187
v_mfma_f32_16x16x32_bf16 v[192:195], v[44:47], a[124:127], v[192:195]
v_fma_f32 v188, v188, s56, v249
v_fma_f32 v189, v189, s56, v249
v_mfma_f32_16x16x32_bf16 v[196:199], v[48:51], a[96:99], 0
v_exp_f32 v188, v188
v_mfma_f32_16x16x32_bf16 v[196:199], v[52:55], a[100:103], v[196:199]
v_exp_f32 v189, v189
v_mfma_f32_16x16x32_bf16 v[196:199], v[56:59], a[104:107], v[196:199]
v_fma_f32 v190, v190, s56, v249
v_fma_f32 v191, v191, s56, v249
v_mfma_f32_16x16x32_bf16 v[196:199], a[236:239], a[108:111], v[196:199]
v_exp_f32 v190, v190
v_mfma_f32_16x16x32_bf16 v[196:199], v[60:63], a[112:115], v[196:199]
v_exp_f32 v191, v191
v_mfma_f32_16x16x32_bf16 v[196:199], v[64:67], a[116:119], v[196:199]
v_fma_f32 v192, v192, s56, v249
v_fma_f32 v193, v193, s56, v249
v_mfma_f32_16x16x32_bf16 v[196:199], v[68:71], a[120:123], v[196:199]
v_exp_f32 v192, v192
v_mfma_f32_16x16x32_bf16 v[196:199], v[72:75], a[124:127], v[196:199]
v_exp_f32 v193, v193
v_mfma_f32_16x16x32_bf16 v[200:203], a[240:243], a[96:99], 0
v_fma_f32 v194, v194, s56, v249
v_fma_f32 v195, v195, s56, v249
v_mfma_f32_16x16x32_bf16 v[200:203], v[76:79], a[100:103], v[200:203]
v_exp_f32 v194, v194
v_mfma_f32_16x16x32_bf16 v[200:203], v[80:83], a[104:107], v[200:203]
v_exp_f32 v195, v195
v_mfma_f32_16x16x32_bf16 v[200:203], v[84:87], a[108:111], v[200:203]
v_fma_f32 v196, v196, s56, v249
v_fma_f32 v197, v197, s56, v249
v_mfma_f32_16x16x32_bf16 v[200:203], v[88:91], a[112:115], v[200:203]
v_exp_f32 v196, v196
v_mfma_f32_16x16x32_bf16 v[200:203], a[244:247], a[116:119], v[200:203]
s_add_u32 m0, s60, 107520
v_exp_f32 v197, v197
v_mfma_f32_16x16x32_bf16 v[200:203], v[92:95], a[120:123], v[200:203]
s_waitcnt vmcnt(4)
v_fma_f32 v198, v198, s56, v249
v_fma_f32 v199, v199, s56, v249
v_mfma_f32_16x16x32_bf16 v[200:203], v[96:99], a[124:127], v[200:203]
s_barrier
v_mfma_f32_16x16x32_bf16 v[164:167], v[100:103], a[96:99], 0
buffer_load_dwordx4 v241, s[36:39], s83 offen offset:0  lds
v_mfma_f32_16x16x32_bf16 v[164:167], v[104:107], a[100:103], v[164:167]
ds_read_b128 a[136:139], v236 offset:0
v_fma_f32 v174, v174, s56, v248
v_mfma_f32_16x16x32_bf16 v[164:167], a[248:251], a[104:107], v[164:167]
v_exp_f32 v198, v198
v_mfma_f32_16x16x32_bf16 v[164:167], v[108:111], a[108:111], v[164:167]
v_exp_f32 v199, v199
v_mfma_f32_16x16x32_bf16 v[164:167], v[112:115], a[112:115], v[164:167]
v_exp_f32 v174, v174
v_mfma_f32_16x16x32_bf16 v[164:167], v[116:119], a[116:119], v[164:167]
ds_read_b128 a[140:143], v236 offset:16
v_fma_f32 v200, v200, s56, v249
v_mfma_f32_16x16x32_bf16 v[164:167], v[120:123], a[120:123], v[164:167]
v_exp_f32 v200, v200
v_mfma_f32_16x16x32_bf16 v[164:167], a[252:255], a[124:127], v[164:167]
s_add_u32 m0, s60, 107536
v_fma_f32 v201, v201, s56, v249
v_fma_f32 v202, v202, s56, v249
v_mfma_f32_16x16x32_bf16 v[168:171], v[124:127], a[96:99], 0
buffer_load_dwordx4 v241, s[36:39], s83 offen offset:1024  lds
v_mfma_f32_16x16x32_bf16 v[168:171], v[128:131], a[100:103], v[168:171]
ds_read_b128 a[144:147], v236 offset:32
v_fma_f32 v203, v203, s56, v249
v_mfma_f32_16x16x32_bf16 v[168:171], v[132:135], a[104:107], v[168:171]
v_exp_f32 v201, v201
v_mfma_f32_16x16x32_bf16 v[168:171], v[136:139], a[108:111], v[168:171]
v_exp_f32 v202, v202
v_mfma_f32_16x16x32_bf16 v[168:171], v[140:143], a[112:115], v[168:171]
v_exp_f32 v203, v203
v_mfma_f32_16x16x32_bf16 v[168:171], v[144:147], a[116:119], v[168:171]
ds_read_b128 a[148:151], v236 offset:48
v_fma_f32 v175, v175, s56, v248
v_mfma_f32_16x16x32_bf16 v[168:171], v[148:151], a[120:123], v[168:171]
s_add_u32 m0, s60, 107552
v_exp_f32 v175, v175
v_mfma_f32_16x16x32_bf16 v[168:171], v[152:155], a[124:127], v[168:171]
s_waitcnt lgkmcnt(2)
v_fma_f32 v164, v164, s56, v249
v_fma_f32 v165, v165, s56, v249
v_mfma_f32_16x16x32_bf16 v[204:207], a[136:139], a[0:3], 0
buffer_load_dwordx4 v241, s[36:39], s83 offen offset:2048  lds
v_mfma_f32_16x16x32_bf16 v[204:207], a[140:143], a[4:7], v[204:207]
ds_read_b128 a[128:131], v236 offset:64
v_fma_f32 v166, v166, s56, v249
v_mfma_f32_16x16x32_bf16 v[208:211], a[140:143], a[36:39], 0
v_exp_f32 v164, v164
v_mfma_f32_16x16x32_bf16 v[208:211], a[136:139], a[32:35], v[208:211]
v_exp_f32 v165, v165
v_mfma_f32_16x16x32_bf16 v[212:215], a[136:139], a[64:67], 0
v_exp_f32 v166, v166
v_mfma_f32_16x16x32_bf16 v[212:215], a[140:143], a[68:71], v[212:215]
ds_read_b128 a[132:135], v236 offset:80
v_fma_f32 v167, v167, s56, v249
v_mfma_f32_16x16x32_bf16 v[216:219], a[140:143], a[100:103], 0
s_add_u32 m0, s60, 107568
v_exp_f32 v167, v167
v_mfma_f32_16x16x32_bf16 v[216:219], a[136:139], a[96:99], v[216:219]
s_waitcnt lgkmcnt(2)
v_fma_f32 v176, v176, s56, v248
v_fma_f32 v177, v177, s56, v248
v_mfma_f32_16x16x32_bf16 v[216:219], a[144:147], a[104:107], v[216:219]
buffer_load_dwordx4 v241, s[36:39], s83 offen offset:3072  lds
v_mfma_f32_16x16x32_bf16 v[216:219], a[148:151], a[108:111], v[216:219]
ds_read_b128 a[136:139], v236 offset:96
v_fma_f32 v178, v178, s56, v248
v_mfma_f32_16x16x32_bf16 v[212:215], a[148:151], a[76:79], v[212:215]
v_exp_f32 v176, v176
v_mfma_f32_16x16x32_bf16 v[212:215], a[144:147], a[72:75], v[212:215]
v_exp_f32 v177, v177
v_mfma_f32_16x16x32_bf16 v[208:211], a[144:147], a[40:43], v[208:211]
v_exp_f32 v178, v178
v_mfma_f32_16x16x32_bf16 v[208:211], a[148:151], a[44:47], v[208:211]
ds_read_b128 a[140:143], v236 offset:112
v_fma_f32 v179, v179, s56, v248
v_mfma_f32_16x16x32_bf16 v[204:207], a[148:151], a[12:15], v[204:207]
s_add_u32 m0, s60, 124416
v_exp_f32 v179, v179
v_mfma_f32_16x16x32_bf16 v[204:207], a[144:147], a[8:11], v[204:207]
s_waitcnt lgkmcnt(2)
v_fma_f32 v168, v168, s56, v249
v_fma_f32 v169, v169, s56, v249
v_mfma_f32_16x16x32_bf16 v[204:207], a[128:131], a[16:19], v[204:207]
buffer_load_dwordx4 v241, s[36:39], s84 offen offset:0  lds
v_mfma_f32_16x16x32_bf16 v[204:207], a[132:135], a[20:23], v[204:207]
ds_read_b128 a[144:147], v236 offset:512
v_fma_f32 v170, v170, s56, v249
v_mfma_f32_16x16x32_bf16 v[208:211], a[132:135], a[52:55], v[208:211]
v_exp_f32 v168, v168
v_mfma_f32_16x16x32_bf16 v[208:211], a[128:131], a[48:51], v[208:211]
v_exp_f32 v169, v169
v_mfma_f32_16x16x32_bf16 v[212:215], a[128:131], a[80:83], v[212:215]
v_exp_f32 v170, v170
v_mfma_f32_16x16x32_bf16 v[212:215], a[132:135], a[84:87], v[212:215]
ds_read_b128 a[148:151], v236 offset:528
v_fma_f32 v171, v171, s56, v249
v_mfma_f32_16x16x32_bf16 v[216:219], a[132:135], a[116:119], v[216:219]
s_add_u32 m0, s60, 124432
v_exp_f32 v171, v171
v_mfma_f32_16x16x32_bf16 v[216:219], a[128:131], a[112:115], v[216:219]
.Lpad1_%=:
s_cbranch_vccnz .Lret0_%=
s_waitcnt lgkmcnt(2)
v_cvt_pk_bf16_f32 v180, v180, v181
v_accvgpr_write_b32 a168, v180
v_mfma_f32_16x16x32_bf16 v[216:219], a[136:139], a[120:123], v[216:219]
buffer_load_dwordx4 v241, s[36:39], s84 offen offset:1024  lds
v_mfma_f32_16x16x32_bf16 v[216:219], a[140:143], a[124:127], v[216:219]
ds_read_b128 a[128:131], v236 offset:544
v_cvt_pk_bf16_f32 v182, v182, v183
v_mfma_f32_16x16x32_bf16 v[212:215], a[140:143], a[92:95], v[212:215]
v_accvgpr_write_b32 a169, v182
v_cvt_pk_bf16_f32 v184, v184, v185
v_mfma_f32_16x16x32_bf16 v[212:215], a[136:139], a[88:91], v[212:215]
v_accvgpr_write_b32 a170, v184
v_cvt_pk_bf16_f32 v186, v186, v187
v_mfma_f32_16x16x32_bf16 v[208:211], a[136:139], a[56:59], v[208:211]
v_accvgpr_write_b32 a171, v186
v_cvt_pk_bf16_f32 v188, v188, v189
v_mfma_f32_16x16x32_bf16 v[208:211], a[140:143], a[60:63], v[208:211]
ds_read_b128 a[132:135], v236 offset:560
v_accvgpr_write_b32 a184, v188
v_mfma_f32_16x16x32_bf16 v[204:207], a[140:143], a[28:31], v[204:207]
v_cvt_pk_bf16_f32 v190, v190, v191
v_accvgpr_write_b32 a185, v190
v_mfma_f32_16x16x32_bf16 v[204:207], a[136:139], a[24:27], v[204:207]
s_waitcnt lgkmcnt(2)
v_cvt_pk_bf16_f32 v192, v192, v193
v_accvgpr_write_b32 a186, v192
v_mfma_f32_16x16x32_bf16 v[220:223], a[144:147], a[0:3], 0
v_cvt_pk_bf16_f32 v194, v194, v195
v_accvgpr_write_b32 a187, v194
v_mfma_f32_16x16x32_bf16 v[220:223], a[148:151], a[4:7], v[220:223]
)"
R"(
ds_read_b128 a[136:139], v236 offset:576
v_cvt_pk_bf16_f32 v196, v196, v197
v_mfma_f32_16x16x32_bf16 v[224:227], a[148:151], a[36:39], 0
v_accvgpr_write_b32 a200, v196
v_cvt_pk_bf16_f32 v198, v198, v199
v_mfma_f32_16x16x32_bf16 v[224:227], a[144:147], a[32:35], v[224:227]
v_accvgpr_write_b32 a201, v198
v_cvt_pk_bf16_f32 v200, v200, v201
v_mfma_f32_16x16x32_bf16 v[228:231], a[144:147], a[64:67], 0
v_accvgpr_write_b32 a202, v200
v_cvt_pk_bf16_f32 v202, v202, v203
v_mfma_f32_16x16x32_bf16 v[228:231], a[148:151], a[68:71], v[228:231]
ds_read_b128 a[140:143], v236 offset:592
v_accvgpr_write_b32 a203, v202
v_mfma_f32_16x16x32_bf16 v[232:235], a[148:151], a[100:103], 0
v_cvt_pk_bf16_f32 v172, v172, v173
v_accvgpr_write_b32 a212, v172
v_mfma_f32_16x16x32_bf16 v[232:235], a[144:147], a[96:99], v[232:235]
s_waitcnt lgkmcnt(2)
v_cvt_pk_bf16_f32 v174, v174, v175
v_accvgpr_write_b32 a213, v174
v_mfma_f32_16x16x32_bf16 v[232:235], a[128:131], a[104:107], v[232:235]
v_cvt_pk_bf16_f32 v164, v164, v165
v_accvgpr_write_b32 a216, v164
v_mfma_f32_16x16x32_bf16 v[232:235], a[132:135], a[108:111], v[232:235]
ds_read_b128 a[144:147], v236 offset:608
v_cvt_pk_bf16_f32 v166, v166, v167
v_mfma_f32_16x16x32_bf16 v[228:231], a[132:135], a[76:79], v[228:231]
v_accvgpr_write_b32 a217, v166
v_cvt_pk_bf16_f32 v176, v176, v177
v_mfma_f32_16x16x32_bf16 v[228:231], a[128:131], a[72:75], v[228:231]
v_accvgpr_write_b32 a214, v176
v_cvt_pk_bf16_f32 v178, v178, v179
v_mfma_f32_16x16x32_bf16 v[224:227], a[128:131], a[40:43], v[224:227]
v_accvgpr_write_b32 a215, v178
v_cvt_pk_bf16_f32 v168, v168, v169
v_mfma_f32_16x16x32_bf16 v[224:227], a[132:135], a[44:47], v[224:227]
ds_read_b128 a[148:151], v236 offset:624
v_accvgpr_write_b32 a218, v168
v_mfma_f32_16x16x32_bf16 v[220:223], a[132:135], a[12:15], v[220:223]
s_waitcnt vmcnt(6) lgkmcnt(2)
v_cvt_pk_bf16_f32 v170, v170, v171
v_accvgpr_write_b32 a219, v170
v_mfma_f32_16x16x32_bf16 v[220:223], a[128:131], a[8:11], v[220:223]
s_barrier
v_mfma_f32_16x16x32_bf16 v[220:223], a[136:139], a[16:19], v[220:223]
v_mfma_f32_16x16x32_bf16 v[220:223], a[140:143], a[20:23], v[220:223]
ds_read_b128 a[128:131], v236 offset:16896
v_mfma_f32_16x16x32_bf16 v[224:227], a[140:143], a[52:55], v[224:227]
v_mfma_f32_16x16x32_bf16 v[224:227], a[136:139], a[48:51], v[224:227]
v_mfma_f32_16x16x32_bf16 v[228:231], a[136:139], a[80:83], v[228:231]
v_mfma_f32_16x16x32_bf16 v[228:231], a[140:143], a[84:87], v[228:231]
ds_read_b128 a[132:135], v236 offset:16912
v_mfma_f32_16x16x32_bf16 v[232:235], a[140:143], a[116:119], v[232:235]
v_mfma_f32_16x16x32_bf16 v[232:235], a[136:139], a[112:115], v[232:235]
s_waitcnt lgkmcnt(2)
v_mfma_f32_16x16x32_bf16 v[232:235], a[144:147], a[120:123], v[232:235]
v_mfma_f32_16x16x32_bf16 v[232:235], a[148:151], a[124:127], v[232:235]
ds_read_b128 a[136:139], v236 offset:16928
v_mfma_f32_16x16x32_bf16 v[228:231], a[148:151], a[92:95], v[228:231]
v_mfma_f32_16x16x32_bf16 v[228:231], a[144:147], a[88:91], v[228:231]
v_mfma_f32_16x16x32_bf16 v[224:227], a[144:147], a[56:59], v[224:227]
v_mfma_f32_16x16x32_bf16 v[224:227], a[148:151], a[60:63], v[224:227]
ds_read_b128 a[140:143], v236 offset:16944
v_mfma_f32_16x16x32_bf16 v[220:223], a[148:151], a[28:31], v[220:223]
v_mfma_f32_16x16x32_bf16 v[220:223], a[144:147], a[24:27], v[220:223]
s_waitcnt lgkmcnt(2)
.LC%=:
v_mfma_f32_16x16x32_bf16 v[156:159], a[128:131], a[0:3], 0
v_fma_f32 v204, v204, s56, v246
v_fma_f32 v205, v205, s56, v246
v_mfma_f32_16x16x32_bf16 v[156:159], a[132:135], a[4:7], v[156:159]
s_add_u32 m0, s60, 124448
ds_read_b128 a[144:147], v236 offset:16960
v_fma_f32 v206, v206, s56, v246
v_mfma_f32_16x16x32_bf16 v[160:163], a[132:135], a[36:39], 0
buffer_load_dwordx4 v241, s[36:39], s84 offen offset:2048  lds
v_mfma_f32_16x16x32_bf16 v[160:163], a[128:131], a[32:35], v[160:163]
v_exp_f32 v204, v204
v_mfma_f32_16x16x32_bf16 v[164:167], a[128:131], a[64:67], 0
v_exp_f32 v205, v205
v_mfma_f32_16x16x32_bf16 v[164:167], a[132:135], a[68:71], v[164:167]
ds_read_b128 a[148:151], v236 offset:16976
v_fma_f32 v207, v207, s56, v246
v_mfma_f32_16x16x32_bf16 v[168:171], a[132:135], a[100:103], 0
v_exp_f32 v206, v206
v_mfma_f32_16x16x32_bf16 v[168:171], a[128:131], a[96:99], v[168:171]
s_waitcnt lgkmcnt(2)
v_exp_f32 v207, v207
v_mfma_f32_16x16x32_bf16 v[168:171], a[136:139], a[104:107], v[168:171]
v_cvt_pk_bf16_f32 v204, v204, v205
v_accvgpr_write_b32 a220, v204
v_mfma_f32_16x16x32_bf16 v[168:171], a[140:143], a[108:111], v[168:171]
s_add_u32 m0, s60, 124464
ds_read_b128 a[128:131], v236 offset:16992
v_cvt_pk_bf16_f32 v206, v206, v207
v_mfma_f32_16x16x32_bf16 v[164:167], a[140:143], a[76:79], v[164:167]
buffer_load_dwordx4 v241, s[36:39], s84 offen offset:3072  lds
v_mfma_f32_16x16x32_bf16 v[164:167], a[136:139], a[72:75], v[164:167]
v_accvgpr_write_b32 a221, v206
v_fma_f32 v208, v208, s56, v247
v_mfma_f32_16x16x32_bf16 v[160:163], a[136:139], a[40:43], v[160:163]
v_exp_f32 v208, v208
v_mfma_f32_16x16x32_bf16 v[160:163], a[140:143], a[44:47], v[160:163]
ds_read_b128 a[132:135], v236 offset:17008
v_fma_f32 v209, v209, s56, v247
v_mfma_f32_16x16x32_bf16 v[156:159], a[140:143], a[12:15], v[156:159]
v_exp_f32 v209, v209
v_mfma_f32_16x16x32_bf16 v[156:159], a[136:139], a[8:11], v[156:159]
s_waitcnt lgkmcnt(2)
v_fma_f32 v210, v210, s56, v247
v_fma_f32 v211, v211, s56, v247
v_mfma_f32_16x16x32_bf16 v[156:159], a[144:147], a[16:19], v[156:159]
v_exp_f32 v210, v210
v_mfma_f32_16x16x32_bf16 v[156:159], a[148:151], a[20:23], v[156:159]
s_add_u32 m0, s62, 36864
ds_read_b128 a[136:139], v236 offset:17408
v_cvt_pk_bf16_f32 v0, v208, v209
v_mfma_f32_16x16x32_bf16 v[160:163], a[148:151], a[52:55], v[160:163]
buffer_load_dwordx4 v254, s[44:47], 0 offen offset:0  lds
v_mfma_f32_16x16x32_bf16 v[160:163], a[144:147], a[48:51], v[160:163]
v_exp_f32 v211, v211
v_mfma_f32_16x16x32_bf16 v[164:167], a[144:147], a[80:83], v[164:167]
v_cvt_pk_bf16_f32 v1, v210, v211
v_fma_f32 v212, v212, s56, v248
v_mfma_f32_16x16x32_bf16 v[164:167], a[148:151], a[84:87], v[164:167]
ds_read_b128 a[140:143], v236 offset:17424
v_fma_f32 v213, v213, s56, v248
v_mfma_f32_16x16x32_bf16 v[168:171], a[148:151], a[116:119], v[168:171]
v_exp_f32 v212, v212
v_mfma_f32_16x16x32_bf16 v[168:171], a[144:147], a[112:115], v[168:171]
s_waitcnt lgkmcnt(2)
v_exp_f32 v213, v213
v_mfma_f32_16x16x32_bf16 v[168:171], a[128:131], a[120:123], v[168:171]
v_fma_f32 v214, v214, s56, v248
v_fma_f32 v215, v215, s56, v248
v_mfma_f32_16x16x32_bf16 v[168:171], a[132:135], a[124:127], v[168:171]
s_mov_b32 m0, 158208
ds_read_b128 a[144:147], v236 offset:17440
v_cvt_pk_bf16_f32 v4, v212, v213
v_mfma_f32_16x16x32_bf16 v[164:167], a[132:135], a[92:95], v[164:167]
buffer_load_dwordx4 v254, s[24:27], s75 offen offset:0  lds
v_mfma_f32_16x16x32_bf16 v[164:167], a[128:131], a[88:91], v[164:167]
v_exp_f32 v214, v214
v_mfma_f32_16x16x32_bf16 v[160:163], a[128:131], a[56:59], v[160:163]
v_exp_f32 v215, v215
v_mfma_f32_16x16x32_bf16 v[160:163], a[132:135], a[60:63], v[160:163]
ds_read_b128 a[148:151], v236 offset:17456
v_cvt_pk_bf16_f32 v5, v214, v215
v_mfma_f32_16x16x32_bf16 v[156:159], a[132:135], a[28:31], v[156:159]
v_fma_f32 v216, v216, s56, v249
v_fma_f32 v217, v217, s56, v249
v_mfma_f32_16x16x32_bf16 v[156:159], a[128:131], a[24:27], v[156:159]
s_waitcnt lgkmcnt(2)
v_exp_f32 v216, v216
v_mfma_f32_16x16x32_bf16 v[172:175], a[136:139], a[0:3], 0
v_exp_f32 v217, v217
v_mfma_f32_16x16x32_bf16 v[172:175], a[140:143], a[4:7], v[172:175]
ds_read_b128 a[128:131], v236 offset:17472
v_fma_f32 v218, v218, s56, v249
v_mfma_f32_16x16x32_bf16 v[176:179], a[140:143], a[36:39], 0
v_exp_f32 v218, v218
v_mfma_f32_16x16x32_bf16 v[176:179], a[136:139], a[32:35], v[176:179]
v_fma_f32 v219, v219, s56, v249
v_cvt_pk_bf16_f32 v8, v216, v217
v_mfma_f32_16x16x32_bf16 v[180:183], a[136:139], a[64:67], 0
v_exp_f32 v219, v219
v_mfma_f32_16x16x32_bf16 v[180:183], a[140:143], a[68:71], v[180:183]
ds_read_b128 a[132:135], v236 offset:17488
v_cvt_pk_bf16_f32 v9, v218, v219
v_mfma_f32_16x16x32_bf16 v[184:187], a[140:143], a[100:103], 0
v_fma_f32 v220, v220, s56, v246
v_fma_f32 v221, v221, s56, v246
v_mfma_f32_16x16x32_bf16 v[184:187], a[136:139], a[96:99], v[184:187]
s_waitcnt lgkmcnt(2)
v_exp_f32 v220, v220
v_mfma_f32_16x16x32_bf16 v[184:187], a[144:147], a[104:107], v[184:187]
v_exp_f32 v221, v221
v_mfma_f32_16x16x32_bf16 v[184:187], a[148:151], a[108:111], v[184:187]
s_add_u32 m0, s62, 36896
ds_read_b128 a[136:139], v236 offset:17504
v_fma_f32 v222, v222, s56, v246
v_mfma_f32_16x16x32_bf16 v[180:183], a[148:151], a[76:79], v[180:183]
buffer_load_dwordx4 v254, s[44:47], 0 offen offset:1024  lds
v_mfma_f32_16x16x32_bf16 v[180:183], a[144:147], a[72:75], v[180:183]
v_exp_f32 v222, v222
v_mfma_f32_16x16x32_bf16 v[176:179], a[144:147], a[40:43], v[176:179]
v_fma_f32 v223, v223, s56, v246
v_cvt_pk_bf16_f32 v220, v220, v221
v_mfma_f32_16x16x32_bf16 v[176:179], a[148:151], a[44:47], v[176:179]
ds_read_b128 a[140:143], v236 offset:17520
v_accvgpr_write_b32 a222, v220
v_mfma_f32_16x16x32_bf16 v[172:175], a[148:151], a[12:15], v[172:175]
s_waitcnt vmcnt(7) lgkmcnt(2)
v_exp_f32 v223, v223
v_mfma_f32_16x16x32_bf16 v[172:175], a[144:147], a[8:11], v[172:175]
s_barrier
v_mfma_f32_16x16x32_bf16 v[172:175], a[128:131], a[16:19], v[172:175]
v_cvt_pk_bf16_f32 v222, v222, v223
v_accvgpr_write_b32 a223, v222
v_mfma_f32_16x16x32_bf16 v[172:175], a[132:135], a[20:23], v[172:175]
ds_read_b128 a[144:147], v236 offset:33792
v_fma_f32 v224, v224, s56, v247
v_mfma_f32_16x16x32_bf16 v[176:179], a[132:135], a[52:55], v[176:179]
v_exp_f32 v224, v224
v_mfma_f32_16x16x32_bf16 v[176:179], a[128:131], a[48:51], v[176:179]
v_fma_f32 v225, v225, s56, v247
v_fma_f32 v226, v226, s56, v247
v_mfma_f32_16x16x32_bf16 v[180:183], a[128:131], a[80:83], v[180:183]
v_exp_f32 v225, v225
v_mfma_f32_16x16x32_bf16 v[180:183], a[132:135], a[84:87], v[180:183]
ds_read_b128 a[148:151], v236 offset:33808
v_fma_f32 v227, v227, s56, v247
v_mfma_f32_16x16x32_bf16 v[184:187], a[132:135], a[116:119], v[184:187]
v_exp_f32 v226, v226
v_mfma_f32_16x16x32_bf16 v[184:187], a[128:131], a[112:115], v[184:187]
s_waitcnt lgkmcnt(2)
v_exp_f32 v227, v227
v_mfma_f32_16x16x32_bf16 v[184:187], a[136:139], a[120:123], v[184:187]
v_cvt_pk_bf16_f32 v2, v224, v225
v_cvt_pk_bf16_f32 v3, v226, v227
v_mfma_f32_16x16x32_bf16 v[184:187], a[140:143], a[124:127], v[184:187]
s_add_u32 m0, s60, 141312
ds_read_b128 a[128:131], v236 offset:33824
v_fma_f32 v228, v228, s56, v248
v_mfma_f32_16x16x32_bf16 v[180:183], a[140:143], a[92:95], v[180:183]
buffer_load_dwordx4 v241, s[36:39], s85 offen offset:0  lds
v_mfma_f32_16x16x32_bf16 v[180:183], a[136:139], a[88:91], v[180:183]
v_exp_f32 v228, v228
v_mfma_f32_16x16x32_bf16 v[176:179], a[136:139], a[56:59], v[176:179]
v_fma_f32 v229, v229, s56, v248
v_fma_f32 v230, v230, s56, v248
v_mfma_f32_16x16x32_bf16 v[176:179], a[140:143], a[60:63], v[176:179]
ds_read_b128 a[132:135], v236 offset:33840
v_fma_f32 v231, v231, s56, v248
v_mfma_f32_16x16x32_bf16 v[172:175], a[140:143], a[28:31], v[172:175]
v_exp_f32 v229, v229
v_mfma_f32_16x16x32_bf16 v[172:175], a[136:139], a[24:27], v[172:175]
s_waitcnt lgkmcnt(2)
v_exp_f32 v230, v230
v_mfma_f32_16x16x32_bf16 v[188:191], a[144:147], a[0:3], 0
v_exp_f32 v231, v231
v_mfma_f32_16x16x32_bf16 v[188:191], a[148:151], a[4:7], v[188:191]
s_add_u32 m0, s60, 141328
ds_read_b128 a[136:139], v236 offset:33856
v_cvt_pk_bf16_f32 v6, v228, v229
v_mfma_f32_16x16x32_bf16 v[192:195], a[148:151], a[36:39], 0
buffer_load_dwordx4 v241, s[36:39], s85 offen offset:1024  lds
v_mfma_f32_16x16x32_bf16 v[192:195], a[144:147], a[32:35], v[192:195]
v_cvt_pk_bf16_f32 v7, v230, v231
v_fma_f32 v232, v232, s56, v249
v_mfma_f32_16x16x32_bf16 v[196:199], a[144:147], a[64:67], 0
v_exp_f32 v232, v232
v_mfma_f32_16x16x32_bf16 v[196:199], a[148:151], a[68:71], v[196:199]
ds_read_b128 a[140:143], v236 offset:33872
v_fma_f32 v233, v233, s56, v249
v_mfma_f32_16x16x32_bf16 v[200:203], a[148:151], a[100:103], 0
v_exp_f32 v233, v233
v_mfma_f32_16x16x32_bf16 v[200:203], a[144:147], a[96:99], v[200:203]
)"
R"(
s_waitcnt lgkmcnt(2)
v_fma_f32 v234, v234, s56, v249
v_fma_f32 v235, v235, s56, v249
v_mfma_f32_16x16x32_bf16 v[200:203], a[128:131], a[104:107], v[200:203]
v_exp_f32 v234, v234
v_mfma_f32_16x16x32_bf16 v[200:203], a[132:135], a[108:111], v[200:203]
s_add_u32 m0, s60, 141344
ds_read_b128 a[144:147], v236 offset:33888
v_cvt_pk_bf16_f32 v10, v232, v233
v_mfma_f32_16x16x32_bf16 v[196:199], a[132:135], a[76:79], v[196:199]
buffer_load_dwordx4 v241, s[36:39], s85 offen offset:2048  lds
v_mfma_f32_16x16x32_bf16 v[196:199], a[128:131], a[72:75], v[196:199]
v_exp_f32 v235, v235
v_mfma_f32_16x16x32_bf16 v[192:195], a[128:131], a[40:43], v[192:195]
v_cvt_pk_bf16_f32 v11, v234, v235
v_fma_f32 v156, v156, s56, v246
v_mfma_f32_16x16x32_bf16 v[192:195], a[132:135], a[44:47], v[192:195]
ds_read_b128 a[148:151], v236 offset:33904
v_fma_f32 v157, v157, s56, v246
v_mfma_f32_16x16x32_bf16 v[188:191], a[132:135], a[12:15], v[188:191]
v_exp_f32 v156, v156
v_mfma_f32_16x16x32_bf16 v[188:191], a[128:131], a[8:11], v[188:191]
s_waitcnt lgkmcnt(2)
v_exp_f32 v157, v157
v_mfma_f32_16x16x32_bf16 v[188:191], a[136:139], a[16:19], v[188:191]
v_fma_f32 v158, v158, s56, v246
v_fma_f32 v159, v159, s56, v246
v_mfma_f32_16x16x32_bf16 v[188:191], a[140:143], a[20:23], v[188:191]
s_add_u32 m0, s60, 141360
ds_read_b128 a[128:131], v236 offset:34304
v_cvt_pk_bf16_f32 v156, v156, v157
v_mfma_f32_16x16x32_bf16 v[192:195], a[140:143], a[52:55], v[192:195]
buffer_load_dwordx4 v241, s[36:39], s85 offen offset:3072  lds
v_mfma_f32_16x16x32_bf16 v[192:195], a[136:139], a[48:51], v[192:195]
v_exp_f32 v158, v158
v_mfma_f32_16x16x32_bf16 v[196:199], a[136:139], a[80:83], v[196:199]
v_exp_f32 v159, v159
v_mfma_f32_16x16x32_bf16 v[196:199], a[140:143], a[84:87], v[196:199]
ds_read_b128 a[132:135], v236 offset:34320
v_accvgpr_write_b32 a224, v156
v_mfma_f32_16x16x32_bf16 v[200:203], a[140:143], a[116:119], v[200:203]
v_cvt_pk_bf16_f32 v158, v158, v159
v_accvgpr_write_b32 a225, v158
v_mfma_f32_16x16x32_bf16 v[200:203], a[136:139], a[112:115], v[200:203]
s_waitcnt lgkmcnt(2)
v_fma_f32 v160, v160, s56, v247
v_fma_f32 v161, v161, s56, v247
v_mfma_f32_16x16x32_bf16 v[200:203], a[144:147], a[120:123], v[200:203]
v_exp_f32 v160, v160
v_mfma_f32_16x16x32_bf16 v[200:203], a[148:151], a[124:127], v[200:203]
s_add_u32 m0, s62, 36928
ds_read_b128 a[136:139], v236 offset:34336
v_fma_f32 v162, v162, s56, v247
v_mfma_f32_16x16x32_bf16 v[196:199], a[148:151], a[92:95], v[196:199]
buffer_load_dwordx4 v254, s[44:47], 0 offen offset:2048  lds
v_mfma_f32_16x16x32_bf16 v[196:199], a[144:147], a[88:91], v[196:199]
v_exp_f32 v161, v161
v_mfma_f32_16x16x32_bf16 v[192:195], a[144:147], a[56:59], v[192:195]
v_exp_f32 v162, v162
v_mfma_f32_16x16x32_bf16 v[192:195], a[148:151], a[60:63], v[192:195]
ds_read_b128 a[140:143], v236 offset:34352
v_fma_f32 v163, v163, s56, v247
v_mfma_f32_16x16x32_bf16 v[188:191], a[148:151], a[28:31], v[188:191]
v_exp_f32 v163, v163
v_mfma_f32_16x16x32_bf16 v[188:191], a[144:147], a[24:27], v[188:191]
s_waitcnt lgkmcnt(2)
v_cvt_pk_bf16_f32 v12, v160, v161
v_cvt_pk_bf16_f32 v13, v162, v163
v_mfma_f32_16x16x32_bf16 v[140:143], a[128:131], a[0:3], 0
v_fma_f32 v164, v164, s56, v248
v_fma_f32 v165, v165, s56, v248
v_mfma_f32_16x16x32_bf16 v[140:143], a[132:135], a[4:7], v[140:143]
ds_read_b128 a[144:147], v236 offset:34368
v_fma_f32 v166, v166, s56, v248
v_mfma_f32_16x16x32_bf16 v[144:147], a[132:135], a[36:39], 0
v_exp_f32 v164, v164
v_mfma_f32_16x16x32_bf16 v[144:147], a[128:131], a[32:35], v[144:147]
v_exp_f32 v165, v165
v_mfma_f32_16x16x32_bf16 v[148:151], a[128:131], a[64:67], 0
v_exp_f32 v166, v166
v_mfma_f32_16x16x32_bf16 v[148:151], a[132:135], a[68:71], v[148:151]
ds_read_b128 a[148:151], v236 offset:34384
v_fma_f32 v167, v167, s56, v248
v_mfma_f32_16x16x32_bf16 v[152:155], a[132:135], a[100:103], 0
v_exp_f32 v167, v167
v_mfma_f32_16x16x32_bf16 v[152:155], a[128:131], a[96:99], v[152:155]
s_waitcnt lgkmcnt(2)
v_cvt_pk_bf16_f32 v16, v164, v165
v_cvt_pk_bf16_f32 v17, v166, v167
v_mfma_f32_16x16x32_bf16 v[152:155], a[136:139], a[104:107], v[152:155]
v_fma_f32 v168, v168, s56, v249
v_fma_f32 v169, v169, s56, v249
v_mfma_f32_16x16x32_bf16 v[152:155], a[140:143], a[108:111], v[152:155]
ds_read_b128 a[128:131], v236 offset:34400
v_fma_f32 v170, v170, s56, v249
v_mfma_f32_16x16x32_bf16 v[148:151], a[140:143], a[76:79], v[148:151]
v_exp_f32 v168, v168
v_mfma_f32_16x16x32_bf16 v[148:151], a[136:139], a[72:75], v[148:151]
v_exp_f32 v169, v169
v_mfma_f32_16x16x32_bf16 v[144:147], a[136:139], a[40:43], v[144:147]
v_exp_f32 v170, v170
v_mfma_f32_16x16x32_bf16 v[144:147], a[140:143], a[44:47], v[144:147]
s_add_u32 m0, s60, 90624
ds_read_b128 a[132:135], v236 offset:34416
v_fma_f32 v171, v171, s56, v249
v_mfma_f32_16x16x32_bf16 v[140:143], a[140:143], a[12:15], v[140:143]
s_waitcnt vmcnt(8) lgkmcnt(2)
v_exp_f32 v171, v171
v_mfma_f32_16x16x32_bf16 v[140:143], a[136:139], a[8:11], v[140:143]
s_barrier
v_mfma_f32_16x16x32_bf16 v[140:143], a[144:147], a[16:19], v[140:143]
buffer_load_dwordx4 v241, s[36:39], s86 offen offset:0  lds
v_mfma_f32_16x16x32_bf16 v[140:143], a[148:151], a[20:23], v[140:143]
ds_read_b128 a[136:139], v236 offset:50688
v_cvt_pk_bf16_f32 v20, v168, v169
v_mfma_f32_16x16x32_bf16 v[144:147], a[148:151], a[52:55], v[144:147]
v_cvt_pk_bf16_f32 v21, v170, v171
v_fma_f32 v172, v172, s56, v246
v_mfma_f32_16x16x32_bf16 v[144:147], a[144:147], a[48:51], v[144:147]
v_exp_f32 v172, v172
v_mfma_f32_16x16x32_bf16 v[148:151], a[144:147], a[80:83], v[148:151]
v_fma_f32 v173, v173, s56, v246
v_fma_f32 v174, v174, s56, v246
v_mfma_f32_16x16x32_bf16 v[148:151], a[148:151], a[84:87], v[148:151]
ds_read_b128 a[140:143], v236 offset:50704
v_fma_f32 v175, v175, s56, v246
v_mfma_f32_16x16x32_bf16 v[152:155], a[148:151], a[116:119], v[152:155]
s_add_u32 m0, s60, 90640
v_exp_f32 v173, v173
v_mfma_f32_16x16x32_bf16 v[152:155], a[144:147], a[112:115], v[152:155]
s_waitcnt lgkmcnt(2)
v_exp_f32 v174, v174
v_mfma_f32_16x16x32_bf16 v[152:155], a[128:131], a[120:123], v[152:155]
buffer_load_dwordx4 v241, s[36:39], s86 offen offset:1024  lds
v_mfma_f32_16x16x32_bf16 v[152:155], a[132:135], a[124:127], v[152:155]
ds_read_b128 a[144:147], v236 offset:50720
v_cvt_pk_bf16_f32 v172, v172, v173
v_mfma_f32_16x16x32_bf16 v[148:151], a[132:135], a[92:95], v[148:151]
v_exp_f32 v175, v175
v_mfma_f32_16x16x32_bf16 v[148:151], a[128:131], a[88:91], v[148:151]
v_accvgpr_write_b32 a226, v172
v_cvt_pk_bf16_f32 v174, v174, v175
v_mfma_f32_16x16x32_bf16 v[144:147], a[128:131], a[56:59], v[144:147]
v_accvgpr_write_b32 a227, v174
v_fma_f32 v176, v176, s56, v247
v_mfma_f32_16x16x32_bf16 v[144:147], a[132:135], a[60:63], v[144:147]
ds_read_b128 a[148:151], v236 offset:50736
v_fma_f32 v177, v177, s56, v247
v_mfma_f32_16x16x32_bf16 v[140:143], a[132:135], a[28:31], v[140:143]
s_add_u32 m0, s62, 36960
v_exp_f32 v176, v176
v_mfma_f32_16x16x32_bf16 v[140:143], a[128:131], a[24:27], v[140:143]
s_waitcnt lgkmcnt(2)
v_exp_f32 v177, v177
v_mfma_f32_16x16x32_bf16 v[204:207], a[136:139], a[0:3], 0
buffer_load_dwordx4 v254, s[44:47], 0 offen offset:3072  lds
v_mfma_f32_16x16x32_bf16 v[204:207], a[140:143], a[4:7], v[204:207]
ds_read_b128 a[128:131], v236 offset:50752
v_fma_f32 v178, v178, s56, v247
v_mfma_f32_16x16x32_bf16 v[208:211], a[140:143], a[36:39], 0
v_exp_f32 v178, v178
v_mfma_f32_16x16x32_bf16 v[208:211], a[136:139], a[32:35], v[208:211]
v_fma_f32 v179, v179, s56, v247
v_cvt_pk_bf16_f32 v14, v176, v177
v_mfma_f32_16x16x32_bf16 v[212:215], a[136:139], a[64:67], 0
v_exp_f32 v179, v179
v_mfma_f32_16x16x32_bf16 v[212:215], a[140:143], a[68:71], v[212:215]
ds_read_b128 a[132:135], v236 offset:50768
v_cvt_pk_bf16_f32 v15, v178, v179
v_mfma_f32_16x16x32_bf16 v[216:219], a[140:143], a[100:103], 0
s_add_u32 m0, s60, 90656
v_fma_f32 v180, v180, s56, v248
v_fma_f32 v181, v181, s56, v248
v_mfma_f32_16x16x32_bf16 v[216:219], a[136:139], a[96:99], v[216:219]
s_waitcnt lgkmcnt(2)
v_exp_f32 v180, v180
v_mfma_f32_16x16x32_bf16 v[216:219], a[144:147], a[104:107], v[216:219]
buffer_load_dwordx4 v241, s[36:39], s86 offen offset:2048  lds
v_mfma_f32_16x16x32_bf16 v[216:219], a[148:151], a[108:111], v[216:219]
ds_read_b128 a[136:139], v236 offset:50784
v_fma_f32 v182, v182, s56, v248
v_mfma_f32_16x16x32_bf16 v[212:215], a[148:151], a[76:79], v[212:215]
v_exp_f32 v181, v181
v_mfma_f32_16x16x32_bf16 v[212:215], a[144:147], a[72:75], v[212:215]
v_exp_f32 v182, v182
v_mfma_f32_16x16x32_bf16 v[208:211], a[144:147], a[40:43], v[208:211]
v_fma_f32 v183, v183, s56, v248
v_cvt_pk_bf16_f32 v18, v180, v181
v_mfma_f32_16x16x32_bf16 v[208:211], a[148:151], a[44:47], v[208:211]
ds_read_b128 a[140:143], v236 offset:50800
v_fma_f32 v184, v184, s56, v249
v_mfma_f32_16x16x32_bf16 v[204:207], a[148:151], a[12:15], v[204:207]
s_add_u32 m0, s60, 90672
v_exp_f32 v183, v183
v_mfma_f32_16x16x32_bf16 v[204:207], a[144:147], a[8:11], v[204:207]
s_waitcnt lgkmcnt(2)
v_exp_f32 v184, v184
v_mfma_f32_16x16x32_bf16 v[204:207], a[128:131], a[16:19], v[204:207]
buffer_load_dwordx4 v241, s[36:39], s86 offen offset:3072  lds
v_mfma_f32_16x16x32_bf16 v[204:207], a[132:135], a[20:23], v[204:207]
ds_read_b128 a[144:147], v236 offset:51200
v_cvt_pk_bf16_f32 v19, v182, v183
v_mfma_f32_16x16x32_bf16 v[208:211], a[132:135], a[52:55], v[208:211]
v_fma_f32 v185, v185, s56, v249
v_fma_f32 v186, v186, s56, v249
v_mfma_f32_16x16x32_bf16 v[208:211], a[128:131], a[48:51], v[208:211]
v_exp_f32 v185, v185
v_mfma_f32_16x16x32_bf16 v[212:215], a[128:131], a[80:83], v[212:215]
v_exp_f32 v186, v186
v_mfma_f32_16x16x32_bf16 v[212:215], a[132:135], a[84:87], v[212:215]
ds_read_b128 a[148:151], v236 offset:51216
v_fma_f32 v187, v187, s56, v249
v_mfma_f32_16x16x32_bf16 v[216:219], a[132:135], a[116:119], v[216:219]
v_exp_f32 v187, v187
v_mfma_f32_16x16x32_bf16 v[216:219], a[128:131], a[112:115], v[216:219]
s_waitcnt lgkmcnt(2)
v_cvt_pk_bf16_f32 v22, v184, v185
v_cvt_pk_bf16_f32 v23, v186, v187
v_mfma_f32_16x16x32_bf16 v[216:219], a[136:139], a[120:123], v[216:219]
v_fma_f32 v188, v188, s56, v246
v_fma_f32 v189, v189, s56, v246
v_mfma_f32_16x16x32_bf16 v[216:219], a[140:143], a[124:127], v[216:219]
ds_read_b128 a[128:131], v236 offset:51232
v_fma_f32 v190, v190, s56, v246
v_mfma_f32_16x16x32_bf16 v[212:215], a[140:143], a[92:95], v[212:215]
v_exp_f32 v188, v188
v_mfma_f32_16x16x32_bf16 v[212:215], a[136:139], a[88:91], v[212:215]
v_exp_f32 v189, v189
v_mfma_f32_16x16x32_bf16 v[208:211], a[136:139], a[56:59], v[208:211]
v_exp_f32 v190, v190
v_mfma_f32_16x16x32_bf16 v[208:211], a[140:143], a[60:63], v[208:211]
ds_read_b128 a[132:135], v236 offset:51248
v_fma_f32 v191, v191, s56, v246
v_mfma_f32_16x16x32_bf16 v[204:207], a[140:143], a[28:31], v[204:207]
v_exp_f32 v191, v191
v_mfma_f32_16x16x32_bf16 v[204:207], a[136:139], a[24:27], v[204:207]
s_waitcnt lgkmcnt(2)
v_cvt_pk_bf16_f32 v24, v188, v189
v_cvt_pk_bf16_f32 v25, v190, v191
v_mfma_f32_16x16x32_bf16 v[220:223], a[144:147], a[0:3], 0
v_fma_f32 v192, v192, s56, v247
v_fma_f32 v193, v193, s56, v247
v_mfma_f32_16x16x32_bf16 v[220:223], a[148:151], a[4:7], v[220:223]
ds_read_b128 a[136:139], v236 offset:51264
v_fma_f32 v194, v194, s56, v247
v_mfma_f32_16x16x32_bf16 v[224:227], a[148:151], a[36:39], 0
v_exp_f32 v192, v192
v_mfma_f32_16x16x32_bf16 v[224:227], a[144:147], a[32:35], v[224:227]
v_exp_f32 v193, v193
v_mfma_f32_16x16x32_bf16 v[228:231], a[144:147], a[64:67], 0
v_exp_f32 v194, v194
v_mfma_f32_16x16x32_bf16 v[228:231], a[148:151], a[68:71], v[228:231]
ds_read_b128 a[140:143], v236 offset:51280
v_fma_f32 v195, v195, s56, v247
v_mfma_f32_16x16x32_bf16 v[232:235], a[148:151], a[100:103], 0
v_exp_f32 v195, v195
v_mfma_f32_16x16x32_bf16 v[232:235], a[144:147], a[96:99], v[232:235]
s_waitcnt lgkmcnt(2)
v_cvt_pk_bf16_f32 v192, v192, v193
v_accvgpr_write_b32 a228, v192
v_mfma_f32_16x16x32_bf16 v[232:235], a[128:131], a[104:107], v[232:235]
v_cvt_pk_bf16_f32 v194, v194, v195
v_accvgpr_write_b32 a229, v194
v_mfma_f32_16x16x32_bf16 v[232:235], a[132:135], a[108:111], v[232:235]
s_add_u32 m0, s62, 41088
ds_read_b128 a[144:147], v236 offset:51296
)"
R"(
v_fma_f32 v196, v196, s56, v248
v_mfma_f32_16x16x32_bf16 v[228:231], a[132:135], a[76:79], v[228:231]
buffer_load_dwordx4 v254, s[44:47], s72 offen offset:0  lds
v_mfma_f32_16x16x32_bf16 v[228:231], a[128:131], a[72:75], v[228:231]
v_exp_f32 v196, v196
v_mfma_f32_16x16x32_bf16 v[224:227], a[128:131], a[40:43], v[224:227]
v_fma_f32 v197, v197, s56, v248
v_fma_f32 v198, v198, s56, v248
v_mfma_f32_16x16x32_bf16 v[224:227], a[132:135], a[44:47], v[224:227]
.Lpad2_%=:
s_cbranch_vccnz .Lret0_%=
ds_read_b128 a[148:151], v236 offset:51312
v_fma_f32 v199, v199, s56, v248
v_mfma_f32_16x16x32_bf16 v[220:223], a[132:135], a[12:15], v[220:223]
s_waitcnt vmcnt(7) lgkmcnt(2)
v_exp_f32 v197, v197
v_mfma_f32_16x16x32_bf16 v[220:223], a[128:131], a[8:11], v[220:223]
s_barrier
v_mfma_f32_16x16x32_bf16 v[220:223], a[136:139], a[16:19], v[220:223]
v_exp_f32 v198, v198
v_mfma_f32_16x16x32_bf16 v[220:223], a[140:143], a[20:23], v[220:223]
s_add_u32 m0, s60, 107520
ds_read_b128 a[128:131], v237 offset:0
v_cvt_pk_bf16_f32 v28, v196, v197
v_mfma_f32_16x16x32_bf16 v[224:227], a[140:143], a[52:55], v[224:227]
buffer_load_dwordx4 v241, s[36:39], s87 offen offset:0  lds
v_mfma_f32_16x16x32_bf16 v[224:227], a[136:139], a[48:51], v[224:227]
v_exp_f32 v199, v199
v_mfma_f32_16x16x32_bf16 v[228:231], a[136:139], a[80:83], v[228:231]
v_cvt_pk_bf16_f32 v29, v198, v199
v_fma_f32 v200, v200, s56, v249
v_mfma_f32_16x16x32_bf16 v[228:231], a[140:143], a[84:87], v[228:231]
ds_read_b128 a[132:135], v237 offset:16
v_fma_f32 v201, v201, s56, v249
v_mfma_f32_16x16x32_bf16 v[232:235], a[140:143], a[116:119], v[232:235]
v_exp_f32 v200, v200
v_mfma_f32_16x16x32_bf16 v[232:235], a[136:139], a[112:115], v[232:235]
s_waitcnt lgkmcnt(2)
v_exp_f32 v201, v201
v_mfma_f32_16x16x32_bf16 v[232:235], a[144:147], a[120:123], v[232:235]
v_fma_f32 v202, v202, s56, v249
v_fma_f32 v203, v203, s56, v249
v_mfma_f32_16x16x32_bf16 v[232:235], a[148:151], a[124:127], v[232:235]
s_add_u32 m0, s60, 107536
ds_read_b128 a[136:139], v237 offset:32
v_cvt_pk_bf16_f32 v32, v200, v201
v_mfma_f32_16x16x32_bf16 v[228:231], a[148:151], a[92:95], v[228:231]
buffer_load_dwordx4 v241, s[36:39], s87 offen offset:1024  lds
v_mfma_f32_16x16x32_bf16 v[228:231], a[144:147], a[88:91], v[228:231]
v_exp_f32 v202, v202
v_mfma_f32_16x16x32_bf16 v[224:227], a[144:147], a[56:59], v[224:227]
v_exp_f32 v203, v203
v_mfma_f32_16x16x32_bf16 v[224:227], a[148:151], a[60:63], v[224:227]
ds_read_b128 a[140:143], v237 offset:48
v_cvt_pk_bf16_f32 v33, v202, v203
v_mfma_f32_16x16x32_bf16 v[220:223], a[148:151], a[28:31], v[220:223]
v_fma_f32 v140, v140, s56, v246
v_fma_f32 v141, v141, s56, v246
v_mfma_f32_16x16x32_bf16 v[220:223], a[144:147], a[24:27], v[220:223]
s_waitcnt lgkmcnt(2)
v_exp_f32 v140, v140
v_mfma_f32_16x16x32_bf16 v[156:159], a[128:131], a[0:3], 0
v_exp_f32 v141, v141
v_mfma_f32_16x16x32_bf16 v[156:159], a[132:135], a[4:7], v[156:159]
s_add_u32 m0, s60, 107552
ds_read_b128 a[144:147], v237 offset:64
v_fma_f32 v142, v142, s56, v246
v_mfma_f32_16x16x32_bf16 v[160:163], a[132:135], a[36:39], 0
buffer_load_dwordx4 v241, s[36:39], s87 offen offset:2048  lds
v_mfma_f32_16x16x32_bf16 v[160:163], a[128:131], a[32:35], v[160:163]
v_exp_f32 v142, v142
v_mfma_f32_16x16x32_bf16 v[164:167], a[128:131], a[64:67], 0
v_fma_f32 v143, v143, s56, v246
v_cvt_pk_bf16_f32 v26, v140, v141
v_mfma_f32_16x16x32_bf16 v[164:167], a[132:135], a[68:71], v[164:167]
ds_read_b128 a[148:151], v237 offset:80
v_fma_f32 v144, v144, s56, v247
v_mfma_f32_16x16x32_bf16 v[168:171], a[132:135], a[100:103], 0
v_exp_f32 v143, v143
v_mfma_f32_16x16x32_bf16 v[168:171], a[128:131], a[96:99], v[168:171]
s_waitcnt lgkmcnt(2)
v_exp_f32 v144, v144
v_mfma_f32_16x16x32_bf16 v[168:171], a[136:139], a[104:107], v[168:171]
v_cvt_pk_bf16_f32 v27, v142, v143
v_fma_f32 v145, v145, s56, v247
v_mfma_f32_16x16x32_bf16 v[168:171], a[140:143], a[108:111], v[168:171]
s_add_u32 m0, s60, 107568
ds_read_b128 a[128:131], v237 offset:96
v_fma_f32 v146, v146, s56, v247
v_mfma_f32_16x16x32_bf16 v[164:167], a[140:143], a[76:79], v[164:167]
buffer_load_dwordx4 v241, s[36:39], s87 offen offset:3072  lds
v_mfma_f32_16x16x32_bf16 v[164:167], a[136:139], a[72:75], v[164:167]
v_exp_f32 v145, v145
v_mfma_f32_16x16x32_bf16 v[160:163], a[136:139], a[40:43], v[160:163]
v_exp_f32 v146, v146
v_mfma_f32_16x16x32_bf16 v[160:163], a[140:143], a[44:47], v[160:163]
ds_read_b128 a[132:135], v237 offset:112
v_fma_f32 v147, v147, s56, v247
v_mfma_f32_16x16x32_bf16 v[156:159], a[140:143], a[12:15], v[156:159]
v_exp_f32 v147, v147
v_mfma_f32_16x16x32_bf16 v[156:159], a[136:139], a[8:11], v[156:159]
s_waitcnt lgkmcnt(2)
v_cvt_pk_bf16_f32 v144, v144, v145
v_accvgpr_write_b32 a230, v144
v_mfma_f32_16x16x32_bf16 v[156:159], a[144:147], a[16:19], v[156:159]
v_cvt_pk_bf16_f32 v146, v146, v147
v_accvgpr_write_b32 a231, v146
v_mfma_f32_16x16x32_bf16 v[156:159], a[148:151], a[20:23], v[156:159]
s_mov_b32 m0, 156160
ds_read_b128 a[136:139], v237 offset:512
v_fma_f32 v148, v148, s56, v248
v_mfma_f32_16x16x32_bf16 v[160:163], a[148:151], a[52:55], v[160:163]
buffer_load_dwordx4 v254, s[24:27], s75 offen offset:2048  lds
v_mfma_f32_16x16x32_bf16 v[160:163], a[144:147], a[48:51], v[160:163]
v_exp_f32 v148, v148
v_mfma_f32_16x16x32_bf16 v[164:167], a[144:147], a[80:83], v[164:167]
v_fma_f32 v149, v149, s56, v248
v_fma_f32 v150, v150, s56, v248
v_mfma_f32_16x16x32_bf16 v[164:167], a[148:151], a[84:87], v[164:167]
ds_read_b128 a[140:143], v237 offset:528
v_fma_f32 v151, v151, s56, v248
v_mfma_f32_16x16x32_bf16 v[168:171], a[148:151], a[116:119], v[168:171]
v_exp_f32 v149, v149
v_mfma_f32_16x16x32_bf16 v[168:171], a[144:147], a[112:115], v[168:171]
s_waitcnt lgkmcnt(2)
v_exp_f32 v150, v150
v_mfma_f32_16x16x32_bf16 v[168:171], a[128:131], a[120:123], v[168:171]
v_exp_f32 v151, v151
v_mfma_f32_16x16x32_bf16 v[168:171], a[132:135], a[124:127], v[168:171]
s_add_u32 m0, s62, 41120
ds_read_b128 a[144:147], v237 offset:544
v_cvt_pk_bf16_f32 v30, v148, v149
v_mfma_f32_16x16x32_bf16 v[164:167], a[132:135], a[92:95], v[164:167]
buffer_load_dwordx4 v254, s[44:47], s72 offen offset:1024  lds
v_mfma_f32_16x16x32_bf16 v[164:167], a[128:131], a[88:91], v[164:167]
v_cvt_pk_bf16_f32 v31, v150, v151
v_fma_f32 v152, v152, s56, v249
v_mfma_f32_16x16x32_bf16 v[160:163], a[128:131], a[56:59], v[160:163]
v_exp_f32 v152, v152
v_mfma_f32_16x16x32_bf16 v[160:163], a[132:135], a[60:63], v[160:163]
ds_read_b128 a[148:151], v237 offset:560
v_fma_f32 v153, v153, s56, v249
v_mfma_f32_16x16x32_bf16 v[156:159], a[132:135], a[28:31], v[156:159]
v_exp_f32 v153, v153
v_mfma_f32_16x16x32_bf16 v[156:159], a[128:131], a[24:27], v[156:159]
s_waitcnt lgkmcnt(2)
v_fma_f32 v154, v154, s56, v249
v_fma_f32 v155, v155, s56, v249
v_mfma_f32_16x16x32_bf16 v[172:175], a[136:139], a[0:3], 0
v_exp_f32 v154, v154
v_mfma_f32_16x16x32_bf16 v[172:175], a[140:143], a[4:7], v[172:175]
ds_read_b128 a[128:131], v237 offset:576
v_cvt_pk_bf16_f32 v34, v152, v153
v_mfma_f32_16x16x32_bf16 v[176:179], a[140:143], a[36:39], 0
v_exp_f32 v155, v155
v_mfma_f32_16x16x32_bf16 v[176:179], a[136:139], a[32:35], v[176:179]
v_cvt_pk_bf16_f32 v35, v154, v155
v_fma_f32 v204, v204, s56, v246
v_mfma_f32_16x16x32_bf16 v[180:183], a[136:139], a[64:67], 0
v_exp_f32 v204, v204
v_mfma_f32_16x16x32_bf16 v[180:183], a[140:143], a[68:71], v[180:183]
ds_read_b128 a[132:135], v237 offset:592
v_fma_f32 v205, v205, s56, v246
v_mfma_f32_16x16x32_bf16 v[184:187], a[140:143], a[100:103], 0
v_exp_f32 v205, v205
v_mfma_f32_16x16x32_bf16 v[184:187], a[136:139], a[96:99], v[184:187]
s_waitcnt lgkmcnt(2)
v_fma_f32 v206, v206, s56, v246
v_fma_f32 v207, v207, s56, v246
v_mfma_f32_16x16x32_bf16 v[184:187], a[144:147], a[104:107], v[184:187]
v_exp_f32 v206, v206
v_mfma_f32_16x16x32_bf16 v[184:187], a[148:151], a[108:111], v[184:187]
ds_read_b128 a[136:139], v237 offset:608
v_cvt_pk_bf16_f32 v36, v204, v205
v_mfma_f32_16x16x32_bf16 v[180:183], a[148:151], a[76:79], v[180:183]
v_exp_f32 v207, v207
v_mfma_f32_16x16x32_bf16 v[180:183], a[144:147], a[72:75], v[180:183]
v_cvt_pk_bf16_f32 v37, v206, v207
v_fma_f32 v208, v208, s56, v247
v_mfma_f32_16x16x32_bf16 v[176:179], a[144:147], a[40:43], v[176:179]
v_exp_f32 v208, v208
v_mfma_f32_16x16x32_bf16 v[176:179], a[148:151], a[44:47], v[176:179]
ds_read_b128 a[140:143], v237 offset:624
v_fma_f32 v209, v209, s56, v247
v_mfma_f32_16x16x32_bf16 v[172:175], a[148:151], a[12:15], v[172:175]
s_waitcnt vmcnt(7) lgkmcnt(2)
v_exp_f32 v209, v209
v_mfma_f32_16x16x32_bf16 v[172:175], a[144:147], a[8:11], v[172:175]
s_barrier
v_mfma_f32_16x16x32_bf16 v[172:175], a[128:131], a[16:19], v[172:175]
v_fma_f32 v210, v210, s56, v247
v_fma_f32 v211, v211, s56, v247
v_mfma_f32_16x16x32_bf16 v[172:175], a[132:135], a[20:23], v[172:175]
s_add_u32 m0, s60, 124416
ds_read_b128 a[144:147], v236 offset:16896
v_cvt_pk_bf16_f32 v40, v208, v209
v_mfma_f32_16x16x32_bf16 v[176:179], a[132:135], a[52:55], v[176:179]
buffer_load_dwordx4 v241, s[36:39], s88 offen offset:0  lds
v_mfma_f32_16x16x32_bf16 v[176:179], a[128:131], a[48:51], v[176:179]
v_exp_f32 v210, v210
v_mfma_f32_16x16x32_bf16 v[180:183], a[128:131], a[80:83], v[180:183]
v_exp_f32 v211, v211
v_mfma_f32_16x16x32_bf16 v[180:183], a[132:135], a[84:87], v[180:183]
ds_read_b128 a[148:151], v236 offset:16912
v_cvt_pk_bf16_f32 v41, v210, v211
v_mfma_f32_16x16x32_bf16 v[184:187], a[132:135], a[116:119], v[184:187]
v_fma_f32 v212, v212, s56, v248
v_fma_f32 v213, v213, s56, v248
v_mfma_f32_16x16x32_bf16 v[184:187], a[128:131], a[112:115], v[184:187]
s_waitcnt lgkmcnt(2)
v_exp_f32 v212, v212
v_mfma_f32_16x16x32_bf16 v[184:187], a[136:139], a[120:123], v[184:187]
v_exp_f32 v213, v213
v_mfma_f32_16x16x32_bf16 v[184:187], a[140:143], a[124:127], v[184:187]
s_add_u32 m0, s60, 124432
ds_read_b128 a[128:131], v236 offset:16928
v_fma_f32 v214, v214, s56, v248
v_mfma_f32_16x16x32_bf16 v[180:183], a[140:143], a[92:95], v[180:183]
buffer_load_dwordx4 v241, s[36:39], s88 offen offset:1024  lds
v_mfma_f32_16x16x32_bf16 v[180:183], a[136:139], a[88:91], v[180:183]
v_exp_f32 v214, v214
v_mfma_f32_16x16x32_bf16 v[176:179], a[136:139], a[56:59], v[176:179]
v_fma_f32 v215, v215, s56, v248
v_cvt_pk_bf16_f32 v212, v212, v213
v_mfma_f32_16x16x32_bf16 v[176:179], a[140:143], a[60:63], v[176:179]
ds_read_b128 a[132:135], v236 offset:16944
v_accvgpr_write_b32 a232, v212
v_mfma_f32_16x16x32_bf16 v[172:175], a[140:143], a[28:31], v[172:175]
v_exp_f32 v215, v215
v_mfma_f32_16x16x32_bf16 v[172:175], a[136:139], a[24:27], v[172:175]
s_waitcnt lgkmcnt(2)
v_cvt_pk_bf16_f32 v214, v214, v215
v_accvgpr_write_b32 a233, v214
v_mfma_f32_16x16x32_bf16 v[188:191], a[144:147], a[0:3], 0
v_fma_f32 v216, v216, s56, v249
v_fma_f32 v217, v217, s56, v249
v_mfma_f32_16x16x32_bf16 v[188:191], a[148:151], a[4:7], v[188:191]
s_add_u32 m0, s60, 124448
ds_read_b128 a[136:139], v236 offset:16960
v_fma_f32 v218, v218, s56, v249
v_mfma_f32_16x16x32_bf16 v[192:195], a[148:151], a[36:39], 0
buffer_load_dwordx4 v241, s[36:39], s88 offen offset:2048  lds
v_mfma_f32_16x16x32_bf16 v[192:195], a[144:147], a[32:35], v[192:195]
v_exp_f32 v216, v216
v_mfma_f32_16x16x32_bf16 v[196:199], a[144:147], a[64:67], 0
v_exp_f32 v217, v217
v_mfma_f32_16x16x32_bf16 v[196:199], a[148:151], a[68:71], v[196:199]
ds_read_b128 a[140:143], v236 offset:16976
v_fma_f32 v219, v219, s56, v249
v_mfma_f32_16x16x32_bf16 v[200:203], a[148:151], a[100:103], 0
v_exp_f32 v218, v218
v_mfma_f32_16x16x32_bf16 v[200:203], a[144:147], a[96:99], v[200:203]
s_waitcnt lgkmcnt(2)
v_exp_f32 v219, v219
v_mfma_f32_16x16x32_bf16 v[200:203], a[128:131], a[104:107], v[200:203]
v_cvt_pk_bf16_f32 v44, v216, v217
v_cvt_pk_bf16_f32 v45, v218, v219
v_mfma_f32_16x16x32_bf16 v[200:203], a[132:135], a[108:111], v[200:203]
s_add_u32 m0, s60, 124464
ds_read_b128 a[144:147], v236 offset:16992
v_fma_f32 v220, v220, s56, v246
v_mfma_f32_16x16x32_bf16 v[196:199], a[132:135], a[76:79], v[196:199]
buffer_load_dwordx4 v241, s[36:39], s88 offen offset:3072  lds
v_mfma_f32_16x16x32_bf16 v[196:199], a[128:131], a[72:75], v[196:199]
v_exp_f32 v220, v220
)"
R"(
v_mfma_f32_16x16x32_bf16 v[192:195], a[128:131], a[40:43], v[192:195]
v_fma_f32 v221, v221, s56, v246
v_fma_f32 v222, v222, s56, v246
v_mfma_f32_16x16x32_bf16 v[192:195], a[132:135], a[44:47], v[192:195]
ds_read_b128 a[148:151], v236 offset:17008
v_fma_f32 v223, v223, s56, v246
v_mfma_f32_16x16x32_bf16 v[188:191], a[132:135], a[12:15], v[188:191]
v_exp_f32 v221, v221
v_mfma_f32_16x16x32_bf16 v[188:191], a[128:131], a[8:11], v[188:191]
s_waitcnt lgkmcnt(2)
v_exp_f32 v222, v222
v_mfma_f32_16x16x32_bf16 v[188:191], a[136:139], a[16:19], v[188:191]
v_exp_f32 v223, v223
v_mfma_f32_16x16x32_bf16 v[188:191], a[140:143], a[20:23], v[188:191]
s_add_u32 m0, s62, 41152
ds_read_b128 a[128:131], v236 offset:17408
v_cvt_pk_bf16_f32 v38, v220, v221
v_mfma_f32_16x16x32_bf16 v[192:195], a[140:143], a[52:55], v[192:195]
buffer_load_dwordx4 v254, s[44:47], s72 offen offset:2048  lds
v_mfma_f32_16x16x32_bf16 v[192:195], a[136:139], a[48:51], v[192:195]
v_cvt_pk_bf16_f32 v39, v222, v223
v_fma_f32 v224, v224, s56, v247
v_mfma_f32_16x16x32_bf16 v[196:199], a[136:139], a[80:83], v[196:199]
v_exp_f32 v224, v224
v_mfma_f32_16x16x32_bf16 v[196:199], a[140:143], a[84:87], v[196:199]
ds_read_b128 a[132:135], v236 offset:17424
v_fma_f32 v225, v225, s56, v247
v_mfma_f32_16x16x32_bf16 v[200:203], a[140:143], a[116:119], v[200:203]
v_exp_f32 v225, v225
v_mfma_f32_16x16x32_bf16 v[200:203], a[136:139], a[112:115], v[200:203]
s_waitcnt lgkmcnt(2)
v_fma_f32 v226, v226, s56, v247
v_fma_f32 v227, v227, s56, v247
v_mfma_f32_16x16x32_bf16 v[200:203], a[144:147], a[120:123], v[200:203]
v_exp_f32 v226, v226
v_mfma_f32_16x16x32_bf16 v[200:203], a[148:151], a[124:127], v[200:203]
ds_read_b128 a[136:139], v236 offset:17440
v_cvt_pk_bf16_f32 v42, v224, v225
v_mfma_f32_16x16x32_bf16 v[196:199], a[148:151], a[92:95], v[196:199]
v_exp_f32 v227, v227
v_mfma_f32_16x16x32_bf16 v[196:199], a[144:147], a[88:91], v[196:199]
v_cvt_pk_bf16_f32 v43, v226, v227
v_fma_f32 v228, v228, s56, v248
v_mfma_f32_16x16x32_bf16 v[192:195], a[144:147], a[56:59], v[192:195]
v_exp_f32 v228, v228
v_mfma_f32_16x16x32_bf16 v[192:195], a[148:151], a[60:63], v[192:195]
ds_read_b128 a[140:143], v236 offset:17456
v_fma_f32 v229, v229, s56, v248
v_mfma_f32_16x16x32_bf16 v[188:191], a[148:151], a[28:31], v[188:191]
v_exp_f32 v229, v229
v_mfma_f32_16x16x32_bf16 v[188:191], a[144:147], a[24:27], v[188:191]
s_waitcnt lgkmcnt(2)
v_fma_f32 v230, v230, s56, v248
v_fma_f32 v231, v231, s56, v248
v_mfma_f32_16x16x32_bf16 v[140:143], a[128:131], a[0:3], 0
v_exp_f32 v230, v230
v_mfma_f32_16x16x32_bf16 v[140:143], a[132:135], a[4:7], v[140:143]
ds_read_b128 a[144:147], v236 offset:17472
v_cvt_pk_bf16_f32 v228, v228, v229
v_mfma_f32_16x16x32_bf16 v[144:147], a[132:135], a[36:39], 0
v_exp_f32 v231, v231
v_mfma_f32_16x16x32_bf16 v[144:147], a[128:131], a[32:35], v[144:147]
v_accvgpr_write_b32 a234, v228
v_cvt_pk_bf16_f32 v230, v230, v231
v_mfma_f32_16x16x32_bf16 v[148:151], a[128:131], a[64:67], 0
v_accvgpr_write_b32 a235, v230
v_fma_f32 v232, v232, s56, v249
v_mfma_f32_16x16x32_bf16 v[148:151], a[132:135], a[68:71], v[148:151]
ds_read_b128 a[148:151], v236 offset:17488
v_fma_f32 v233, v233, s56, v249
v_mfma_f32_16x16x32_bf16 v[152:155], a[132:135], a[100:103], 0
v_exp_f32 v232, v232
v_mfma_f32_16x16x32_bf16 v[152:155], a[128:131], a[96:99], v[152:155]
s_waitcnt lgkmcnt(2)
v_exp_f32 v233, v233
v_mfma_f32_16x16x32_bf16 v[152:155], a[136:139], a[104:107], v[152:155]
v_fma_f32 v234, v234, s56, v249
v_fma_f32 v235, v235, s56, v249
v_mfma_f32_16x16x32_bf16 v[152:155], a[140:143], a[108:111], v[152:155]
s_add_u32 m0, s62, 41184
ds_read_b128 a[128:131], v236 offset:17504
v_cvt_pk_bf16_f32 v46, v232, v233
v_mfma_f32_16x16x32_bf16 v[148:151], a[140:143], a[76:79], v[148:151]
buffer_load_dwordx4 v254, s[44:47], s72 offen offset:3072  lds
v_mfma_f32_16x16x32_bf16 v[148:151], a[136:139], a[72:75], v[148:151]
v_exp_f32 v234, v234
v_mfma_f32_16x16x32_bf16 v[144:147], a[136:139], a[40:43], v[144:147]
v_exp_f32 v235, v235
v_mfma_f32_16x16x32_bf16 v[144:147], a[140:143], a[44:47], v[144:147]
ds_read_b128 a[132:135], v236 offset:17520
v_cvt_pk_bf16_f32 v47, v234, v235
v_mfma_f32_16x16x32_bf16 v[140:143], a[140:143], a[12:15], v[140:143]
s_waitcnt vmcnt(8) lgkmcnt(2)
v_fma_f32 v156, v156, s56, v246
v_fma_f32 v157, v157, s56, v246
v_mfma_f32_16x16x32_bf16 v[140:143], a[136:139], a[8:11], v[140:143]
s_barrier
v_mfma_f32_16x16x32_bf16 v[140:143], a[144:147], a[16:19], v[140:143]
v_exp_f32 v156, v156
v_mfma_f32_16x16x32_bf16 v[140:143], a[148:151], a[20:23], v[140:143]
s_add_u32 m0, s60, 141312
ds_read_b128 a[136:139], v236 offset:33792
v_fma_f32 v158, v158, s56, v246
v_mfma_f32_16x16x32_bf16 v[144:147], a[148:151], a[52:55], v[144:147]
buffer_load_dwordx4 v241, s[36:39], s89 offen offset:0  lds
v_mfma_f32_16x16x32_bf16 v[144:147], a[144:147], a[48:51], v[144:147]
v_exp_f32 v157, v157
v_mfma_f32_16x16x32_bf16 v[148:151], a[144:147], a[80:83], v[148:151]
v_exp_f32 v158, v158
v_mfma_f32_16x16x32_bf16 v[148:151], a[148:151], a[84:87], v[148:151]
ds_read_b128 a[140:143], v236 offset:33808
v_fma_f32 v159, v159, s56, v246
v_mfma_f32_16x16x32_bf16 v[152:155], a[148:151], a[116:119], v[152:155]
v_exp_f32 v159, v159
v_mfma_f32_16x16x32_bf16 v[152:155], a[144:147], a[112:115], v[152:155]
s_waitcnt lgkmcnt(2)
v_cvt_pk_bf16_f32 v48, v156, v157
v_cvt_pk_bf16_f32 v49, v158, v159
v_mfma_f32_16x16x32_bf16 v[152:155], a[128:131], a[120:123], v[152:155]
v_fma_f32 v160, v160, s56, v247
v_fma_f32 v161, v161, s56, v247
v_mfma_f32_16x16x32_bf16 v[152:155], a[132:135], a[124:127], v[152:155]
s_add_u32 m0, s60, 141328
ds_read_b128 a[144:147], v236 offset:33824
v_fma_f32 v162, v162, s56, v247
v_mfma_f32_16x16x32_bf16 v[148:151], a[132:135], a[92:95], v[148:151]
buffer_load_dwordx4 v241, s[36:39], s89 offen offset:1024  lds
v_mfma_f32_16x16x32_bf16 v[148:151], a[128:131], a[88:91], v[148:151]
v_exp_f32 v160, v160
v_mfma_f32_16x16x32_bf16 v[144:147], a[128:131], a[56:59], v[144:147]
v_exp_f32 v161, v161
v_mfma_f32_16x16x32_bf16 v[144:147], a[132:135], a[60:63], v[144:147]
ds_read_b128 a[148:151], v236 offset:33840
v_fma_f32 v163, v163, s56, v247
v_mfma_f32_16x16x32_bf16 v[140:143], a[132:135], a[28:31], v[140:143]
v_exp_f32 v162, v162
v_mfma_f32_16x16x32_bf16 v[140:143], a[128:131], a[24:27], v[140:143]
s_waitcnt lgkmcnt(2)
v_exp_f32 v163, v163
v_mfma_f32_16x16x32_bf16 v[204:207], a[136:139], a[0:3], 0
v_cvt_pk_bf16_f32 v52, v160, v161
v_cvt_pk_bf16_f32 v53, v162, v163
v_mfma_f32_16x16x32_bf16 v[204:207], a[140:143], a[4:7], v[204:207]
s_add_u32 m0, s60, 141344
ds_read_b128 a[128:131], v236 offset:33856
v_fma_f32 v164, v164, s56, v248
v_mfma_f32_16x16x32_bf16 v[208:211], a[140:143], a[36:39], 0
buffer_load_dwordx4 v241, s[36:39], s89 offen offset:2048  lds
v_mfma_f32_16x16x32_bf16 v[208:211], a[136:139], a[32:35], v[208:211]
v_exp_f32 v164, v164
v_mfma_f32_16x16x32_bf16 v[212:215], a[136:139], a[64:67], 0
v_fma_f32 v165, v165, s56, v248
v_fma_f32 v166, v166, s56, v248
v_mfma_f32_16x16x32_bf16 v[212:215], a[140:143], a[68:71], v[212:215]
ds_read_b128 a[132:135], v236 offset:33872
v_fma_f32 v167, v167, s56, v248
v_mfma_f32_16x16x32_bf16 v[216:219], a[140:143], a[100:103], 0
v_exp_f32 v165, v165
v_mfma_f32_16x16x32_bf16 v[216:219], a[136:139], a[96:99], v[216:219]
s_waitcnt lgkmcnt(2)
v_exp_f32 v166, v166
v_mfma_f32_16x16x32_bf16 v[216:219], a[144:147], a[104:107], v[216:219]
v_exp_f32 v167, v167
v_mfma_f32_16x16x32_bf16 v[216:219], a[148:151], a[108:111], v[216:219]
s_add_u32 m0, s60, 141360
ds_read_b128 a[136:139], v236 offset:33888
v_cvt_pk_bf16_f32 v56, v164, v165
v_mfma_f32_16x16x32_bf16 v[212:215], a[148:151], a[76:79], v[212:215]
buffer_load_dwordx4 v241, s[36:39], s89 offen offset:3072  lds
v_mfma_f32_16x16x32_bf16 v[212:215], a[144:147], a[72:75], v[212:215]
v_cvt_pk_bf16_f32 v57, v166, v167
v_fma_f32 v168, v168, s56, v249
v_mfma_f32_16x16x32_bf16 v[208:211], a[144:147], a[40:43], v[208:211]
v_exp_f32 v168, v168
v_mfma_f32_16x16x32_bf16 v[208:211], a[148:151], a[44:47], v[208:211]
ds_read_b128 a[140:143], v236 offset:33904
v_fma_f32 v169, v169, s56, v249
v_mfma_f32_16x16x32_bf16 v[204:207], a[148:151], a[12:15], v[204:207]
v_exp_f32 v169, v169
v_mfma_f32_16x16x32_bf16 v[204:207], a[144:147], a[8:11], v[204:207]
s_waitcnt lgkmcnt(2)
v_fma_f32 v170, v170, s56, v249
v_fma_f32 v171, v171, s56, v249
v_mfma_f32_16x16x32_bf16 v[204:207], a[128:131], a[16:19], v[204:207]
v_exp_f32 v170, v170
v_mfma_f32_16x16x32_bf16 v[204:207], a[132:135], a[20:23], v[204:207]
s_add_u32 m0, s60, 73728
ds_read_b128 a[144:147], v236 offset:34304
v_cvt_pk_bf16_f32 v168, v168, v169
v_mfma_f32_16x16x32_bf16 v[208:211], a[132:135], a[52:55], v[208:211]
buffer_load_dwordx4 v241, s[36:39], s90 offen offset:0  lds
v_mfma_f32_16x16x32_bf16 v[208:211], a[128:131], a[48:51], v[208:211]
v_exp_f32 v171, v171
v_mfma_f32_16x16x32_bf16 v[212:215], a[128:131], a[80:83], v[212:215]
v_accvgpr_write_b32 a236, v168
v_cvt_pk_bf16_f32 v170, v170, v171
v_mfma_f32_16x16x32_bf16 v[212:215], a[132:135], a[84:87], v[212:215]
ds_read_b128 a[148:151], v236 offset:34320
v_accvgpr_write_b32 a237, v170
v_mfma_f32_16x16x32_bf16 v[216:219], a[132:135], a[116:119], v[216:219]
v_fma_f32 v172, v172, s56, v246
v_fma_f32 v173, v173, s56, v246
v_mfma_f32_16x16x32_bf16 v[216:219], a[128:131], a[112:115], v[216:219]
s_waitcnt lgkmcnt(2)
v_exp_f32 v172, v172
v_mfma_f32_16x16x32_bf16 v[216:219], a[136:139], a[120:123], v[216:219]
v_exp_f32 v173, v173
v_mfma_f32_16x16x32_bf16 v[216:219], a[140:143], a[124:127], v[216:219]
s_add_u32 m0, s62, 0
ds_read_b128 a[128:131], v236 offset:34336
v_fma_f32 v174, v174, s56, v246
v_mfma_f32_16x16x32_bf16 v[212:215], a[140:143], a[92:95], v[212:215]
buffer_load_dwordx4 v254, s[20:23], s73 offen offset:0  lds
v_mfma_f32_16x16x32_bf16 v[212:215], a[136:139], a[88:91], v[212:215]
v_exp_f32 v174, v174
v_mfma_f32_16x16x32_bf16 v[208:211], a[136:139], a[56:59], v[208:211]
v_fma_f32 v175, v175, s56, v246
v_cvt_pk_bf16_f32 v50, v172, v173
v_mfma_f32_16x16x32_bf16 v[208:211], a[140:143], a[60:63], v[208:211]
ds_read_b128 a[132:135], v236 offset:34352
v_fma_f32 v176, v176, s56, v247
v_mfma_f32_16x16x32_bf16 v[204:207], a[140:143], a[28:31], v[204:207]
v_exp_f32 v175, v175
v_mfma_f32_16x16x32_bf16 v[204:207], a[136:139], a[24:27], v[204:207]
s_waitcnt lgkmcnt(2)
v_exp_f32 v176, v176
v_mfma_f32_16x16x32_bf16 v[220:223], a[144:147], a[0:3], 0
v_cvt_pk_bf16_f32 v51, v174, v175
v_fma_f32 v177, v177, s56, v247
v_mfma_f32_16x16x32_bf16 v[220:223], a[148:151], a[4:7], v[220:223]
ds_read_b128 a[136:139], v236 offset:34368
v_fma_f32 v178, v178, s56, v247
v_mfma_f32_16x16x32_bf16 v[224:227], a[148:151], a[36:39], 0
v_exp_f32 v177, v177
v_mfma_f32_16x16x32_bf16 v[224:227], a[144:147], a[32:35], v[224:227]
v_exp_f32 v178, v178
v_mfma_f32_16x16x32_bf16 v[228:231], a[144:147], a[64:67], 0
v_fma_f32 v179, v179, s56, v247
v_cvt_pk_bf16_f32 v54, v176, v177
v_mfma_f32_16x16x32_bf16 v[228:231], a[148:151], a[68:71], v[228:231]
ds_read_b128 a[140:143], v236 offset:34384
v_fma_f32 v180, v180, s56, v248
v_mfma_f32_16x16x32_bf16 v[232:235], a[148:151], a[100:103], 0
s_mov_b32 m0, 158208
v_exp_f32 v179, v179
v_mfma_f32_16x16x32_bf16 v[232:235], a[144:147], a[96:99], v[232:235]
s_waitcnt lgkmcnt(2)
v_exp_f32 v180, v180
v_mfma_f32_16x16x32_bf16 v[232:235], a[128:131], a[104:107], v[232:235]
buffer_load_dwordx4 v254, s[24:27], s76 offen offset:0  lds
v_mfma_f32_16x16x32_bf16 v[232:235], a[132:135], a[108:111], v[232:235]
ds_read_b128 a[144:147], v236 offset:34400
v_cvt_pk_bf16_f32 v55, v178, v179
v_mfma_f32_16x16x32_bf16 v[228:231], a[132:135], a[76:79], v[228:231]
v_fma_f32 v181, v181, s56, v248
v_fma_f32 v182, v182, s56, v248
v_mfma_f32_16x16x32_bf16 v[228:231], a[128:131], a[72:75], v[228:231]
v_exp_f32 v181, v181
v_mfma_f32_16x16x32_bf16 v[224:227], a[128:131], a[40:43], v[224:227]
v_exp_f32 v182, v182
v_mfma_f32_16x16x32_bf16 v[224:227], a[132:135], a[44:47], v[224:227]
ds_read_b128 a[148:151], v236 offset:34416
v_fma_f32 v183, v183, s56, v248
v_mfma_f32_16x16x32_bf16 v[220:223], a[132:135], a[12:15], v[220:223]
)"
R"(
s_waitcnt vmcnt(9) lgkmcnt(2)
v_exp_f32 v183, v183
v_mfma_f32_16x16x32_bf16 v[220:223], a[128:131], a[8:11], v[220:223]
s_barrier
v_mfma_f32_16x16x32_bf16 v[220:223], a[136:139], a[16:19], v[220:223]
v_cvt_pk_bf16_f32 v58, v180, v181
v_cvt_pk_bf16_f32 v59, v182, v183
v_mfma_f32_16x16x32_bf16 v[220:223], a[140:143], a[20:23], v[220:223]
ds_read_b128 a[128:131], v236 offset:50688
v_fma_f32 v184, v184, s56, v249
v_mfma_f32_16x16x32_bf16 v[224:227], a[140:143], a[52:55], v[224:227]
v_exp_f32 v184, v184
v_mfma_f32_16x16x32_bf16 v[224:227], a[136:139], a[48:51], v[224:227]
v_fma_f32 v185, v185, s56, v249
v_fma_f32 v186, v186, s56, v249
v_mfma_f32_16x16x32_bf16 v[228:231], a[136:139], a[80:83], v[228:231]
v_exp_f32 v185, v185
v_mfma_f32_16x16x32_bf16 v[228:231], a[140:143], a[84:87], v[228:231]
ds_read_b128 a[132:135], v236 offset:50704
v_fma_f32 v187, v187, s56, v249
v_mfma_f32_16x16x32_bf16 v[232:235], a[140:143], a[116:119], v[232:235]
v_exp_f32 v186, v186
v_mfma_f32_16x16x32_bf16 v[232:235], a[136:139], a[112:115], v[232:235]
s_waitcnt lgkmcnt(2)
v_exp_f32 v187, v187
v_mfma_f32_16x16x32_bf16 v[232:235], a[144:147], a[120:123], v[232:235]
v_cvt_pk_bf16_f32 v184, v184, v185
v_accvgpr_write_b32 a238, v184
v_mfma_f32_16x16x32_bf16 v[232:235], a[148:151], a[124:127], v[232:235]
s_add_u32 m0, s60, 73744
ds_read_b128 a[136:139], v236 offset:50720
v_cvt_pk_bf16_f32 v186, v186, v187
v_mfma_f32_16x16x32_bf16 v[228:231], a[148:151], a[92:95], v[228:231]
buffer_load_dwordx4 v241, s[36:39], s90 offen offset:1024  lds
v_mfma_f32_16x16x32_bf16 v[228:231], a[144:147], a[88:91], v[228:231]
v_accvgpr_write_b32 a239, v186
v_fma_f32 v188, v188, s56, v246
v_mfma_f32_16x16x32_bf16 v[224:227], a[144:147], a[56:59], v[224:227]
v_exp_f32 v188, v188
v_mfma_f32_16x16x32_bf16 v[224:227], a[148:151], a[60:63], v[224:227]
ds_read_b128 a[140:143], v236 offset:50736
v_fma_f32 v189, v189, s56, v246
v_mfma_f32_16x16x32_bf16 v[220:223], a[148:151], a[28:31], v[220:223]
v_exp_f32 v189, v189
v_mfma_f32_16x16x32_bf16 v[220:223], a[144:147], a[24:27], v[220:223]
s_waitcnt lgkmcnt(2)
v_fma_f32 v190, v190, s56, v246
v_fma_f32 v191, v191, s56, v246
v_mfma_f32_16x16x32_bf16 v[156:159], a[128:131], a[0:3], 0
v_exp_f32 v190, v190
v_mfma_f32_16x16x32_bf16 v[156:159], a[132:135], a[4:7], v[156:159]
s_add_u32 m0, s60, 73760
ds_read_b128 a[144:147], v236 offset:50752
v_cvt_pk_bf16_f32 v60, v188, v189
v_mfma_f32_16x16x32_bf16 v[160:163], a[132:135], a[36:39], 0
.Lpad3_%=:
s_cbranch_vccnz .Lret0_%=
buffer_load_dwordx4 v241, s[36:39], s90 offen offset:2048  lds
v_mfma_f32_16x16x32_bf16 v[160:163], a[128:131], a[32:35], v[160:163]
v_exp_f32 v191, v191
v_mfma_f32_16x16x32_bf16 v[164:167], a[128:131], a[64:67], 0
v_cvt_pk_bf16_f32 v61, v190, v191
v_fma_f32 v192, v192, s56, v247
v_mfma_f32_16x16x32_bf16 v[164:167], a[132:135], a[68:71], v[164:167]
ds_read_b128 a[148:151], v236 offset:50768
v_fma_f32 v193, v193, s56, v247
v_mfma_f32_16x16x32_bf16 v[168:171], a[132:135], a[100:103], 0
v_exp_f32 v192, v192
v_mfma_f32_16x16x32_bf16 v[168:171], a[128:131], a[96:99], v[168:171]
s_waitcnt lgkmcnt(2)
v_exp_f32 v193, v193
v_mfma_f32_16x16x32_bf16 v[168:171], a[136:139], a[104:107], v[168:171]
v_fma_f32 v194, v194, s56, v247
v_fma_f32 v195, v195, s56, v247
v_mfma_f32_16x16x32_bf16 v[168:171], a[140:143], a[108:111], v[168:171]
s_add_u32 m0, s60, 73776
ds_read_b128 a[128:131], v236 offset:50784
v_cvt_pk_bf16_f32 v64, v192, v193
v_mfma_f32_16x16x32_bf16 v[164:167], a[140:143], a[76:79], v[164:167]
buffer_load_dwordx4 v241, s[36:39], s90 offen offset:3072  lds
v_mfma_f32_16x16x32_bf16 v[164:167], a[136:139], a[72:75], v[164:167]
v_exp_f32 v194, v194
v_mfma_f32_16x16x32_bf16 v[160:163], a[136:139], a[40:43], v[160:163]
v_exp_f32 v195, v195
v_mfma_f32_16x16x32_bf16 v[160:163], a[140:143], a[44:47], v[160:163]
ds_read_b128 a[132:135], v236 offset:50800
v_cvt_pk_bf16_f32 v65, v194, v195
v_mfma_f32_16x16x32_bf16 v[156:159], a[140:143], a[12:15], v[156:159]
v_fma_f32 v196, v196, s56, v248
v_fma_f32 v197, v197, s56, v248
v_mfma_f32_16x16x32_bf16 v[156:159], a[136:139], a[8:11], v[156:159]
s_waitcnt lgkmcnt(2)
v_exp_f32 v196, v196
v_mfma_f32_16x16x32_bf16 v[156:159], a[144:147], a[16:19], v[156:159]
v_exp_f32 v197, v197
v_mfma_f32_16x16x32_bf16 v[156:159], a[148:151], a[20:23], v[156:159]
s_add_u32 m0, s62, 32
ds_read_b128 a[136:139], v236 offset:51200
v_fma_f32 v198, v198, s56, v248
v_mfma_f32_16x16x32_bf16 v[160:163], a[148:151], a[52:55], v[160:163]
buffer_load_dwordx4 v254, s[20:23], s73 offen offset:1024  lds
v_mfma_f32_16x16x32_bf16 v[160:163], a[144:147], a[48:51], v[160:163]
v_exp_f32 v198, v198
v_mfma_f32_16x16x32_bf16 v[164:167], a[144:147], a[80:83], v[164:167]
v_fma_f32 v199, v199, s56, v248
v_cvt_pk_bf16_f32 v68, v196, v197
v_mfma_f32_16x16x32_bf16 v[164:167], a[148:151], a[84:87], v[164:167]
ds_read_b128 a[140:143], v236 offset:51216
v_fma_f32 v200, v200, s56, v249
v_mfma_f32_16x16x32_bf16 v[168:171], a[148:151], a[116:119], v[168:171]
v_exp_f32 v199, v199
v_mfma_f32_16x16x32_bf16 v[168:171], a[144:147], a[112:115], v[168:171]
s_waitcnt lgkmcnt(2)
v_exp_f32 v200, v200
v_mfma_f32_16x16x32_bf16 v[168:171], a[128:131], a[120:123], v[168:171]
v_cvt_pk_bf16_f32 v69, v198, v199
v_fma_f32 v201, v201, s56, v249
v_mfma_f32_16x16x32_bf16 v[168:171], a[132:135], a[124:127], v[168:171]
ds_read_b128 a[144:147], v236 offset:51232
v_fma_f32 v202, v202, s56, v249
v_mfma_f32_16x16x32_bf16 v[164:167], a[132:135], a[92:95], v[164:167]
v_exp_f32 v201, v201
v_mfma_f32_16x16x32_bf16 v[164:167], a[128:131], a[88:91], v[164:167]
v_exp_f32 v202, v202
v_mfma_f32_16x16x32_bf16 v[160:163], a[128:131], a[56:59], v[160:163]
v_fma_f32 v203, v203, s56, v249
v_cvt_pk_bf16_f32 v72, v200, v201
v_mfma_f32_16x16x32_bf16 v[160:163], a[132:135], a[60:63], v[160:163]
ds_read_b128 a[148:151], v236 offset:51248
v_fma_f32 v140, v140, s56, v246
v_mfma_f32_16x16x32_bf16 v[156:159], a[132:135], a[28:31], v[156:159]
v_exp_f32 v203, v203
v_mfma_f32_16x16x32_bf16 v[156:159], a[128:131], a[24:27], v[156:159]
s_waitcnt lgkmcnt(2)
v_exp_f32 v140, v140
v_mfma_f32_16x16x32_bf16 v[172:175], a[136:139], a[0:3], 0
v_cvt_pk_bf16_f32 v73, v202, v203
v_fma_f32 v141, v141, s56, v246
v_mfma_f32_16x16x32_bf16 v[172:175], a[140:143], a[4:7], v[172:175]
ds_read_b128 a[128:131], v236 offset:51264
v_fma_f32 v142, v142, s56, v246
v_mfma_f32_16x16x32_bf16 v[176:179], a[140:143], a[36:39], 0
v_exp_f32 v141, v141
v_mfma_f32_16x16x32_bf16 v[176:179], a[136:139], a[32:35], v[176:179]
v_exp_f32 v142, v142
v_mfma_f32_16x16x32_bf16 v[180:183], a[136:139], a[64:67], 0
v_fma_f32 v143, v143, s56, v246
v_cvt_pk_bf16_f32 v62, v140, v141
v_mfma_f32_16x16x32_bf16 v[180:183], a[140:143], a[68:71], v[180:183]
ds_read_b128 a[132:135], v236 offset:51280
v_fma_f32 v144, v144, s56, v247
v_mfma_f32_16x16x32_bf16 v[184:187], a[140:143], a[100:103], 0
v_exp_f32 v143, v143
v_mfma_f32_16x16x32_bf16 v[184:187], a[136:139], a[96:99], v[184:187]
s_waitcnt lgkmcnt(2)
v_exp_f32 v144, v144
v_mfma_f32_16x16x32_bf16 v[184:187], a[144:147], a[104:107], v[184:187]
v_cvt_pk_bf16_f32 v63, v142, v143
v_fma_f32 v145, v145, s56, v247
v_mfma_f32_16x16x32_bf16 v[184:187], a[148:151], a[108:111], v[184:187]
s_add_u32 m0, s62, 64
ds_read_b128 a[136:139], v236 offset:51296
v_fma_f32 v146, v146, s56, v247
v_mfma_f32_16x16x32_bf16 v[180:183], a[148:151], a[76:79], v[180:183]
buffer_load_dwordx4 v254, s[20:23], s73 offen offset:2048  lds
v_mfma_f32_16x16x32_bf16 v[180:183], a[144:147], a[72:75], v[180:183]
v_exp_f32 v145, v145
v_mfma_f32_16x16x32_bf16 v[176:179], a[144:147], a[40:43], v[176:179]
v_exp_f32 v146, v146
v_mfma_f32_16x16x32_bf16 v[176:179], a[148:151], a[44:47], v[176:179]
ds_read_b128 a[140:143], v236 offset:51312
v_fma_f32 v147, v147, s56, v247
v_mfma_f32_16x16x32_bf16 v[172:175], a[148:151], a[12:15], v[172:175]
s_waitcnt vmcnt(8) lgkmcnt(2)
v_exp_f32 v147, v147
v_mfma_f32_16x16x32_bf16 v[172:175], a[144:147], a[8:11], v[172:175]
s_barrier
v_mfma_f32_16x16x32_bf16 v[172:175], a[128:131], a[16:19], v[172:175]
v_cvt_pk_bf16_f32 v66, v144, v145
v_cvt_pk_bf16_f32 v67, v146, v147
v_mfma_f32_16x16x32_bf16 v[172:175], a[132:135], a[20:23], v[172:175]
s_add_u32 m0, s60, 107520
ds_read_b128 a[144:147], v237 offset:0
v_fma_f32 v148, v148, s56, v248
v_mfma_f32_16x16x32_bf16 v[176:179], a[132:135], a[52:55], v[176:179]
buffer_load_dwordx4 v241, s[36:39], s91 offen offset:0  lds
v_mfma_f32_16x16x32_bf16 v[176:179], a[128:131], a[48:51], v[176:179]
v_exp_f32 v148, v148
v_mfma_f32_16x16x32_bf16 v[180:183], a[128:131], a[80:83], v[180:183]
v_fma_f32 v149, v149, s56, v248
v_fma_f32 v150, v150, s56, v248
v_mfma_f32_16x16x32_bf16 v[180:183], a[132:135], a[84:87], v[180:183]
ds_read_b128 a[148:151], v237 offset:16
v_fma_f32 v151, v151, s56, v248
v_mfma_f32_16x16x32_bf16 v[184:187], a[132:135], a[116:119], v[184:187]
v_exp_f32 v149, v149
v_mfma_f32_16x16x32_bf16 v[184:187], a[128:131], a[112:115], v[184:187]
s_waitcnt lgkmcnt(2)
v_exp_f32 v150, v150
v_mfma_f32_16x16x32_bf16 v[184:187], a[136:139], a[120:123], v[184:187]
v_exp_f32 v151, v151
v_mfma_f32_16x16x32_bf16 v[184:187], a[140:143], a[124:127], v[184:187]
s_add_u32 m0, s60, 107536
ds_read_b128 a[128:131], v237 offset:32
v_cvt_pk_bf16_f32 v70, v148, v149
v_mfma_f32_16x16x32_bf16 v[180:183], a[140:143], a[92:95], v[180:183]
buffer_load_dwordx4 v241, s[36:39], s91 offen offset:1024  lds
v_mfma_f32_16x16x32_bf16 v[180:183], a[136:139], a[88:91], v[180:183]
v_cvt_pk_bf16_f32 v71, v150, v151
v_fma_f32 v152, v152, s56, v249
v_mfma_f32_16x16x32_bf16 v[176:179], a[136:139], a[56:59], v[176:179]
v_exp_f32 v152, v152
v_mfma_f32_16x16x32_bf16 v[176:179], a[140:143], a[60:63], v[176:179]
ds_read_b128 a[132:135], v237 offset:48
v_fma_f32 v153, v153, s56, v249
v_mfma_f32_16x16x32_bf16 v[172:175], a[140:143], a[28:31], v[172:175]
v_exp_f32 v153, v153
v_mfma_f32_16x16x32_bf16 v[172:175], a[136:139], a[24:27], v[172:175]
s_waitcnt lgkmcnt(2)
v_fma_f32 v154, v154, s56, v249
v_fma_f32 v155, v155, s56, v249
v_mfma_f32_16x16x32_bf16 v[188:191], a[144:147], a[0:3], 0
v_exp_f32 v154, v154
v_mfma_f32_16x16x32_bf16 v[188:191], a[148:151], a[4:7], v[188:191]
s_add_u32 m0, s60, 107552
ds_read_b128 a[136:139], v237 offset:64
v_cvt_pk_bf16_f32 v74, v152, v153
v_mfma_f32_16x16x32_bf16 v[192:195], a[148:151], a[36:39], 0
buffer_load_dwordx4 v241, s[36:39], s91 offen offset:2048  lds
v_mfma_f32_16x16x32_bf16 v[192:195], a[144:147], a[32:35], v[192:195]
v_exp_f32 v155, v155
v_mfma_f32_16x16x32_bf16 v[196:199], a[144:147], a[64:67], 0
v_cvt_pk_bf16_f32 v75, v154, v155
v_fma_f32 v204, v204, s56, v246
v_mfma_f32_16x16x32_bf16 v[196:199], a[148:151], a[68:71], v[196:199]
ds_read_b128 a[140:143], v237 offset:80
v_fma_f32 v205, v205, s56, v246
v_mfma_f32_16x16x32_bf16 v[200:203], a[148:151], a[100:103], 0
v_exp_f32 v204, v204
v_mfma_f32_16x16x32_bf16 v[200:203], a[144:147], a[96:99], v[200:203]
s_waitcnt lgkmcnt(2)
v_exp_f32 v205, v205
v_mfma_f32_16x16x32_bf16 v[200:203], a[128:131], a[104:107], v[200:203]
v_fma_f32 v206, v206, s56, v246
v_fma_f32 v207, v207, s56, v246
v_mfma_f32_16x16x32_bf16 v[200:203], a[132:135], a[108:111], v[200:203]
s_add_u32 m0, s60, 107568
ds_read_b128 a[144:147], v237 offset:96
v_cvt_pk_bf16_f32 v204, v204, v205
v_mfma_f32_16x16x32_bf16 v[196:199], a[132:135], a[76:79], v[196:199]
buffer_load_dwordx4 v241, s[36:39], s91 offen offset:3072  lds
v_mfma_f32_16x16x32_bf16 v[196:199], a[128:131], a[72:75], v[196:199]
v_exp_f32 v206, v206
v_mfma_f32_16x16x32_bf16 v[192:195], a[128:131], a[40:43], v[192:195]
v_exp_f32 v207, v207
v_mfma_f32_16x16x32_bf16 v[192:195], a[132:135], a[44:47], v[192:195]
ds_read_b128 a[148:151], v237 offset:112
v_accvgpr_write_b32 a240, v204
v_mfma_f32_16x16x32_bf16 v[188:191], a[132:135], a[12:15], v[188:191]
v_cvt_pk_bf16_f32 v206, v206, v207
v_accvgpr_write_b32 a241, v206
v_mfma_f32_16x16x32_bf16 v[188:191], a[128:131], a[8:11], v[188:191]
s_waitcnt lgkmcnt(2)
v_fma_f32 v208, v208, s56, v247
v_fma_f32 v209, v209, s56, v247
v_mfma_f32_16x16x32_bf16 v[188:191], a[136:139], a[16:19], v[188:191]
)"
R"(
v_exp_f32 v208, v208
v_mfma_f32_16x16x32_bf16 v[188:191], a[140:143], a[20:23], v[188:191]
ds_read_b128 a[128:131], v237 offset:512
v_fma_f32 v210, v210, s56, v247
v_mfma_f32_16x16x32_bf16 v[192:195], a[140:143], a[52:55], v[192:195]
s_add_u32 m0, s62, 96
v_exp_f32 v209, v209
v_mfma_f32_16x16x32_bf16 v[192:195], a[136:139], a[48:51], v[192:195]
buffer_load_dwordx4 v254, s[20:23], s73 offen offset:3072  lds
v_mfma_f32_16x16x32_bf16 v[196:199], a[136:139], a[80:83], v[196:199]
v_exp_f32 v210, v210
v_mfma_f32_16x16x32_bf16 v[196:199], a[140:143], a[84:87], v[196:199]
ds_read_b128 a[132:135], v237 offset:528
v_fma_f32 v211, v211, s56, v247
v_mfma_f32_16x16x32_bf16 v[200:203], a[140:143], a[116:119], v[200:203]
v_exp_f32 v211, v211
v_mfma_f32_16x16x32_bf16 v[200:203], a[136:139], a[112:115], v[200:203]
s_waitcnt lgkmcnt(2)
v_cvt_pk_bf16_f32 v76, v208, v209
v_cvt_pk_bf16_f32 v77, v210, v211
v_mfma_f32_16x16x32_bf16 v[200:203], a[144:147], a[120:123], v[200:203]
v_fma_f32 v212, v212, s56, v248
v_fma_f32 v213, v213, s56, v248
v_mfma_f32_16x16x32_bf16 v[200:203], a[148:151], a[124:127], v[200:203]
ds_read_b128 a[136:139], v237 offset:544
v_fma_f32 v214, v214, s56, v248
v_mfma_f32_16x16x32_bf16 v[196:199], a[148:151], a[92:95], v[196:199]
v_exp_f32 v212, v212
v_mfma_f32_16x16x32_bf16 v[196:199], a[144:147], a[88:91], v[196:199]
v_exp_f32 v213, v213
v_mfma_f32_16x16x32_bf16 v[192:195], a[144:147], a[56:59], v[192:195]
v_exp_f32 v214, v214
v_fma_f32 v215, v215, s56, v248
v_mfma_f32_16x16x32_bf16 v[192:195], a[148:151], a[60:63], v[192:195]
ds_read_b128 a[140:143], v237 offset:560
v_cvt_pk_bf16_f32 v80, v212, v213
v_fma_f32 v216, v216, s56, v249
v_mfma_f32_16x16x32_bf16 v[188:191], a[148:151], a[28:31], v[188:191]
v_exp_f32 v215, v215
v_mfma_f32_16x16x32_bf16 v[188:191], a[144:147], a[24:27], v[188:191]
s_waitcnt lgkmcnt(2)
v_exp_f32 v216, v216
v_mfma_f32_16x16x32_bf16 v[140:143], a[128:131], a[0:3], 0
v_cvt_pk_bf16_f32 v81, v214, v215
v_fma_f32 v217, v217, s56, v249
v_mfma_f32_16x16x32_bf16 v[140:143], a[132:135], a[4:7], v[140:143]
ds_read_b128 a[144:147], v237 offset:576
v_fma_f32 v218, v218, s56, v249
v_mfma_f32_16x16x32_bf16 v[144:147], a[132:135], a[36:39], 0
v_exp_f32 v217, v217
v_mfma_f32_16x16x32_bf16 v[144:147], a[128:131], a[32:35], v[144:147]
v_exp_f32 v218, v218
v_mfma_f32_16x16x32_bf16 v[148:151], a[128:131], a[64:67], 0
v_fma_f32 v219, v219, s56, v249
v_cvt_pk_bf16_f32 v84, v216, v217
v_mfma_f32_16x16x32_bf16 v[148:151], a[132:135], a[68:71], v[148:151]
ds_read_b128 a[148:151], v237 offset:592
v_fma_f32 v220, v220, s56, v246
v_mfma_f32_16x16x32_bf16 v[152:155], a[132:135], a[100:103], 0
v_exp_f32 v219, v219
v_mfma_f32_16x16x32_bf16 v[152:155], a[128:131], a[96:99], v[152:155]
s_waitcnt lgkmcnt(2)
v_exp_f32 v220, v220
v_mfma_f32_16x16x32_bf16 v[152:155], a[136:139], a[104:107], v[152:155]
v_cvt_pk_bf16_f32 v85, v218, v219
v_fma_f32 v221, v221, s56, v246
v_mfma_f32_16x16x32_bf16 v[152:155], a[140:143], a[108:111], v[152:155]
ds_read_b128 a[128:131], v237 offset:608
v_fma_f32 v222, v222, s56, v246
v_mfma_f32_16x16x32_bf16 v[148:151], a[140:143], a[76:79], v[148:151]
v_exp_f32 v221, v221
v_mfma_f32_16x16x32_bf16 v[148:151], a[136:139], a[72:75], v[148:151]
v_exp_f32 v222, v222
v_mfma_f32_16x16x32_bf16 v[144:147], a[136:139], a[40:43], v[144:147]
v_fma_f32 v223, v223, s56, v246
v_cvt_pk_bf16_f32 v220, v220, v221
v_mfma_f32_16x16x32_bf16 v[144:147], a[140:143], a[44:47], v[144:147]
s_add_u32 m0, s60, 124416
ds_read_b128 a[132:135], v237 offset:624
v_accvgpr_write_b32 a242, v220
v_mfma_f32_16x16x32_bf16 v[140:143], a[140:143], a[12:15], v[140:143]
s_waitcnt vmcnt(7) lgkmcnt(2)
v_exp_f32 v223, v223
v_mfma_f32_16x16x32_bf16 v[140:143], a[136:139], a[8:11], v[140:143]
s_barrier
v_mfma_f32_16x16x32_bf16 v[140:143], a[144:147], a[16:19], v[140:143]
buffer_load_dwordx4 v241, s[36:39], s92 offen offset:0  lds
v_mfma_f32_16x16x32_bf16 v[140:143], a[148:151], a[20:23], v[140:143]
ds_read_b128 a[136:139], v236 offset:0
v_cvt_pk_bf16_f32 v222, v222, v223
v_mfma_f32_16x16x32_bf16 v[144:147], a[148:151], a[52:55], v[144:147]
v_accvgpr_write_b32 a243, v222
v_fma_f32 v224, v224, s56, v247
v_mfma_f32_16x16x32_bf16 v[144:147], a[144:147], a[48:51], v[144:147]
v_exp_f32 v224, v224
v_mfma_f32_16x16x32_bf16 v[148:151], a[144:147], a[80:83], v[148:151]
v_fma_f32 v225, v225, s56, v247
v_fma_f32 v226, v226, s56, v247
v_mfma_f32_16x16x32_bf16 v[148:151], a[148:151], a[84:87], v[148:151]
ds_read_b128 a[140:143], v236 offset:16
v_fma_f32 v227, v227, s56, v247
v_mfma_f32_16x16x32_bf16 v[152:155], a[148:151], a[116:119], v[152:155]
s_add_u32 m0, s60, 124432
v_exp_f32 v225, v225
v_mfma_f32_16x16x32_bf16 v[152:155], a[144:147], a[112:115], v[152:155]
s_waitcnt lgkmcnt(2)
v_exp_f32 v226, v226
v_mfma_f32_16x16x32_bf16 v[152:155], a[128:131], a[120:123], v[152:155]
buffer_load_dwordx4 v241, s[36:39], s92 offen offset:1024  lds
v_mfma_f32_16x16x32_bf16 v[152:155], a[132:135], a[124:127], v[152:155]
ds_read_b128 a[144:147], v236 offset:32
v_cvt_pk_bf16_f32 v78, v224, v225
v_mfma_f32_16x16x32_bf16 v[148:151], a[132:135], a[92:95], v[148:151]
v_exp_f32 v227, v227
v_mfma_f32_16x16x32_bf16 v[148:151], a[128:131], a[88:91], v[148:151]
v_cvt_pk_bf16_f32 v79, v226, v227
v_fma_f32 v228, v228, s56, v248
v_mfma_f32_16x16x32_bf16 v[144:147], a[128:131], a[56:59], v[144:147]
v_exp_f32 v228, v228
v_mfma_f32_16x16x32_bf16 v[144:147], a[132:135], a[60:63], v[144:147]
ds_read_b128 a[148:151], v236 offset:48
v_fma_f32 v229, v229, s56, v248
v_mfma_f32_16x16x32_bf16 v[140:143], a[132:135], a[28:31], v[140:143]
s_add_u32 m0, s62, 4224
v_exp_f32 v229, v229
v_mfma_f32_16x16x32_bf16 v[140:143], a[128:131], a[24:27], v[140:143]
s_waitcnt lgkmcnt(2)
v_fma_f32 v230, v230, s56, v248
v_fma_f32 v231, v231, s56, v248
v_mfma_f32_16x16x32_bf16 v[204:207], a[136:139], a[0:3], 0
buffer_load_dwordx4 v254, s[20:23], s74 offen offset:0  lds
v_mfma_f32_16x16x32_bf16 v[204:207], a[140:143], a[4:7], v[204:207]
ds_read_b128 a[128:131], v236 offset:64
v_cvt_pk_bf16_f32 v82, v228, v229
v_mfma_f32_16x16x32_bf16 v[208:211], a[140:143], a[36:39], 0
v_exp_f32 v230, v230
v_mfma_f32_16x16x32_bf16 v[208:211], a[136:139], a[32:35], v[208:211]
v_exp_f32 v231, v231
v_mfma_f32_16x16x32_bf16 v[212:215], a[136:139], a[64:67], 0
v_cvt_pk_bf16_f32 v83, v230, v231
v_fma_f32 v232, v232, s56, v249
v_mfma_f32_16x16x32_bf16 v[212:215], a[140:143], a[68:71], v[212:215]
ds_read_b128 a[132:135], v236 offset:80
v_fma_f32 v233, v233, s56, v249
v_mfma_f32_16x16x32_bf16 v[216:219], a[140:143], a[100:103], 0
s_add_u32 m0, s60, 124448
v_exp_f32 v232, v232
v_mfma_f32_16x16x32_bf16 v[216:219], a[136:139], a[96:99], v[216:219]
s_waitcnt lgkmcnt(2)
v_exp_f32 v233, v233
v_mfma_f32_16x16x32_bf16 v[216:219], a[144:147], a[104:107], v[216:219]
buffer_load_dwordx4 v241, s[36:39], s92 offen offset:2048  lds
v_mfma_f32_16x16x32_bf16 v[216:219], a[148:151], a[108:111], v[216:219]
ds_read_b128 a[136:139], v236 offset:96
v_fma_f32 v234, v234, s56, v249
v_mfma_f32_16x16x32_bf16 v[212:215], a[148:151], a[76:79], v[212:215]
v_exp_f32 v234, v234
v_mfma_f32_16x16x32_bf16 v[212:215], a[144:147], a[72:75], v[212:215]
v_fma_f32 v235, v235, s56, v249
v_cvt_pk_bf16_f32 v86, v232, v233
v_mfma_f32_16x16x32_bf16 v[208:211], a[144:147], a[40:43], v[208:211]
v_exp_f32 v235, v235
v_mfma_f32_16x16x32_bf16 v[208:211], a[148:151], a[44:47], v[208:211]
ds_read_b128 a[140:143], v236 offset:112
v_cvt_pk_bf16_f32 v87, v234, v235
v_mfma_f32_16x16x32_bf16 v[204:207], a[148:151], a[12:15], v[204:207]
s_add_u32 m0, s60, 124464
v_fma_f32 v156, v156, s56, v246
v_fma_f32 v157, v157, s56, v246
v_mfma_f32_16x16x32_bf16 v[204:207], a[144:147], a[8:11], v[204:207]
s_waitcnt lgkmcnt(2)
v_exp_f32 v156, v156
v_mfma_f32_16x16x32_bf16 v[204:207], a[128:131], a[16:19], v[204:207]
buffer_load_dwordx4 v241, s[36:39], s92 offen offset:3072  lds
v_mfma_f32_16x16x32_bf16 v[204:207], a[132:135], a[20:23], v[204:207]
ds_read_b128 a[144:147], v236 offset:512
v_fma_f32 v158, v158, s56, v246
v_mfma_f32_16x16x32_bf16 v[208:211], a[132:135], a[52:55], v[208:211]
v_exp_f32 v157, v157
v_mfma_f32_16x16x32_bf16 v[208:211], a[128:131], a[48:51], v[208:211]
v_exp_f32 v158, v158
v_mfma_f32_16x16x32_bf16 v[212:215], a[128:131], a[80:83], v[212:215]
v_fma_f32 v159, v159, s56, v246
v_cvt_pk_bf16_f32 v88, v156, v157
v_mfma_f32_16x16x32_bf16 v[212:215], a[132:135], a[84:87], v[212:215]
ds_read_b128 a[148:151], v236 offset:528
v_fma_f32 v160, v160, s56, v247
v_mfma_f32_16x16x32_bf16 v[216:219], a[132:135], a[116:119], v[216:219]
s_mov_b32 m0, 156160
v_exp_f32 v159, v159
v_mfma_f32_16x16x32_bf16 v[216:219], a[128:131], a[112:115], v[216:219]
s_waitcnt lgkmcnt(2)
v_exp_f32 v160, v160
v_mfma_f32_16x16x32_bf16 v[216:219], a[136:139], a[120:123], v[216:219]
buffer_load_dwordx4 v254, s[24:27], s76 offen offset:2048  lds
v_mfma_f32_16x16x32_bf16 v[216:219], a[140:143], a[124:127], v[216:219]
ds_read_b128 a[128:131], v236 offset:544
v_cvt_pk_bf16_f32 v89, v158, v159
v_mfma_f32_16x16x32_bf16 v[212:215], a[140:143], a[92:95], v[212:215]
v_fma_f32 v161, v161, s56, v247
v_fma_f32 v162, v162, s56, v247
v_mfma_f32_16x16x32_bf16 v[212:215], a[136:139], a[88:91], v[212:215]
v_exp_f32 v161, v161
v_mfma_f32_16x16x32_bf16 v[208:211], a[136:139], a[56:59], v[208:211]
v_exp_f32 v162, v162
v_mfma_f32_16x16x32_bf16 v[208:211], a[140:143], a[60:63], v[208:211]
ds_read_b128 a[132:135], v236 offset:560
v_fma_f32 v163, v163, s56, v247
v_mfma_f32_16x16x32_bf16 v[204:207], a[140:143], a[28:31], v[204:207]
v_exp_f32 v163, v163
v_mfma_f32_16x16x32_bf16 v[204:207], a[136:139], a[24:27], v[204:207]
s_waitcnt lgkmcnt(2)
v_cvt_pk_bf16_f32 v160, v160, v161
v_accvgpr_write_b32 a244, v160
v_mfma_f32_16x16x32_bf16 v[220:223], a[144:147], a[0:3], 0
v_cvt_pk_bf16_f32 v162, v162, v163
v_accvgpr_write_b32 a245, v162
v_mfma_f32_16x16x32_bf16 v[220:223], a[148:151], a[4:7], v[220:223]
ds_read_b128 a[136:139], v236 offset:576
v_fma_f32 v164, v164, s56, v248
v_mfma_f32_16x16x32_bf16 v[224:227], a[148:151], a[36:39], 0
v_exp_f32 v164, v164
v_mfma_f32_16x16x32_bf16 v[224:227], a[144:147], a[32:35], v[224:227]
v_fma_f32 v165, v165, s56, v248
v_fma_f32 v166, v166, s56, v248
v_mfma_f32_16x16x32_bf16 v[228:231], a[144:147], a[64:67], 0
v_exp_f32 v165, v165
v_mfma_f32_16x16x32_bf16 v[228:231], a[148:151], a[68:71], v[228:231]
ds_read_b128 a[140:143], v236 offset:592
v_fma_f32 v167, v167, s56, v248
v_mfma_f32_16x16x32_bf16 v[232:235], a[148:151], a[100:103], 0
s_add_u32 m0, s62, 4256
v_exp_f32 v166, v166
v_mfma_f32_16x16x32_bf16 v[232:235], a[144:147], a[96:99], v[232:235]
s_waitcnt lgkmcnt(2)
v_exp_f32 v167, v167
v_mfma_f32_16x16x32_bf16 v[232:235], a[128:131], a[104:107], v[232:235]
buffer_load_dwordx4 v254, s[20:23], s74 offen offset:1024  lds
v_mfma_f32_16x16x32_bf16 v[232:235], a[132:135], a[108:111], v[232:235]
ds_read_b128 a[144:147], v236 offset:608
v_cvt_pk_bf16_f32 v92, v164, v165
v_mfma_f32_16x16x32_bf16 v[228:231], a[132:135], a[76:79], v[228:231]
v_cvt_pk_bf16_f32 v93, v166, v167
v_fma_f32 v168, v168, s56, v249
v_mfma_f32_16x16x32_bf16 v[228:231], a[128:131], a[72:75], v[228:231]
v_exp_f32 v168, v168
v_mfma_f32_16x16x32_bf16 v[224:227], a[128:131], a[40:43], v[224:227]
v_fma_f32 v169, v169, s56, v249
v_fma_f32 v170, v170, s56, v249
v_mfma_f32_16x16x32_bf16 v[224:227], a[132:135], a[44:47], v[224:227]
ds_read_b128 a[148:151], v236 offset:624
v_fma_f32 v171, v171, s56, v249
v_mfma_f32_16x16x32_bf16 v[220:223], a[132:135], a[12:15], v[220:223]
s_waitcnt vmcnt(8) lgkmcnt(2)
v_exp_f32 v169, v169
v_mfma_f32_16x16x32_bf16 v[220:223], a[128:131], a[8:11], v[220:223]
s_barrier
v_mfma_f32_16x16x32_bf16 v[220:223], a[136:139], a[16:19], v[220:223]
v_exp_f32 v170, v170
v_mfma_f32_16x16x32_bf16 v[220:223], a[140:143], a[20:23], v[220:223]
ds_read_b128 a[128:131], v236 offset:33792
v_cvt_pk_bf16_f32 v96, v168, v169
v_mfma_f32_16x16x32_bf16 v[224:227], a[140:143], a[52:55], v[224:227]
v_exp_f32 v171, v171
v_mfma_f32_16x16x32_bf16 v[224:227], a[136:139], a[48:51], v[224:227]
v_cvt_pk_bf16_f32 v97, v170, v171
v_fma_f32 v172, v172, s56, v246
)"
R"(
v_mfma_f32_16x16x32_bf16 v[228:231], a[136:139], a[80:83], v[228:231]
v_exp_f32 v172, v172
v_mfma_f32_16x16x32_bf16 v[228:231], a[140:143], a[84:87], v[228:231]
ds_read_b128 a[132:135], v236 offset:33808
v_fma_f32 v173, v173, s56, v246
v_mfma_f32_16x16x32_bf16 v[232:235], a[140:143], a[116:119], v[232:235]
s_add_u32 m0, s61, 141312
v_exp_f32 v173, v173
v_mfma_f32_16x16x32_bf16 v[232:235], a[136:139], a[112:115], v[232:235]
s_waitcnt lgkmcnt(2)
v_fma_f32 v174, v174, s56, v246
v_fma_f32 v175, v175, s56, v246
v_mfma_f32_16x16x32_bf16 v[232:235], a[144:147], a[120:123], v[232:235]
buffer_load_dwordx4 v242, s[40:43], 0 offen offset:0  lds
v_mfma_f32_16x16x32_bf16 v[232:235], a[148:151], a[124:127], v[232:235]
ds_read_b128 a[136:139], v236 offset:33824
v_cvt_pk_bf16_f32 v90, v172, v173
v_mfma_f32_16x16x32_bf16 v[228:231], a[148:151], a[92:95], v[228:231]
v_exp_f32 v174, v174
v_mfma_f32_16x16x32_bf16 v[228:231], a[144:147], a[88:91], v[228:231]
v_exp_f32 v175, v175
v_mfma_f32_16x16x32_bf16 v[224:227], a[144:147], a[56:59], v[224:227]
v_cvt_pk_bf16_f32 v91, v174, v175
v_fma_f32 v176, v176, s56, v247
v_mfma_f32_16x16x32_bf16 v[224:227], a[148:151], a[60:63], v[224:227]
ds_read_b128 a[140:143], v236 offset:33840
v_fma_f32 v177, v177, s56, v247
v_mfma_f32_16x16x32_bf16 v[220:223], a[148:151], a[28:31], v[220:223]
s_add_u32 m0, s61, 141328
v_exp_f32 v176, v176
v_mfma_f32_16x16x32_bf16 v[220:223], a[144:147], a[24:27], v[220:223]
s_waitcnt lgkmcnt(2)
v_exp_f32 v177, v177
v_mfma_f32_16x16x32_bf16 v[156:159], a[128:131], a[0:3], 0
buffer_load_dwordx4 v242, s[40:43], 0 offen offset:1024  lds
v_mfma_f32_16x16x32_bf16 v[156:159], a[132:135], a[4:7], v[156:159]
ds_read_b128 a[144:147], v236 offset:33856
v_fma_f32 v178, v178, s56, v247
v_mfma_f32_16x16x32_bf16 v[160:163], a[132:135], a[36:39], 0
v_exp_f32 v178, v178
v_mfma_f32_16x16x32_bf16 v[160:163], a[128:131], a[32:35], v[160:163]
v_fma_f32 v179, v179, s56, v247
v_cvt_pk_bf16_f32 v176, v176, v177
v_mfma_f32_16x16x32_bf16 v[164:167], a[128:131], a[64:67], 0
v_exp_f32 v179, v179
v_mfma_f32_16x16x32_bf16 v[164:167], a[132:135], a[68:71], v[164:167]
ds_read_b128 a[148:151], v236 offset:33872
v_accvgpr_write_b32 a246, v176
v_mfma_f32_16x16x32_bf16 v[168:171], a[132:135], a[100:103], 0
s_add_u32 m0, s61, 149760
v_cvt_pk_bf16_f32 v178, v178, v179
v_accvgpr_write_b32 a247, v178
v_mfma_f32_16x16x32_bf16 v[168:171], a[128:131], a[96:99], v[168:171]
s_waitcnt lgkmcnt(2)
v_fma_f32 v180, v180, s56, v248
v_fma_f32 v181, v181, s56, v248
v_mfma_f32_16x16x32_bf16 v[168:171], a[136:139], a[104:107], v[168:171]
buffer_load_dwordx4 v242, s[40:43], s79 offen offset:0  lds
v_mfma_f32_16x16x32_bf16 v[168:171], a[140:143], a[108:111], v[168:171]
ds_read_b128 a[128:131], v236 offset:33888
v_fma_f32 v182, v182, s56, v248
v_mfma_f32_16x16x32_bf16 v[164:167], a[140:143], a[76:79], v[164:167]
v_exp_f32 v180, v180
v_mfma_f32_16x16x32_bf16 v[164:167], a[136:139], a[72:75], v[164:167]
v_exp_f32 v181, v181
v_mfma_f32_16x16x32_bf16 v[160:163], a[136:139], a[40:43], v[160:163]
v_exp_f32 v182, v182
v_mfma_f32_16x16x32_bf16 v[160:163], a[140:143], a[44:47], v[160:163]
ds_read_b128 a[132:135], v236 offset:33904
v_fma_f32 v183, v183, s56, v248
v_mfma_f32_16x16x32_bf16 v[156:159], a[140:143], a[12:15], v[156:159]
s_add_u32 m0, s61, 149776
v_exp_f32 v183, v183
v_mfma_f32_16x16x32_bf16 v[156:159], a[136:139], a[8:11], v[156:159]
s_waitcnt lgkmcnt(2)
v_cvt_pk_bf16_f32 v94, v180, v181
v_cvt_pk_bf16_f32 v95, v182, v183
v_mfma_f32_16x16x32_bf16 v[156:159], a[144:147], a[16:19], v[156:159]
buffer_load_dwordx4 v242, s[40:43], s79 offen offset:1024  lds
v_mfma_f32_16x16x32_bf16 v[156:159], a[148:151], a[20:23], v[156:159]
ds_read_b128 a[136:139], v236 offset:34304
v_fma_f32 v184, v184, s56, v249
v_mfma_f32_16x16x32_bf16 v[160:163], a[148:151], a[52:55], v[160:163]
v_exp_f32 v184, v184
v_mfma_f32_16x16x32_bf16 v[160:163], a[144:147], a[48:51], v[160:163]
v_fma_f32 v185, v185, s56, v249
v_fma_f32 v186, v186, s56, v249
v_mfma_f32_16x16x32_bf16 v[164:167], a[144:147], a[80:83], v[164:167]
v_exp_f32 v185, v185
v_mfma_f32_16x16x32_bf16 v[164:167], a[148:151], a[84:87], v[164:167]
ds_read_b128 a[140:143], v236 offset:34320
v_fma_f32 v187, v187, s56, v249
v_mfma_f32_16x16x32_bf16 v[168:171], a[148:151], a[116:119], v[168:171]
s_add_u32 m0, s62, 4288
v_exp_f32 v186, v186
v_mfma_f32_16x16x32_bf16 v[168:171], a[144:147], a[112:115], v[168:171]
.Lpad4_%=:
s_cbranch_vccnz .Lret1_%=
s_waitcnt lgkmcnt(2)
v_exp_f32 v187, v187
v_mfma_f32_16x16x32_bf16 v[168:171], a[128:131], a[120:123], v[168:171]
buffer_load_dwordx4 v254, s[20:23], s74 offen offset:2048  lds
v_mfma_f32_16x16x32_bf16 v[168:171], a[132:135], a[124:127], v[168:171]
ds_read_b128 a[144:147], v236 offset:34336
v_cvt_pk_bf16_f32 v98, v184, v185
v_mfma_f32_16x16x32_bf16 v[164:167], a[132:135], a[92:95], v[164:167]
v_cvt_pk_bf16_f32 v99, v186, v187
v_fma_f32 v188, v188, s56, v246
v_mfma_f32_16x16x32_bf16 v[164:167], a[128:131], a[88:91], v[164:167]
v_exp_f32 v188, v188
v_mfma_f32_16x16x32_bf16 v[160:163], a[128:131], a[56:59], v[160:163]
v_fma_f32 v189, v189, s56, v246
v_fma_f32 v190, v190, s56, v246
v_mfma_f32_16x16x32_bf16 v[160:163], a[132:135], a[60:63], v[160:163]
ds_read_b128 a[148:151], v236 offset:34352
v_fma_f32 v191, v191, s56, v246
v_mfma_f32_16x16x32_bf16 v[156:159], a[132:135], a[28:31], v[156:159]
v_exp_f32 v189, v189
v_mfma_f32_16x16x32_bf16 v[156:159], a[128:131], a[24:27], v[156:159]
s_waitcnt lgkmcnt(2)
v_exp_f32 v190, v190
v_mfma_f32_16x16x32_bf16 v[172:175], a[136:139], a[0:3], 0
v_exp_f32 v191, v191
v_mfma_f32_16x16x32_bf16 v[172:175], a[140:143], a[4:7], v[172:175]
ds_read_b128 a[128:131], v236 offset:34368
v_cvt_pk_bf16_f32 v100, v188, v189
v_mfma_f32_16x16x32_bf16 v[176:179], a[140:143], a[36:39], 0
v_cvt_pk_bf16_f32 v101, v190, v191
v_fma_f32 v192, v192, s56, v247
v_mfma_f32_16x16x32_bf16 v[176:179], a[136:139], a[32:35], v[176:179]
v_exp_f32 v192, v192
v_mfma_f32_16x16x32_bf16 v[180:183], a[136:139], a[64:67], 0
v_fma_f32 v193, v193, s56, v247
v_fma_f32 v194, v194, s56, v247
v_mfma_f32_16x16x32_bf16 v[180:183], a[140:143], a[68:71], v[180:183]
ds_read_b128 a[132:135], v236 offset:34384
v_fma_f32 v195, v195, s56, v247
v_mfma_f32_16x16x32_bf16 v[184:187], a[140:143], a[100:103], 0
v_exp_f32 v193, v193
v_mfma_f32_16x16x32_bf16 v[184:187], a[136:139], a[96:99], v[184:187]
s_waitcnt lgkmcnt(2)
v_exp_f32 v194, v194
v_mfma_f32_16x16x32_bf16 v[184:187], a[144:147], a[104:107], v[184:187]
v_exp_f32 v195, v195
v_mfma_f32_16x16x32_bf16 v[184:187], a[148:151], a[108:111], v[184:187]
ds_read_b128 a[136:139], v236 offset:34400
v_cvt_pk_bf16_f32 v104, v192, v193
v_mfma_f32_16x16x32_bf16 v[180:183], a[148:151], a[76:79], v[180:183]
v_cvt_pk_bf16_f32 v105, v194, v195
v_fma_f32 v196, v196, s56, v248
v_mfma_f32_16x16x32_bf16 v[180:183], a[144:147], a[72:75], v[180:183]
v_exp_f32 v196, v196
v_mfma_f32_16x16x32_bf16 v[176:179], a[144:147], a[40:43], v[176:179]
v_fma_f32 v197, v197, s56, v248
v_fma_f32 v198, v198, s56, v248
v_mfma_f32_16x16x32_bf16 v[176:179], a[148:151], a[44:47], v[176:179]
s_add_u32 m0, s61, 73728
ds_read_b128 a[140:143], v236 offset:34416
v_fma_f32 v199, v199, s56, v248
v_mfma_f32_16x16x32_bf16 v[172:175], a[148:151], a[12:15], v[172:175]
s_waitcnt vmcnt(7) lgkmcnt(2)
v_exp_f32 v197, v197
v_mfma_f32_16x16x32_bf16 v[172:175], a[144:147], a[8:11], v[172:175]
s_barrier
v_mfma_f32_16x16x32_bf16 v[172:175], a[128:131], a[16:19], v[172:175]
buffer_load_dwordx4 v242, s[40:43], s81 offen offset:0  lds
v_mfma_f32_16x16x32_bf16 v[172:175], a[132:135], a[20:23], v[172:175]
ds_read_b128 a[144:147], v236 offset:50688
v_cvt_pk_bf16_f32 v196, v196, v197
v_mfma_f32_16x16x32_bf16 v[176:179], a[132:135], a[52:55], v[176:179]
v_exp_f32 v198, v198
v_mfma_f32_16x16x32_bf16 v[176:179], a[128:131], a[48:51], v[176:179]
v_exp_f32 v199, v199
v_mfma_f32_16x16x32_bf16 v[180:183], a[128:131], a[80:83], v[180:183]
v_accvgpr_write_b32 a248, v196
v_cvt_pk_bf16_f32 v198, v198, v199
v_mfma_f32_16x16x32_bf16 v[180:183], a[132:135], a[84:87], v[180:183]
ds_read_b128 a[148:151], v236 offset:50704
v_accvgpr_write_b32 a249, v198
v_mfma_f32_16x16x32_bf16 v[184:187], a[132:135], a[116:119], v[184:187]
s_add_u32 m0, s61, 73744
v_fma_f32 v200, v200, s56, v249
v_fma_f32 v201, v201, s56, v249
v_mfma_f32_16x16x32_bf16 v[184:187], a[128:131], a[112:115], v[184:187]
s_waitcnt lgkmcnt(2)
v_exp_f32 v200, v200
v_mfma_f32_16x16x32_bf16 v[184:187], a[136:139], a[120:123], v[184:187]
buffer_load_dwordx4 v242, s[40:43], s81 offen offset:1024  lds
v_mfma_f32_16x16x32_bf16 v[184:187], a[140:143], a[124:127], v[184:187]
ds_read_b128 a[128:131], v236 offset:50720
v_fma_f32 v202, v202, s56, v249
v_mfma_f32_16x16x32_bf16 v[180:183], a[140:143], a[92:95], v[180:183]
v_exp_f32 v201, v201
v_mfma_f32_16x16x32_bf16 v[180:183], a[136:139], a[88:91], v[180:183]
v_exp_f32 v202, v202
v_mfma_f32_16x16x32_bf16 v[176:179], a[136:139], a[56:59], v[176:179]
v_fma_f32 v203, v203, s56, v249
v_cvt_pk_bf16_f32 v108, v200, v201
v_mfma_f32_16x16x32_bf16 v[176:179], a[140:143], a[60:63], v[176:179]
ds_read_b128 a[132:135], v236 offset:50736
v_fma_f32 v140, v140, s56, v246
v_mfma_f32_16x16x32_bf16 v[172:175], a[140:143], a[28:31], v[172:175]
s_add_u32 m0, s62, 4320
v_exp_f32 v203, v203
v_mfma_f32_16x16x32_bf16 v[172:175], a[136:139], a[24:27], v[172:175]
s_waitcnt lgkmcnt(2)
v_exp_f32 v140, v140
v_mfma_f32_16x16x32_bf16 v[188:191], a[144:147], a[0:3], 0
buffer_load_dwordx4 v254, s[20:23], s74 offen offset:3072  lds
v_mfma_f32_16x16x32_bf16 v[188:191], a[148:151], a[4:7], v[188:191]
ds_read_b128 a[136:139], v236 offset:50752
v_cvt_pk_bf16_f32 v109, v202, v203
v_mfma_f32_16x16x32_bf16 v[192:195], a[148:151], a[36:39], 0
v_fma_f32 v141, v141, s56, v246
v_fma_f32 v142, v142, s56, v246
v_mfma_f32_16x16x32_bf16 v[192:195], a[144:147], a[32:35], v[192:195]
v_exp_f32 v141, v141
v_mfma_f32_16x16x32_bf16 v[196:199], a[144:147], a[64:67], 0
v_exp_f32 v142, v142
v_mfma_f32_16x16x32_bf16 v[196:199], a[148:151], a[68:71], v[196:199]
ds_read_b128 a[140:143], v236 offset:50768
v_fma_f32 v143, v143, s56, v246
v_mfma_f32_16x16x32_bf16 v[200:203], a[148:151], a[100:103], 0
s_add_u32 m0, s61, 82176
v_exp_f32 v143, v143
v_mfma_f32_16x16x32_bf16 v[200:203], a[144:147], a[96:99], v[200:203]
s_waitcnt lgkmcnt(2)
v_cvt_pk_bf16_f32 v102, v140, v141
v_cvt_pk_bf16_f32 v103, v142, v143
v_mfma_f32_16x16x32_bf16 v[200:203], a[128:131], a[104:107], v[200:203]
buffer_load_dwordx4 v242, s[40:43], s83 offen offset:0  lds
v_mfma_f32_16x16x32_bf16 v[200:203], a[132:135], a[108:111], v[200:203]
ds_read_b128 a[144:147], v236 offset:50784
v_fma_f32 v144, v144, s56, v247
v_mfma_f32_16x16x32_bf16 v[196:199], a[132:135], a[76:79], v[196:199]
v_exp_f32 v144, v144
v_mfma_f32_16x16x32_bf16 v[196:199], a[128:131], a[72:75], v[196:199]
v_fma_f32 v145, v145, s56, v247
v_fma_f32 v146, v146, s56, v247
v_mfma_f32_16x16x32_bf16 v[192:195], a[128:131], a[40:43], v[192:195]
v_exp_f32 v145, v145
v_mfma_f32_16x16x32_bf16 v[192:195], a[132:135], a[44:47], v[192:195]
ds_read_b128 a[148:151], v236 offset:50800
v_fma_f32 v147, v147, s56, v247
v_mfma_f32_16x16x32_bf16 v[188:191], a[132:135], a[12:15], v[188:191]
s_add_u32 m0, s61, 82192
v_exp_f32 v146, v146
v_mfma_f32_16x16x32_bf16 v[188:191], a[128:131], a[8:11], v[188:191]
s_waitcnt lgkmcnt(2)
v_exp_f32 v147, v147
v_mfma_f32_16x16x32_bf16 v[188:191], a[136:139], a[16:19], v[188:191]
buffer_load_dwordx4 v242, s[40:43], s83 offen offset:1024  lds
v_mfma_f32_16x16x32_bf16 v[188:191], a[140:143], a[20:23], v[188:191]
ds_read_b128 a[128:131], v236 offset:51200
v_cvt_pk_bf16_f32 v106, v144, v145
v_mfma_f32_16x16x32_bf16 v[192:195], a[140:143], a[52:55], v[192:195]
v_cvt_pk_bf16_f32 v107, v146, v147
v_fma_f32 v148, v148, s56, v248
v_mfma_f32_16x16x32_bf16 v[192:195], a[136:139], a[48:51], v[192:195]
v_exp_f32 v148, v148
v_mfma_f32_16x16x32_bf16 v[196:199], a[136:139], a[80:83], v[196:199]
v_fma_f32 v149, v149, s56, v248
v_fma_f32 v150, v150, s56, v248
v_mfma_f32_16x16x32_bf16 v[196:199], a[140:143], a[84:87], v[196:199]
)"
R"(
ds_read_b128 a[132:135], v236 offset:51216
v_fma_f32 v151, v151, s56, v248
v_mfma_f32_16x16x32_bf16 v[200:203], a[140:143], a[116:119], v[200:203]
s_add_u32 m0, s61, 90624
v_exp_f32 v149, v149
v_mfma_f32_16x16x32_bf16 v[200:203], a[136:139], a[112:115], v[200:203]
s_waitcnt lgkmcnt(2)
v_exp_f32 v150, v150
v_mfma_f32_16x16x32_bf16 v[200:203], a[144:147], a[120:123], v[200:203]
buffer_load_dwordx4 v242, s[40:43], s85 offen offset:0  lds
v_mfma_f32_16x16x32_bf16 v[200:203], a[148:151], a[124:127], v[200:203]
ds_read_b128 a[136:139], v236 offset:51232
v_cvt_pk_bf16_f32 v148, v148, v149
v_mfma_f32_16x16x32_bf16 v[196:199], a[148:151], a[92:95], v[196:199]
v_exp_f32 v151, v151
v_mfma_f32_16x16x32_bf16 v[196:199], a[144:147], a[88:91], v[196:199]
v_accvgpr_write_b32 a250, v148
v_cvt_pk_bf16_f32 v150, v150, v151
v_mfma_f32_16x16x32_bf16 v[192:195], a[144:147], a[56:59], v[192:195]
v_accvgpr_write_b32 a251, v150
v_fma_f32 v152, v152, s56, v249
v_mfma_f32_16x16x32_bf16 v[192:195], a[148:151], a[60:63], v[192:195]
ds_read_b128 a[140:143], v236 offset:51248
v_fma_f32 v153, v153, s56, v249
v_mfma_f32_16x16x32_bf16 v[188:191], a[148:151], a[28:31], v[188:191]
s_add_u32 m0, s61, 90640
v_exp_f32 v152, v152
v_mfma_f32_16x16x32_bf16 v[188:191], a[144:147], a[24:27], v[188:191]
s_waitcnt lgkmcnt(2)
v_exp_f32 v153, v153
v_mfma_f32_16x16x32_bf16 v[140:143], a[128:131], a[0:3], 0
buffer_load_dwordx4 v242, s[40:43], s85 offen offset:1024  lds
v_mfma_f32_16x16x32_bf16 v[140:143], a[132:135], a[4:7], v[140:143]
ds_read_b128 a[144:147], v236 offset:51264
v_fma_f32 v154, v154, s56, v249
v_mfma_f32_16x16x32_bf16 v[144:147], a[132:135], a[36:39], 0
v_exp_f32 v154, v154
v_mfma_f32_16x16x32_bf16 v[144:147], a[128:131], a[32:35], v[144:147]
v_fma_f32 v155, v155, s56, v249
v_cvt_pk_bf16_f32 v110, v152, v153
v_mfma_f32_16x16x32_bf16 v[148:151], a[128:131], a[64:67], 0
v_exp_f32 v155, v155
v_mfma_f32_16x16x32_bf16 v[148:151], a[132:135], a[68:71], v[148:151]
ds_read_b128 a[148:151], v236 offset:51280
v_cvt_pk_bf16_f32 v111, v154, v155
v_mfma_f32_16x16x32_bf16 v[152:155], a[132:135], a[100:103], 0
s_add_u32 m0, s61, 99072
v_fma_f32 v204, v204, s56, v246
v_fma_f32 v205, v205, s56, v246
v_mfma_f32_16x16x32_bf16 v[152:155], a[128:131], a[96:99], v[152:155]
s_waitcnt lgkmcnt(2)
v_exp_f32 v204, v204
v_mfma_f32_16x16x32_bf16 v[152:155], a[136:139], a[104:107], v[152:155]
buffer_load_dwordx4 v242, s[40:43], s87 offen offset:0  lds
v_mfma_f32_16x16x32_bf16 v[152:155], a[140:143], a[108:111], v[152:155]
ds_read_b128 a[128:131], v236 offset:51296
v_accvgpr_write_b32 a96, s71
v_mfma_f32_16x16x32_bf16 v[148:151], a[140:143], a[76:79], v[148:151]
v_exp_f32 v205, v205
v_mfma_f32_16x16x32_bf16 v[148:151], a[136:139], a[72:75], v[148:151]
v_accvgpr_write_b32 a97, s71
v_accvgpr_write_b32 a98, s71
v_mfma_f32_16x16x32_bf16 v[144:147], a[136:139], a[40:43], v[144:147]
v_accvgpr_write_b32 a99, s71
v_fma_f32 v206, v206, s56, v246
v_mfma_f32_16x16x32_bf16 v[144:147], a[140:143], a[44:47], v[144:147]
ds_read_b128 a[132:135], v236 offset:51312
v_fma_f32 v207, v207, s56, v246
v_mfma_f32_16x16x32_bf16 v[140:143], a[140:143], a[12:15], v[140:143]
s_add_u32 m0, s61, 99088
v_exp_f32 v206, v206
v_mfma_f32_16x16x32_bf16 v[140:143], a[136:139], a[8:11], v[140:143]
s_waitcnt lgkmcnt(2)
v_exp_f32 v207, v207
v_mfma_f32_16x16x32_bf16 v[140:143], a[144:147], a[16:19], v[140:143]
buffer_load_dwordx4 v242, s[40:43], s87 offen offset:1024  lds
v_mfma_f32_16x16x32_bf16 v[140:143], a[148:151], a[20:23], v[140:143]
v_cvt_pk_bf16_f32 v112, v204, v205
v_cvt_pk_bf16_f32 v113, v206, v207
v_mfma_f32_16x16x32_bf16 v[144:147], a[148:151], a[52:55], v[144:147]
v_fma_f32 v208, v208, s56, v247
v_fma_f32 v209, v209, s56, v247
v_mfma_f32_16x16x32_bf16 v[144:147], a[144:147], a[48:51], v[144:147]
v_exp_f32 v208, v208
v_mfma_f32_16x16x32_bf16 v[148:151], a[144:147], a[80:83], v[148:151]
v_exp_f32 v209, v209
v_mfma_f32_16x16x32_bf16 v[148:151], a[148:151], a[84:87], v[148:151]
v_fma_f32 v210, v210, s56, v247
v_fma_f32 v211, v211, s56, v247
v_mfma_f32_16x16x32_bf16 v[152:155], a[148:151], a[116:119], v[152:155]
v_exp_f32 v210, v210
v_mfma_f32_16x16x32_bf16 v[152:155], a[144:147], a[112:115], v[152:155]
s_waitcnt lgkmcnt(0)
v_exp_f32 v211, v211
v_mfma_f32_16x16x32_bf16 v[152:155], a[128:131], a[120:123], v[152:155]
v_cvt_pk_bf16_f32 v116, v208, v209
v_cvt_pk_bf16_f32 v117, v210, v211
v_mfma_f32_16x16x32_bf16 v[152:155], a[132:135], a[124:127], v[152:155]
v_fma_f32 v212, v212, s56, v248
v_fma_f32 v213, v213, s56, v248
v_mfma_f32_16x16x32_bf16 v[148:151], a[132:135], a[92:95], v[148:151]
v_exp_f32 v212, v212
v_mfma_f32_16x16x32_bf16 v[148:151], a[128:131], a[88:91], v[148:151]
v_exp_f32 v213, v213
v_mfma_f32_16x16x32_bf16 v[144:147], a[128:131], a[56:59], v[144:147]
v_fma_f32 v214, v214, s56, v248
v_fma_f32 v215, v215, s56, v248
v_mfma_f32_16x16x32_bf16 v[144:147], a[132:135], a[60:63], v[144:147]
s_add_u32 m0, s61, 107520
v_exp_f32 v214, v214
v_mfma_f32_16x16x32_bf16 v[140:143], a[132:135], a[28:31], v[140:143]
s_waitcnt vmcnt(10)
v_exp_f32 v215, v215
v_mfma_f32_16x16x32_bf16 v[140:143], a[128:131], a[24:27], v[140:143]
s_barrier
v_mfma_f32_16x16x32_bf16 a[100:103], a[96:99], a[156:159], 0
buffer_load_dwordx4 v242, s[40:43], s89 offen offset:0  lds
v_mfma_f32_16x16x32_bf16 a[100:103], a[96:99], a[172:175], a[100:103]
s_waitcnt vmcnt(7)
ds_read_b128 a[32:35], v240 offset:0
v_cvt_pk_bf16_f32 v120, v212, v213
v_mfma_f32_16x16x32_bf16 a[100:103], a[96:99], a[188:191], a[100:103]
v_cvt_pk_bf16_f32 v121, v214, v215
v_fma_f32 v216, v216, s56, v249
v_mfma_f32_16x16x32_bf16 a[100:103], a[96:99], a[204:207], a[100:103]
ds_read_b128 a[36:39], v240 offset:16
v_fma_f32 v217, v217, s56, v249
v_mfma_f32_16x16x32_bf16 a[100:103], a[96:99], a[220:223], a[100:103]
v_exp_f32 v216, v216
v_mfma_f32_16x16x32_bf16 a[100:103], a[96:99], a[224:227], a[100:103]
ds_read_b128 a[40:43], v240 offset:32
v_fma_f32 v218, v218, s56, v249
v_mfma_f32_16x16x32_bf16 a[100:103], a[96:99], v[24:27], a[100:103]
v_exp_f32 v217, v217
v_mfma_f32_16x16x32_bf16 a[100:103], a[96:99], v[36:39], a[100:103]
s_add_u32 m0, s61, 107536
ds_read_b128 a[44:47], v240 offset:48
v_fma_f32 v219, v219, s56, v249
v_mfma_f32_16x16x32_bf16 a[104:107], a[96:99], a[160:163], 0
buffer_load_dwordx4 v242, s[40:43], s89 offen offset:1024  lds
v_mfma_f32_16x16x32_bf16 a[104:107], a[96:99], a[176:179], a[104:107]
ds_read_b128 a[48:51], v240 offset:64
v_cvt_pk_bf16_f32 v216, v216, v217
v_mfma_f32_16x16x32_bf16 a[104:107], a[96:99], a[192:195], a[104:107]
v_exp_f32 v218, v218
v_mfma_f32_16x16x32_bf16 a[104:107], a[96:99], a[208:211], a[104:107]
ds_read_b128 a[52:55], v240 offset:80
v_accvgpr_write_b32 a252, v216
v_mfma_f32_16x16x32_bf16 a[104:107], a[96:99], v[0:3], a[104:107]
v_exp_f32 v219, v219
v_mfma_f32_16x16x32_bf16 a[104:107], a[96:99], v[12:15], a[104:107]
ds_read_b128 a[56:59], v240 offset:96
v_cvt_pk_bf16_f32 v218, v218, v219
v_mfma_f32_16x16x32_bf16 a[104:107], a[96:99], a[228:231], a[104:107]
v_accvgpr_write_b32 a253, v218
v_fma_f32 v220, v220, s56, v246
v_mfma_f32_16x16x32_bf16 a[104:107], a[96:99], v[40:43], a[104:107]
ds_read_b128 a[60:63], v240 offset:112
v_fma_f32 v221, v221, s56, v246
v_mfma_f32_16x16x32_bf16 a[108:111], a[96:99], a[164:167], 0
ds_read_b64_tr_b16 a[128:129], v239 offset:0
v_fma_f32 v222, v222, s56, v246
v_mfma_f32_16x16x32_bf16 a[108:111], a[96:99], a[180:183], a[108:111]
ds_read_b128 a[0:3], v240 offset:36864
v_fma_f32 v223, v223, s56, v246
v_mfma_f32_16x16x32_bf16 a[108:111], a[96:99], a[196:199], a[108:111]
ds_read_b64_tr_b16 a[130:131], v239 offset:128
v_fma_f32 v224, v224, s56, v247
v_mfma_f32_16x16x32_bf16 a[108:111], a[96:99], a[212:215], a[108:111]
ds_read_b128 a[4:7], v240 offset:36880
v_fma_f32 v225, v225, s56, v247
v_mfma_f32_16x16x32_bf16 a[108:111], a[96:99], v[4:7], a[108:111]
ds_read_b64_tr_b16 a[132:133], v239 offset:512
v_fma_f32 v226, v226, s56, v247
v_mfma_f32_16x16x32_bf16 a[108:111], a[96:99], v[16:19], a[108:111]
ds_read_b128 a[8:11], v240 offset:36896
v_fma_f32 v227, v227, s56, v247
v_mfma_f32_16x16x32_bf16 a[108:111], a[96:99], v[28:31], a[108:111]
ds_read_b64_tr_b16 a[134:135], v239 offset:640
v_fma_f32 v228, v228, s56, v248
v_mfma_f32_16x16x32_bf16 a[108:111], a[96:99], a[232:235], a[108:111]
s_add_u32 m0, s61, 115968
ds_read_b128 a[12:15], v240 offset:36912
v_fma_f32 v229, v229, s56, v248
v_mfma_f32_16x16x32_bf16 a[112:115], a[96:99], a[168:171], 0
buffer_load_dwordx4 v242, s[40:43], s91 offen offset:0  lds
v_mfma_f32_16x16x32_bf16 a[112:115], a[96:99], a[184:187], a[112:115]
ds_read_b128 a[16:19], v240 offset:36928
v_fma_f32 v230, v230, s56, v248
v_mfma_f32_16x16x32_bf16 a[112:115], a[96:99], a[200:203], a[112:115]
v_exp_f32 v220, v220
v_mfma_f32_16x16x32_bf16 a[112:115], a[96:99], a[216:219], a[112:115]
ds_read_b128 a[20:23], v240 offset:36944
v_fma_f32 v231, v231, s56, v248
v_mfma_f32_16x16x32_bf16 a[112:115], a[96:99], v[8:11], a[112:115]
v_exp_f32 v221, v221
v_fma_f32 v232, v232, s56, v249
v_mfma_f32_16x16x32_bf16 a[112:115], a[96:99], v[20:23], a[112:115]
ds_read_b128 a[24:27], v240 offset:36960
v_cvt_pk_bf16_f32 v114, v220, v221
v_mfma_f32_16x16x32_bf16 a[112:115], a[96:99], v[32:35], a[112:115]
s_add_u32 m0, s61, 115984
v_exp_f32 v222, v222
v_mfma_f32_16x16x32_bf16 a[112:115], a[96:99], v[44:47], a[112:115]
s_waitcnt lgkmcnt(4)
ds_read_b128 a[28:31], v240 offset:36976
v_fma_f32 v233, v233, s56, v249
v_mfma_f32_16x16x32_bf16 v[204:207], a[156:159], a[128:131], 0
buffer_load_dwordx4 v242, s[40:43], s91 offen offset:1024  lds
v_mfma_f32_16x16x32_bf16 v[204:207], a[172:175], a[132:135], v[204:207]
v_exp_f32 v223, v223
v_mfma_f32_16x16x32_bf16 v[208:211], a[176:179], a[132:135], 0
v_exp_f32 v224, v224
v_mfma_f32_16x16x32_bf16 v[208:211], a[160:163], a[128:131], v[208:211]
v_exp_f32 v225, v225
v_mfma_f32_16x16x32_bf16 v[212:215], a[164:167], a[128:131], 0
v_exp_f32 v226, v226
v_mfma_f32_16x16x32_bf16 v[212:215], a[180:183], a[132:135], v[212:215]
v_exp_f32 v227, v227
v_mfma_f32_16x16x32_bf16 v[216:219], a[184:187], a[132:135], 0
v_exp_f32 v228, v228
v_mfma_f32_16x16x32_bf16 v[216:219], a[168:171], a[128:131], v[216:219]
v_exp_f32 v229, v229
v_mfma_f32_16x16x32_bf16 a[100:103], a[96:99], v[48:51], a[100:103]
s_add_u32 m0, s62, 0
ds_read_b64_tr_b16 a[136:137], v239 offset:8448
v_cvt_pk_bf16_f32 v115, v222, v223
v_mfma_f32_16x16x32_bf16 a[100:103], a[96:99], v[60:63], a[100:103]
buffer_load_dwordx4 v254, s[24:27], s75 offen offset:0  lds
v_mfma_f32_16x16x32_bf16 a[100:103], a[96:99], a[240:243], a[100:103]
ds_read_b64_tr_b16 a[138:139], v239 offset:8576
v_cvt_pk_bf16_f32 v118, v224, v225
v_mfma_f32_16x16x32_bf16 a[100:103], a[96:99], v[88:91], a[100:103]
v_exp_f32 v230, v230
v_mfma_f32_16x16x32_bf16 a[104:107], a[96:99], v[52:55], a[104:107]
ds_read_b64_tr_b16 a[140:141], v239 offset:8960
v_cvt_pk_bf16_f32 v119, v226, v227
v_mfma_f32_16x16x32_bf16 a[104:107], a[96:99], v[64:67], a[104:107]
v_exp_f32 v231, v231
v_mfma_f32_16x16x32_bf16 a[104:107], a[96:99], v[76:79], a[104:107]
ds_read_b64_tr_b16 a[142:143], v239 offset:9088
v_cvt_pk_bf16_f32 v122, v228, v229
v_mfma_f32_16x16x32_bf16 a[104:107], a[96:99], a[244:247], a[104:107]
v_exp_f32 v232, v232
v_mfma_f32_16x16x32_bf16 a[108:111], a[96:99], v[56:59], a[108:111]
s_add_u32 m0, s61, 124288
ds_read_b64_tr_b16 a[144:145], v239 offset:16
v_cvt_pk_bf16_f32 v123, v230, v231
v_mfma_f32_16x16x32_bf16 a[108:111], a[96:99], v[68:71], a[108:111]
buffer_load_dwordx4 v242, s[40:43], 0 offen offset:128  lds
v_mfma_f32_16x16x32_bf16 a[108:111], a[96:99], v[80:83], a[108:111]
ds_read_b64_tr_b16 a[146:147], v239 offset:144
v_fma_f32 v234, v234, s56, v249
v_mfma_f32_16x16x32_bf16 a[108:111], a[96:99], v[92:95], a[108:111]
v_exp_f32 v233, v233
v_mfma_f32_16x16x32_bf16 a[112:115], a[96:99], a[236:239], a[112:115]
ds_read_b64_tr_b16 a[148:149], v239 offset:528
v_fma_f32 v235, v235, s56, v249
v_mfma_f32_16x16x32_bf16 a[112:115], a[96:99], v[72:75], a[112:115]
v_exp_f32 v234, v234
v_mfma_f32_16x16x32_bf16 a[112:115], a[96:99], v[84:87], a[112:115]
ds_read_b64_tr_b16 a[150:151], v239 offset:656
)"
R"(
v_cvt_pk_bf16_f32 v232, v232, v233
v_mfma_f32_16x16x32_bf16 a[112:115], a[96:99], v[96:99], a[112:115]
s_waitcnt lgkmcnt(4)
v_exp_f32 v235, v235
v_mfma_f32_16x16x32_bf16 v[204:207], a[188:191], a[136:139], v[204:207]
s_add_u32 m0, s62, 32
ds_read_b64_tr_b16 a[128:129], v239 offset:8464
v_accvgpr_write_b32 a254, v232
v_mfma_f32_16x16x32_bf16 v[204:207], a[204:207], a[140:143], v[204:207]
buffer_load_dwordx4 v254, s[24:27], s75 offen offset:1024  lds
v_mfma_f32_16x16x32_bf16 v[208:211], a[208:211], a[140:143], v[208:211]
ds_read_b64_tr_b16 a[130:131], v239 offset:8592
v_cvt_pk_bf16_f32 v234, v234, v235
v_mfma_f32_16x16x32_bf16 v[208:211], a[192:195], a[136:139], v[208:211]
v_accvgpr_write_b32 a255, v234
v_fma_f32 v156, v156, s56, v246
v_mfma_f32_16x16x32_bf16 v[212:215], a[196:199], a[136:139], v[212:215]
ds_read_b64_tr_b16 a[132:133], v239 offset:8976
v_fma_f32 v157, v157, s56, v246
v_mfma_f32_16x16x32_bf16 v[212:215], a[212:215], a[140:143], v[212:215]
v_exp_f32 v156, v156
v_mfma_f32_16x16x32_bf16 v[216:219], a[216:219], a[140:143], v[216:219]
s_waitcnt vmcnt(11) lgkmcnt(3)
ds_read_b64_tr_b16 a[134:135], v239 offset:9104
v_fma_f32 v158, v158, s56, v246
v_mfma_f32_16x16x32_bf16 v[216:219], a[200:203], a[136:139], v[216:219]
s_barrier
v_mfma_f32_16x16x32_bf16 v[220:223], a[156:159], a[144:147], 0
s_add_u32 m0, s61, 124304
ds_read_b64_tr_b16 a[136:137], v238 offset:0
v_fma_f32 v159, v159, s56, v246
v_mfma_f32_16x16x32_bf16 v[220:223], a[172:175], a[148:151], v[220:223]
buffer_load_dwordx4 v242, s[40:43], 0 offen offset:1152  lds
v_mfma_f32_16x16x32_bf16 v[224:227], a[176:179], a[148:151], 0
ds_read_b64_tr_b16 a[138:139], v238 offset:128
v_fma_f32 v160, v160, s56, v247
v_mfma_f32_16x16x32_bf16 v[224:227], a[160:163], a[144:147], v[224:227]
v_exp_f32 v157, v157
v_mfma_f32_16x16x32_bf16 v[228:231], a[164:167], a[144:147], 0
ds_read_b64_tr_b16 a[140:141], v238 offset:512
v_cvt_pk_bf16_f32 v124, v156, v157
v_mfma_f32_16x16x32_bf16 v[228:231], a[180:183], a[148:151], v[228:231]
v_exp_f32 v158, v158
v_mfma_f32_16x16x32_bf16 v[232:235], a[184:187], a[148:151], 0
ds_read_b64_tr_b16 a[142:143], v238 offset:640
v_fma_f32 v161, v161, s56, v247
v_mfma_f32_16x16x32_bf16 v[232:235], a[168:171], a[144:147], v[232:235]
s_waitcnt lgkmcnt(4)
v_exp_f32 v159, v159
v_mfma_f32_16x16x32_bf16 v[220:223], a[188:191], a[128:131], v[220:223]
s_add_u32 m0, s62, 64
ds_read_b64_tr_b16 a[144:145], v238 offset:8448
v_cvt_pk_bf16_f32 v125, v158, v159
v_mfma_f32_16x16x32_bf16 v[220:223], a[204:207], a[132:135], v[220:223]
buffer_load_dwordx4 v254, s[24:27], s75 offen offset:2048  lds
v_mfma_f32_16x16x32_bf16 v[224:227], a[208:211], a[132:135], v[224:227]
ds_read_b64_tr_b16 a[146:147], v238 offset:8576
v_fma_f32 v162, v162, s56, v247
v_mfma_f32_16x16x32_bf16 v[224:227], a[192:195], a[128:131], v[224:227]
v_exp_f32 v160, v160
v_mfma_f32_16x16x32_bf16 v[228:231], a[196:199], a[128:131], v[228:231]
ds_read_b64_tr_b16 a[148:149], v238 offset:8960
v_fma_f32 v163, v163, s56, v247
v_mfma_f32_16x16x32_bf16 v[228:231], a[212:215], a[132:135], v[228:231]
v_exp_f32 v161, v161
v_mfma_f32_16x16x32_bf16 v[232:235], a[216:219], a[132:135], v[232:235]
ds_read_b64_tr_b16 a[150:151], v238 offset:9088
v_cvt_pk_bf16_f32 v128, v160, v161
v_mfma_f32_16x16x32_bf16 v[232:235], a[200:203], a[128:131], v[232:235]
s_waitcnt lgkmcnt(4)
v_exp_f32 v162, v162
v_mfma_f32_16x16x32_bf16 v[204:207], a[220:223], a[136:139], v[204:207]
s_add_u32 m0, s61, 132736
ds_read_b64_tr_b16 a[128:129], v238 offset:16
v_fma_f32 v164, v164, s56, v248
v_mfma_f32_16x16x32_bf16 v[204:207], a[224:227], a[140:143], v[204:207]
buffer_load_dwordx4 v242, s[40:43], s79 offen offset:128  lds
v_mfma_f32_16x16x32_bf16 v[208:211], v[12:15], a[140:143], v[208:211]
ds_read_b64_tr_b16 a[130:131], v238 offset:144
v_fma_f32 v165, v165, s56, v248
v_mfma_f32_16x16x32_bf16 v[208:211], v[0:3], a[136:139], v[208:211]
v_exp_f32 v163, v163
v_mfma_f32_16x16x32_bf16 v[212:215], v[4:7], a[136:139], v[212:215]
ds_read_b64_tr_b16 a[132:133], v238 offset:528
v_cvt_pk_bf16_f32 v129, v162, v163
v_mfma_f32_16x16x32_bf16 v[212:215], v[16:19], a[140:143], v[212:215]
v_exp_f32 v164, v164
v_mfma_f32_16x16x32_bf16 v[216:219], v[20:23], a[140:143], v[216:219]
ds_read_b64_tr_b16 a[134:135], v238 offset:656
v_fma_f32 v166, v166, s56, v248
v_mfma_f32_16x16x32_bf16 v[216:219], v[8:11], a[136:139], v[216:219]
s_waitcnt lgkmcnt(4)
v_exp_f32 v165, v165
v_mfma_f32_16x16x32_bf16 v[204:207], v[24:27], a[144:147], v[204:207]
s_add_u32 m0, s61, 132752
ds_read_b64_tr_b16 a[136:137], v238 offset:8464
v_fma_f32 v167, v167, s56, v248
v_mfma_f32_16x16x32_bf16 v[204:207], v[36:39], a[148:151], v[204:207]
buffer_load_dwordx4 v242, s[40:43], s79 offen offset:1152  lds
v_mfma_f32_16x16x32_bf16 v[208:211], v[40:43], a[148:151], v[208:211]
ds_read_b64_tr_b16 a[138:139], v238 offset:8592
v_cvt_pk_bf16_f32 v132, v164, v165
v_mfma_f32_16x16x32_bf16 v[208:211], a[228:231], a[144:147], v[208:211]
v_exp_f32 v166, v166
v_mfma_f32_16x16x32_bf16 v[212:215], v[28:31], a[144:147], v[212:215]
ds_read_b64_tr_b16 a[140:141], v238 offset:8976
v_fma_f32 v168, v168, s56, v249
v_mfma_f32_16x16x32_bf16 v[212:215], a[232:235], a[148:151], v[212:215]
v_exp_f32 v167, v167
v_mfma_f32_16x16x32_bf16 v[216:219], v[44:47], a[148:151], v[216:219]
ds_read_b64_tr_b16 a[142:143], v238 offset:9104
v_cvt_pk_bf16_f32 v133, v166, v167
v_mfma_f32_16x16x32_bf16 v[216:219], v[32:35], a[144:147], v[216:219]
s_waitcnt lgkmcnt(4)
v_exp_f32 v168, v168
v_mfma_f32_16x16x32_bf16 v[220:223], a[220:223], a[128:131], v[220:223]
s_add_u32 m0, s62, 96
ds_read_b64_tr_b16 a[144:145], v239 offset:64
v_fma_f32 v169, v169, s56, v249
v_mfma_f32_16x16x32_bf16 v[220:223], a[224:227], a[132:135], v[220:223]
buffer_load_dwordx4 v254, s[24:27], s75 offen offset:3072  lds
v_mfma_f32_16x16x32_bf16 v[224:227], v[12:15], a[132:135], v[224:227]
ds_read_b64_tr_b16 a[146:147], v239 offset:192
v_fma_f32 v170, v170, s56, v249
v_mfma_f32_16x16x32_bf16 v[224:227], v[0:3], a[128:131], v[224:227]
v_exp_f32 v169, v169
v_mfma_f32_16x16x32_bf16 v[228:231], v[4:7], a[128:131], v[228:231]
ds_read_b64_tr_b16 a[148:149], v239 offset:576
v_fma_f32 v171, v171, s56, v249
v_mfma_f32_16x16x32_bf16 v[228:231], v[16:19], a[132:135], v[228:231]
v_exp_f32 v170, v170
v_mfma_f32_16x16x32_bf16 v[232:235], v[20:23], a[132:135], v[232:235]
ds_read_b64_tr_b16 a[150:151], v239 offset:704
v_cvt_pk_bf16_f32 v136, v168, v169
v_mfma_f32_16x16x32_bf16 v[232:235], v[8:11], a[128:131], v[232:235]
s_waitcnt lgkmcnt(4)
v_exp_f32 v171, v171
v_mfma_f32_16x16x32_bf16 v[220:223], v[24:27], a[136:139], v[220:223]
.Lpad5_%=:
s_cbranch_vccnz .Lret1_%=
ds_read_b64_tr_b16 a[128:129], v239 offset:8512
v_cvt_pk_bf16_f32 v137, v170, v171
v_mfma_f32_16x16x32_bf16 v[220:223], v[36:39], a[140:143], v[220:223]
v_fma_f32 v172, v172, s56, v246
v_fma_f32 v173, v173, s56, v246
v_mfma_f32_16x16x32_bf16 v[224:227], v[40:43], a[140:143], v[224:227]
s_add_u32 m0, s62, 4224
ds_read_b64_tr_b16 a[130:131], v239 offset:8640
v_fma_f32 v174, v174, s56, v246
v_mfma_f32_16x16x32_bf16 v[224:227], a[228:231], a[136:139], v[224:227]
buffer_load_dwordx4 v254, s[24:27], s76 offen offset:0  lds
v_mfma_f32_16x16x32_bf16 v[228:231], v[28:31], a[136:139], v[228:231]
ds_read_b64_tr_b16 a[132:133], v239 offset:9024
v_fma_f32 v175, v175, s56, v246
v_mfma_f32_16x16x32_bf16 v[228:231], a[232:235], a[140:143], v[228:231]
v_exp_f32 v172, v172
v_mfma_f32_16x16x32_bf16 v[232:235], v[44:47], a[140:143], v[232:235]
ds_read_b64_tr_b16 a[134:135], v239 offset:9152
v_fma_f32 v176, v176, s56, v247
v_mfma_f32_16x16x32_bf16 v[232:235], v[32:35], a[136:139], v[232:235]
s_waitcnt lgkmcnt(4)
v_exp_f32 v173, v173
v_mfma_f32_16x16x32_bf16 v[156:159], a[156:159], a[144:147], 0
ds_read_b64_tr_b16 a[136:137], v238 offset:64
v_cvt_pk_bf16_f32 v126, v172, v173
v_mfma_f32_16x16x32_bf16 v[156:159], a[172:175], a[148:151], v[156:159]
v_exp_f32 v174, v174
v_mfma_f32_16x16x32_bf16 v[160:163], a[176:179], a[148:151], 0
ds_read_b64_tr_b16 a[138:139], v238 offset:192
v_fma_f32 v177, v177, s56, v247
v_mfma_f32_16x16x32_bf16 v[160:163], a[160:163], a[144:147], v[160:163]
v_exp_f32 v175, v175
v_mfma_f32_16x16x32_bf16 v[164:167], a[164:167], a[144:147], 0
ds_read_b64_tr_b16 a[140:141], v238 offset:576
v_cvt_pk_bf16_f32 v127, v174, v175
v_mfma_f32_16x16x32_bf16 v[164:167], a[180:183], a[148:151], v[164:167]
v_exp_f32 v176, v176
v_mfma_f32_16x16x32_bf16 v[168:171], a[184:187], a[148:151], 0
ds_read_b64_tr_b16 a[142:143], v238 offset:704
v_fma_f32 v178, v178, s56, v247
v_mfma_f32_16x16x32_bf16 v[168:171], a[168:171], a[144:147], v[168:171]
s_waitcnt lgkmcnt(4)
v_exp_f32 v177, v177
v_mfma_f32_16x16x32_bf16 v[156:159], a[188:191], a[128:131], v[156:159]
ds_read_b64_tr_b16 a[144:145], v238 offset:8512
v_fma_f32 v179, v179, s56, v247
v_mfma_f32_16x16x32_bf16 v[156:159], a[204:207], a[132:135], v[156:159]
v_exp_f32 v178, v178
v_mfma_f32_16x16x32_bf16 v[160:163], a[208:211], a[132:135], v[160:163]
ds_read_b64_tr_b16 a[146:147], v238 offset:8640
v_cvt_pk_bf16_f32 v130, v176, v177
v_mfma_f32_16x16x32_bf16 v[160:163], a[192:195], a[128:131], v[160:163]
v_exp_f32 v179, v179
v_mfma_f32_16x16x32_bf16 v[164:167], a[196:199], a[128:131], v[164:167]
s_add_u32 m0, s62, 4256
ds_read_b64_tr_b16 a[148:149], v238 offset:9024
v_cvt_pk_bf16_f32 v131, v178, v179
v_mfma_f32_16x16x32_bf16 v[164:167], a[212:215], a[132:135], v[164:167]
buffer_load_dwordx4 v254, s[24:27], s76 offen offset:1024  lds
v_mfma_f32_16x16x32_bf16 v[168:171], a[216:219], a[132:135], v[168:171]
ds_read_b64_tr_b16 a[150:151], v238 offset:9152
v_fma_f32 v180, v180, s56, v248
v_mfma_f32_16x16x32_bf16 v[168:171], a[200:203], a[128:131], v[168:171]
s_waitcnt lgkmcnt(4)
v_exp_f32 v180, v180
v_mfma_f32_16x16x32_bf16 v[156:159], a[220:223], a[136:139], v[156:159]
ds_read_b64_tr_b16 a[128:129], v239 offset:80
v_fma_f32 v181, v181, s56, v248
v_mfma_f32_16x16x32_bf16 v[156:159], a[224:227], a[140:143], v[156:159]
v_exp_f32 v181, v181
v_mfma_f32_16x16x32_bf16 v[160:163], v[12:15], a[140:143], v[160:163]
ds_read_b64_tr_b16 a[130:131], v239 offset:208
v_fma_f32 v182, v182, s56, v248
v_mfma_f32_16x16x32_bf16 v[160:163], v[0:3], a[136:139], v[160:163]
v_exp_f32 v182, v182
v_mfma_f32_16x16x32_bf16 v[164:167], v[4:7], a[136:139], v[164:167]
ds_read_b64_tr_b16 a[132:133], v239 offset:592
v_fma_f32 v183, v183, s56, v248
v_mfma_f32_16x16x32_bf16 v[164:167], v[16:19], a[140:143], v[164:167]
v_exp_f32 v183, v183
v_mfma_f32_16x16x32_bf16 v[168:171], v[20:23], a[140:143], v[168:171]
ds_read_b64_tr_b16 a[134:135], v239 offset:720
v_cvt_pk_bf16_f32 v134, v180, v181
v_mfma_f32_16x16x32_bf16 v[168:171], v[8:11], a[136:139], v[168:171]
s_waitcnt lgkmcnt(4)
v_cvt_pk_bf16_f32 v135, v182, v183
v_fma_f32 v184, v184, s56, v249
v_mfma_f32_16x16x32_bf16 v[156:159], v[24:27], a[144:147], v[156:159]
ds_read_b64_tr_b16 a[136:137], v239 offset:8528
v_fma_f32 v185, v185, s56, v249
v_mfma_f32_16x16x32_bf16 v[156:159], v[36:39], a[148:151], v[156:159]
v_exp_f32 v184, v184
v_mfma_f32_16x16x32_bf16 v[160:163], v[40:43], a[148:151], v[160:163]
ds_read_b64_tr_b16 a[138:139], v239 offset:8656
v_fma_f32 v186, v186, s56, v249
v_mfma_f32_16x16x32_bf16 v[160:163], a[228:231], a[144:147], v[160:163]
v_exp_f32 v185, v185
v_mfma_f32_16x16x32_bf16 v[164:167], v[28:31], a[144:147], v[164:167]
s_add_u32 m0, s62, 4288
ds_read_b64_tr_b16 a[140:141], v239 offset:9040
v_fma_f32 v187, v187, s56, v249
v_mfma_f32_16x16x32_bf16 v[164:167], a[232:235], a[148:151], v[164:167]
buffer_load_dwordx4 v254, s[24:27], s76 offen offset:2048  lds
v_mfma_f32_16x16x32_bf16 v[168:171], v[44:47], a[148:151], v[168:171]
ds_read_b64_tr_b16 a[142:143], v239 offset:9168
v_cvt_pk_bf16_f32 v138, v184, v185
v_mfma_f32_16x16x32_bf16 v[168:171], v[32:35], a[144:147], v[168:171]
s_waitcnt lgkmcnt(4)
v_exp_f32 v186, v186
v_mfma_f32_16x16x32_bf16 v[172:175], a[156:159], a[128:131], 0
ds_read_b64_tr_b16 a[144:145], v238 offset:80
v_fma_f32 v140, v140, s56, v246
v_mfma_f32_16x16x32_bf16 v[172:175], a[172:175], a[132:135], v[172:175]
v_exp_f32 v187, v187
)"
R"(
v_mfma_f32_16x16x32_bf16 v[176:179], a[176:179], a[132:135], 0
ds_read_b64_tr_b16 a[146:147], v238 offset:208
v_cvt_pk_bf16_f32 v139, v186, v187
v_mfma_f32_16x16x32_bf16 v[176:179], a[160:163], a[128:131], v[176:179]
v_exp_f32 v140, v140
v_mfma_f32_16x16x32_bf16 v[180:183], a[164:167], a[128:131], 0
ds_read_b64_tr_b16 a[148:149], v238 offset:592
v_fma_f32 v141, v141, s56, v246
v_mfma_f32_16x16x32_bf16 v[180:183], a[180:183], a[132:135], v[180:183]
v_exp_f32 v141, v141
v_mfma_f32_16x16x32_bf16 v[184:187], a[184:187], a[132:135], 0
s_waitcnt vmcnt(15) lgkmcnt(3)
ds_read_b64_tr_b16 a[150:151], v238 offset:720
v_fma_f32 v142, v142, s56, v246
v_mfma_f32_16x16x32_bf16 v[184:187], a[168:171], a[128:131], v[184:187]
s_barrier
v_mfma_f32_16x16x32_bf16 v[172:175], a[188:191], a[136:139], v[172:175]
s_add_u32 m0, s61, 141184
ds_read_b64_tr_b16 a[128:129], v238 offset:16896
v_fma_f32 v143, v143, s56, v246
v_mfma_f32_16x16x32_bf16 v[172:175], a[204:207], a[140:143], v[172:175]
buffer_load_dwordx4 v242, s[40:43], s81 offen offset:128  lds
v_mfma_f32_16x16x32_bf16 v[176:179], a[208:211], a[140:143], v[176:179]
ds_read_b64_tr_b16 a[130:131], v238 offset:17024
v_fma_f32 v188, v188, s56, v246
v_mfma_f32_16x16x32_bf16 v[176:179], a[192:195], a[136:139], v[176:179]
v_exp_f32 v142, v142
v_mfma_f32_16x16x32_bf16 v[180:183], a[196:199], a[136:139], v[180:183]
ds_read_b64_tr_b16 a[132:133], v238 offset:17408
v_fma_f32 v189, v189, s56, v246
v_mfma_f32_16x16x32_bf16 v[180:183], a[212:215], a[140:143], v[180:183]
v_exp_f32 v143, v143
v_mfma_f32_16x16x32_bf16 v[184:187], a[216:219], a[140:143], v[184:187]
ds_read_b64_tr_b16 a[134:135], v238 offset:17536
v_fma_f32 v190, v190, s56, v246
v_mfma_f32_16x16x32_bf16 v[184:187], a[200:203], a[136:139], v[184:187]
s_waitcnt lgkmcnt(4)
v_exp_f32 v188, v188
v_mfma_f32_16x16x32_bf16 v[172:175], a[220:223], a[144:147], v[172:175]
s_add_u32 m0, s62, 4320
v_exp_f32 v189, v189
v_mfma_f32_16x16x32_bf16 v[172:175], a[224:227], a[148:151], v[172:175]
buffer_load_dwordx4 v254, s[24:27], s76 offen offset:3072  lds
v_mfma_f32_16x16x32_bf16 v[176:179], v[12:15], a[148:151], v[176:179]
v_exp_f32 v190, v190
v_mfma_f32_16x16x32_bf16 v[176:179], v[0:3], a[144:147], v[176:179]
v_fma_f32 v191, v191, s56, v246
v_cvt_pk_bf16_f32 v143, v142, v143
v_mfma_f32_16x16x32_bf16 v[180:183], v[4:7], a[144:147], v[180:183]
v_exp_f32 v191, v191
v_mfma_f32_16x16x32_bf16 v[180:183], v[16:19], a[148:151], v[180:183]
v_fma_f32 v144, v144, s56, v247
v_fma_f32 v145, v145, s56, v247
v_mfma_f32_16x16x32_bf16 v[184:187], v[20:23], a[148:151], v[184:187]
v_exp_f32 v144, v144
v_mfma_f32_16x16x32_bf16 v[184:187], v[8:11], a[144:147], v[184:187]
s_waitcnt lgkmcnt(0)
v_exp_f32 v145, v145
v_mfma_f32_16x16x32_bf16 v[204:207], v[48:51], a[128:131], v[204:207]
s_add_u32 m0, s61, 141200
ds_read_b64_tr_b16 a[136:137], v238 offset:16912
v_fma_f32 v146, v146, s56, v247
v_mfma_f32_16x16x32_bf16 v[204:207], v[60:63], a[132:135], v[204:207]
buffer_load_dwordx4 v242, s[40:43], s81 offen offset:1152  lds
v_mfma_f32_16x16x32_bf16 v[208:211], v[64:67], a[132:135], v[208:211]
ds_read_b64_tr_b16 a[138:139], v238 offset:17040
v_fma_f32 v147, v147, s56, v247
v_mfma_f32_16x16x32_bf16 v[208:211], v[52:55], a[128:131], v[208:211]
v_exp_f32 v146, v146
v_mfma_f32_16x16x32_bf16 v[212:215], v[56:59], a[128:131], v[212:215]
ds_read_b64_tr_b16 a[140:141], v238 offset:17424
v_cvt_pk_bf16_f32 v142, v140, v141
v_mfma_f32_16x16x32_bf16 v[212:215], v[68:71], a[132:135], v[212:215]
v_exp_f32 v147, v147
v_mfma_f32_16x16x32_bf16 v[216:219], v[72:75], a[132:135], v[216:219]
ds_read_b64_tr_b16 a[142:143], v238 offset:17552
v_cvt_pk_bf16_f32 v140, v188, v189
v_mfma_f32_16x16x32_bf16 v[216:219], a[236:239], a[128:131], v[216:219]
v_cvt_pk_bf16_f32 v141, v190, v191
v_fma_f32 v192, v192, s56, v247
v_mfma_f32_16x16x32_bf16 a[100:103], a[96:99], v[100:103], a[100:103]
s_add_u32 m0, s61, 149632
ds_read_b64_tr_b16 a[144:145], v238 offset:8528
v_fma_f32 v193, v193, s56, v247
v_mfma_f32_16x16x32_bf16 a[100:103], a[96:99], v[112:115], a[100:103]
buffer_load_dwordx4 v242, s[40:43], s83 offen offset:128  lds
v_mfma_f32_16x16x32_bf16 a[104:107], a[96:99], v[104:107], a[104:107]
ds_read_b64_tr_b16 a[146:147], v238 offset:8656
v_fma_f32 v194, v194, s56, v247
v_mfma_f32_16x16x32_bf16 a[104:107], a[96:99], v[116:119], a[104:107]
v_exp_f32 v192, v192
v_mfma_f32_16x16x32_bf16 a[108:111], a[96:99], a[248:251], a[108:111]
ds_read_b64_tr_b16 a[148:149], v238 offset:9040
v_fma_f32 v195, v195, s56, v247
v_mfma_f32_16x16x32_bf16 a[108:111], a[96:99], v[120:123], a[108:111]
v_exp_f32 v193, v193
v_mfma_f32_16x16x32_bf16 a[112:115], a[96:99], v[108:111], a[112:115]
ds_read_b64_tr_b16 a[150:151], v238 offset:9168
v_cvt_pk_bf16_f32 v147, v146, v147
v_mfma_f32_16x16x32_bf16 a[112:115], a[96:99], a[252:255], a[112:115]
s_waitcnt lgkmcnt(4)
v_exp_f32 v194, v194
v_mfma_f32_16x16x32_bf16 v[220:223], v[48:51], a[136:139], v[220:223]
s_add_u32 m0, s62, 36864
ds_read_b64_tr_b16 a[128:129], v238 offset:25344
v_fma_f32 v148, v148, s56, v248
v_mfma_f32_16x16x32_bf16 v[220:223], v[60:63], a[140:143], v[220:223]
buffer_load_dwordx4 v254, s[28:31], s77 offen offset:0  lds
v_mfma_f32_16x16x32_bf16 v[224:227], v[64:67], a[140:143], v[224:227]
ds_read_b64_tr_b16 a[130:131], v238 offset:25472
v_fma_f32 v149, v149, s56, v248
v_mfma_f32_16x16x32_bf16 v[224:227], v[52:55], a[136:139], v[224:227]
v_exp_f32 v195, v195
v_mfma_f32_16x16x32_bf16 v[228:231], v[56:59], a[136:139], v[228:231]
ds_read_b64_tr_b16 a[132:133], v238 offset:25856
v_fma_f32 v150, v150, s56, v248
v_mfma_f32_16x16x32_bf16 v[228:231], v[68:71], a[140:143], v[228:231]
v_exp_f32 v148, v148
v_mfma_f32_16x16x32_bf16 v[232:235], v[72:75], a[140:143], v[232:235]
ds_read_b64_tr_b16 a[134:135], v238 offset:25984
v_fma_f32 v151, v151, s56, v248
v_mfma_f32_16x16x32_bf16 v[232:235], a[236:239], a[136:139], v[232:235]
s_waitcnt lgkmcnt(4)
v_exp_f32 v149, v149
v_mfma_f32_16x16x32_bf16 v[172:175], v[24:27], a[144:147], v[172:175]
ds_read_b64_tr_b16 a[136:137], v238 offset:16960
v_cvt_pk_bf16_f32 v146, v144, v145
v_mfma_f32_16x16x32_bf16 v[172:175], v[36:39], a[148:151], v[172:175]
v_exp_f32 v150, v150
v_mfma_f32_16x16x32_bf16 v[176:179], v[40:43], a[148:151], v[176:179]
ds_read_b64_tr_b16 a[138:139], v238 offset:17088
v_cvt_pk_bf16_f32 v144, v192, v193
v_mfma_f32_16x16x32_bf16 v[176:179], a[228:231], a[144:147], v[176:179]
v_exp_f32 v151, v151
v_mfma_f32_16x16x32_bf16 v[180:183], v[28:31], a[144:147], v[180:183]
ds_read_b64_tr_b16 a[140:141], v238 offset:17472
v_cvt_pk_bf16_f32 v145, v194, v195
v_mfma_f32_16x16x32_bf16 v[180:183], a[232:235], a[148:151], v[180:183]
v_fma_f32 v196, v196, s56, v248
v_fma_f32 v197, v197, s56, v248
v_mfma_f32_16x16x32_bf16 v[184:187], v[44:47], a[148:151], v[184:187]
s_add_u32 m0, s61, 149648
ds_read_b64_tr_b16 a[142:143], v238 offset:17600
v_fma_f32 v198, v198, s56, v248
v_mfma_f32_16x16x32_bf16 v[184:187], v[32:35], a[144:147], v[184:187]
s_waitcnt lgkmcnt(4)
buffer_load_dwordx4 v242, s[40:43], s83 offen offset:1152  lds
v_mfma_f32_16x16x32_bf16 v[204:207], a[240:243], a[128:131], v[204:207]
ds_read_b64_tr_b16 a[144:145], v238 offset:25360
v_fma_f32 v199, v199, s56, v248
v_mfma_f32_16x16x32_bf16 v[204:207], v[88:91], a[132:135], v[204:207]
v_exp_f32 v196, v196
v_mfma_f32_16x16x32_bf16 v[208:211], a[244:247], a[132:135], v[208:211]
ds_read_b64_tr_b16 a[146:147], v238 offset:25488
v_cvt_pk_bf16_f32 v151, v150, v151
v_mfma_f32_16x16x32_bf16 v[208:211], v[76:79], a[128:131], v[208:211]
v_exp_f32 v197, v197
v_mfma_f32_16x16x32_bf16 v[212:215], v[80:83], a[128:131], v[212:215]
ds_read_b64_tr_b16 a[148:149], v238 offset:25872
v_fma_f32 v152, v152, s56, v249
v_mfma_f32_16x16x32_bf16 v[212:215], v[92:95], a[132:135], v[212:215]
v_exp_f32 v198, v198
v_mfma_f32_16x16x32_bf16 v[216:219], v[96:99], a[132:135], v[216:219]
s_add_u32 m0, s62, 36896
ds_read_b64_tr_b16 a[150:151], v238 offset:26000
v_fma_f32 v153, v153, s56, v249
v_mfma_f32_16x16x32_bf16 v[216:219], v[84:87], a[128:131], v[216:219]
s_waitcnt lgkmcnt(4)
buffer_load_dwordx4 v254, s[28:31], s77 offen offset:1024  lds
v_mfma_f32_16x16x32_bf16 v[156:159], v[48:51], a[136:139], v[156:159]
v_exp_f32 v199, v199
v_mfma_f32_16x16x32_bf16 v[156:159], v[60:63], a[140:143], v[156:159]
v_exp_f32 v152, v152
v_mfma_f32_16x16x32_bf16 v[160:163], v[64:67], a[140:143], v[160:163]
v_exp_f32 v153, v153
v_mfma_f32_16x16x32_bf16 v[160:163], v[52:55], a[136:139], v[160:163]
v_fma_f32 v154, v154, s56, v249
v_fma_f32 v155, v155, s56, v249
v_mfma_f32_16x16x32_bf16 v[164:167], v[56:59], a[136:139], v[164:167]
v_exp_f32 v154, v154
v_mfma_f32_16x16x32_bf16 v[164:167], v[68:71], a[140:143], v[164:167]
v_exp_f32 v155, v155
v_mfma_f32_16x16x32_bf16 v[168:171], v[72:75], a[140:143], v[168:171]
s_waitcnt vmcnt(18) lgkmcnt(0)
v_cvt_pk_bf16_f32 v150, v148, v149
v_cvt_pk_bf16_f32 v148, v196, v197
v_mfma_f32_16x16x32_bf16 v[168:171], a[236:239], a[136:139], v[168:171]
s_barrier
v_mfma_f32_16x16x32_bf16 v[220:223], a[240:243], a[144:147], v[220:223]
ds_read_b64_tr_b16 a[128:129], v238 offset:33792
v_cvt_pk_bf16_f32 v149, v198, v199
v_mfma_f32_16x16x32_bf16 v[220:223], v[88:91], a[148:151], v[220:223]
v_fma_f32 v200, v200, s56, v249
v_fma_f32 v201, v201, s56, v249
v_mfma_f32_16x16x32_bf16 v[224:227], a[244:247], a[148:151], v[224:227]
ds_read_b64_tr_b16 a[130:131], v238 offset:33920
v_fma_f32 v202, v202, s56, v249
v_mfma_f32_16x16x32_bf16 v[224:227], v[76:79], a[144:147], v[224:227]
v_exp_f32 v200, v200
v_mfma_f32_16x16x32_bf16 v[228:231], v[80:83], a[144:147], v[228:231]
ds_read_b64_tr_b16 a[132:133], v238 offset:34304
v_fma_f32 v203, v203, s56, v249
v_mfma_f32_16x16x32_bf16 v[228:231], v[92:95], a[148:151], v[228:231]
v_exp_f32 v201, v201
v_mfma_f32_16x16x32_bf16 v[232:235], v[96:99], a[148:151], v[232:235]
s_add_u32 m0, s61, 73600
ds_read_b64_tr_b16 a[134:135], v238 offset:34432
v_cvt_pk_bf16_f32 v155, v154, v155
v_mfma_f32_16x16x32_bf16 v[232:235], v[84:87], a[144:147], v[232:235]
buffer_load_dwordx4 v242, s[40:43], s85 offen offset:128  lds
v_mfma_f32_16x16x32_bf16 a[100:103], a[96:99], v[124:127], a[100:103]
ds_read_b64_tr_b16 a[136:137], v238 offset:16976
v_cvt_pk_bf16_f32 v154, v152, v153
v_mfma_f32_16x16x32_bf16 a[100:103], a[96:99], v[140:143], a[100:103]
v_exp_f32 v202, v202
v_mfma_f32_16x16x32_bf16 a[104:107], a[96:99], v[128:131], a[104:107]
ds_read_b64_tr_b16 a[138:139], v238 offset:17104
v_cvt_pk_bf16_f32 v152, v200, v201
v_mfma_f32_16x16x32_bf16 a[104:107], a[96:99], v[144:147], a[104:107]
v_exp_f32 v203, v203
v_mfma_f32_16x16x32_bf16 a[108:111], a[96:99], v[132:135], a[108:111]
ds_read_b64_tr_b16 a[140:141], v238 offset:17488
v_cvt_pk_bf16_f32 v153, v202, v203
v_mfma_f32_16x16x32_bf16 a[108:111], a[96:99], v[148:151], a[108:111]
v_accvgpr_read_b32 v250, a100
v_mfma_f32_16x16x32_bf16 a[112:115], a[96:99], v[136:139], a[112:115]
ds_read_b64_tr_b16 a[142:143], v238 offset:17616
v_accvgpr_read_b32 v251, a104
v_mfma_f32_16x16x32_bf16 a[112:115], a[96:99], v[152:155], a[112:115]
s_waitcnt lgkmcnt(4)
v_mfma_f32_16x16x32_bf16 v[204:207], v[100:103], a[128:131], v[204:207]
ds_read_b64_tr_b16 a[144:145], v238 offset:25408
v_mfma_f32_16x16x32_bf16 v[204:207], v[112:115], a[132:135], v[204:207]
v_accvgpr_read_b32 v252, a108
v_maximum3_f32 v246, v250, v251, v252
v_mfma_f32_16x16x32_bf16 v[208:211], v[116:119], a[132:135], v[208:211]
s_add_u32 m0, s62, 36928
ds_read_b64_tr_b16 a[146:147], v238 offset:25536
v_accvgpr_read_b32 v253, a112
v_mfma_f32_16x16x32_bf16 v[208:211], v[104:107], a[128:131], v[208:211]
buffer_load_dwordx4 v254, s[28:31], s77 offen offset:2048  lds
v_mfma_f32_16x16x32_bf16 v[212:215], a[248:251], a[128:131], v[212:215]
ds_read_b64_tr_b16 a[148:149], v238 offset:25920
v_maximum3_f32 v246, v246, v253, v253
v_mfma_f32_16x16x32_bf16 v[212:215], v[120:123], a[132:135], v[212:215]
v_rcp_f32 v250, v250
v_mfma_f32_16x16x32_bf16 v[216:219], a[252:255], a[132:135], v[216:219]
ds_read_b64_tr_b16 a[150:151], v238 offset:26048
v_mfma_f32_16x16x32_bf16 v[216:219], v[108:111], a[128:131], v[216:219]
)"
R"(
s_waitcnt lgkmcnt(4)
v_rcp_f32 v251, v251
v_mfma_f32_16x16x32_bf16 v[172:175], v[48:51], a[136:139], v[172:175]
ds_read_b64_tr_b16 a[128:129], v238 offset:33808
v_maximum3_f32 v246, v246, v250, v251
v_mfma_f32_16x16x32_bf16 v[172:175], v[60:63], a[140:143], v[172:175]
v_rcp_f32 v252, v252
v_mfma_f32_16x16x32_bf16 v[176:179], v[64:67], a[140:143], v[176:179]
ds_read_b64_tr_b16 a[130:131], v238 offset:33936
v_mov_b32_dpp v250, v250 row_shl:4 row_mask:0x2 bank_mask:0xf
v_mfma_f32_16x16x32_bf16 v[176:179], v[52:55], a[136:139], v[176:179]
v_rcp_f32 v253, v253
v_mfma_f32_16x16x32_bf16 v[180:183], v[56:59], a[136:139], v[180:183]
ds_read_b64_tr_b16 a[132:133], v238 offset:34320
v_maximum3_f32 v246, v246, v252, v253
v_mfma_f32_16x16x32_bf16 v[180:183], v[68:71], a[140:143], v[180:183]
s_waitcnt vmcnt(7)
ds_read_b128 a[64:67], v240 offset:0
v_mov_b32_dpp v250, v250 row_shl:8 row_mask:0x4 bank_mask:0xf
v_mfma_f32_16x16x32_bf16 v[184:187], v[72:75], a[140:143], v[184:187]
ds_read_b64_tr_b16 a[134:135], v238 offset:34448
v_mov_b32_dpp v251, v251 row_shl:4 row_mask:0x2 bank_mask:0xf
v_mfma_f32_16x16x32_bf16 v[184:187], a[236:239], a[136:139], v[184:187]
s_waitcnt lgkmcnt(5)
ds_read_b128 a[68:71], v240 offset:16
v_mov_b32_dpp v250, v250 row_shl:12 row_mask:0x8 bank_mask:0xf
v_mfma_f32_16x16x32_bf16 v[156:159], a[240:243], a[144:147], v[156:159]
ds_read_b64_tr_b16 a[136:137], v238 offset:42240
v_mov_b32_dpp v251, v251 row_shl:8 row_mask:0x4 bank_mask:0xf
v_mfma_f32_16x16x32_bf16 v[156:159], v[88:91], a[148:151], v[156:159]
ds_read_b128 a[72:75], v240 offset:32
v_mov_b32_dpp v252, v252 row_shl:4 row_mask:0x2 bank_mask:0xf
v_mfma_f32_16x16x32_bf16 v[160:163], a[244:247], a[148:151], v[160:163]
ds_read_b64_tr_b16 a[138:139], v238 offset:42368
v_mov_b32_dpp v251, v251 row_shl:12 row_mask:0x8 bank_mask:0xf
v_mfma_f32_16x16x32_bf16 v[160:163], v[76:79], a[144:147], v[160:163]
ds_read_b128 a[76:79], v240 offset:48
v_mov_b32_dpp v252, v252 row_shl:8 row_mask:0x4 bank_mask:0xf
v_mfma_f32_16x16x32_bf16 v[164:167], v[80:83], a[144:147], v[164:167]
ds_read_b64_tr_b16 a[140:141], v238 offset:42752
v_mov_b32_dpp v253, v253 row_shl:4 row_mask:0x2 bank_mask:0xf
v_mfma_f32_16x16x32_bf16 v[164:167], v[92:95], a[148:151], v[164:167]
ds_read_b128 a[80:83], v240 offset:64
v_mov_b32_dpp v252, v252 row_shl:12 row_mask:0x8 bank_mask:0xf
v_mfma_f32_16x16x32_bf16 v[168:171], v[96:99], a[148:151], v[168:171]
ds_read_b64_tr_b16 a[142:143], v238 offset:42880
v_mov_b32_dpp v253, v253 row_shl:8 row_mask:0x4 bank_mask:0xf
v_mfma_f32_16x16x32_bf16 v[168:171], v[84:87], a[144:147], v[168:171]
s_waitcnt lgkmcnt(8)
ds_read_b128 a[84:87], v240 offset:80
v_mov_b32_dpp v253, v253 row_shl:12 row_mask:0x8 bank_mask:0xf
v_mfma_f32_16x16x32_bf16 v[220:223], v[100:103], a[128:131], v[220:223]
ds_read_b64_tr_b16 a[144:145], v238 offset:25424
v_cmp_nle_f32 s[64:65], v246, s58
v_mfma_f32_16x16x32_bf16 v[220:223], v[112:115], a[132:135], v[220:223]
ds_read_b128 a[88:91], v240 offset:96
v_mfma_f32_16x16x32_bf16 v[224:227], v[116:119], a[132:135], v[224:227]
ds_read_b64_tr_b16 a[146:147], v238 offset:25552
v_mfma_f32_16x16x32_bf16 v[224:227], v[104:107], a[128:131], v[224:227]
ds_read_b128 a[92:95], v240 offset:112
v_mfma_f32_16x16x32_bf16 v[228:231], a[248:251], a[128:131], v[228:231]
s_add_u32 m0, s61, 73616
ds_read_b64_tr_b16 a[148:149], v238 offset:25936
v_mfma_f32_16x16x32_bf16 v[228:231], v[120:123], a[132:135], v[228:231]
buffer_load_dwordx4 v242, s[40:43], s85 offen offset:1152  lds
v_mfma_f32_16x16x32_bf16 v[232:235], a[252:255], a[132:135], v[232:235]
ds_read_b64_tr_b16 a[150:151], v238 offset:26064
v_mfma_f32_16x16x32_bf16 v[232:235], v[108:111], a[128:131], v[232:235]
s_waitcnt lgkmcnt(7)
v_mfma_f32_16x16x32_bf16 v[204:207], v[124:127], a[136:139], v[204:207]
ds_read_b64_tr_b16 a[128:129], v238 offset:33856
v_mfma_f32_16x16x32_bf16 v[204:207], v[140:143], a[140:143], v[204:207]
v_mfma_f32_16x16x32_bf16 v[208:211], v[144:147], a[140:143], v[208:211]
ds_read_b64_tr_b16 a[130:131], v238 offset:33984
v_mfma_f32_16x16x32_bf16 v[208:211], v[128:131], a[136:139], v[208:211]
v_mfma_f32_16x16x32_bf16 v[212:215], v[132:135], a[136:139], v[212:215]
s_add_u32 m0, s62, 36960
ds_read_b64_tr_b16 a[132:133], v238 offset:34368
v_mfma_f32_16x16x32_bf16 v[212:215], v[148:151], a[140:143], v[212:215]
buffer_load_dwordx4 v254, s[28:31], s77 offen offset:3072  lds
v_mfma_f32_16x16x32_bf16 v[216:219], v[152:155], a[140:143], v[216:219]
ds_read_b64_tr_b16 a[134:135], v238 offset:34496
v_mul_f32_dpp v204, v250, v204 row_newbcast:0 row_mask:0xf bank_mask:0xf
v_mfma_f32_16x16x32_bf16 v[216:219], v[136:139], a[136:139], v[216:219]
s_waitcnt lgkmcnt(4)
v_mul_f32_dpp v205, v250, v205 row_newbcast:1 row_mask:0xf bank_mask:0xf
v_mul_f32_dpp v206, v250, v206 row_newbcast:2 row_mask:0xf bank_mask:0xf
v_mfma_f32_16x16x32_bf16 v[172:175], a[240:243], a[144:147], v[172:175]
ds_read_b64_tr_b16 a[136:137], v238 offset:42256
v_mul_f32_dpp v207, v250, v207 row_newbcast:3 row_mask:0xf bank_mask:0xf
v_mfma_f32_16x16x32_bf16 v[172:175], v[88:91], a[148:151], v[172:175]
v_mul_f32_dpp v208, v251, v208 row_newbcast:0 row_mask:0xf bank_mask:0xf
v_mul_f32_dpp v209, v251, v209 row_newbcast:1 row_mask:0xf bank_mask:0xf
v_mfma_f32_16x16x32_bf16 v[176:179], a[244:247], a[148:151], v[176:179]
ds_read_b64_tr_b16 a[138:139], v238 offset:42384
v_mul_f32_dpp v210, v251, v210 row_newbcast:2 row_mask:0xf bank_mask:0xf
v_mfma_f32_16x16x32_bf16 v[176:179], v[76:79], a[144:147], v[176:179]
v_mul_f32_dpp v211, v251, v211 row_newbcast:3 row_mask:0xf bank_mask:0xf
v_mul_f32_dpp v212, v252, v212 row_newbcast:0 row_mask:0xf bank_mask:0xf
v_mfma_f32_16x16x32_bf16 v[180:183], v[80:83], a[144:147], v[180:183]
s_add_u32 m0, s61, 82048
ds_read_b64_tr_b16 a[140:141], v238 offset:42768
v_mul_f32_dpp v213, v252, v213 row_newbcast:1 row_mask:0xf bank_mask:0xf
v_mfma_f32_16x16x32_bf16 v[180:183], v[92:95], a[148:151], v[180:183]
buffer_load_dwordx4 v242, s[40:43], s87 offen offset:128  lds
v_mfma_f32_16x16x32_bf16 v[184:187], v[96:99], a[148:151], v[184:187]
s_waitcnt lgkmcnt(3)
ds_read_b64_tr_b16 a[142:143], v238 offset:42896
v_mul_f32_dpp v214, v252, v214 row_newbcast:2 row_mask:0xf bank_mask:0xf
v_mfma_f32_16x16x32_bf16 v[184:187], v[84:87], a[144:147], v[184:187]
s_barrier
v_mfma_f32_16x16x32_bf16 v[156:159], v[100:103], a[128:131], v[156:159]
ds_read_b64_tr_b16 a[144:145], v238 offset:50688
v_mul_f32_dpp v215, v252, v215 row_newbcast:3 row_mask:0xf bank_mask:0xf
v_mfma_f32_16x16x32_bf16 v[156:159], v[112:115], a[132:135], v[156:159]
v_mul_f32_dpp v216, v253, v216 row_newbcast:0 row_mask:0xf bank_mask:0xf
v_mul_f32_dpp v217, v253, v217 row_newbcast:1 row_mask:0xf bank_mask:0xf
v_mfma_f32_16x16x32_bf16 v[160:163], v[116:119], a[132:135], v[160:163]
ds_read_b64_tr_b16 a[146:147], v238 offset:50816
v_mul_f32_dpp v218, v253, v218 row_newbcast:2 row_mask:0xf bank_mask:0xf
v_mfma_f32_16x16x32_bf16 v[160:163], v[104:107], a[128:131], v[160:163]
v_mul_f32_dpp v219, v253, v219 row_newbcast:3 row_mask:0xf bank_mask:0xf
v_mfma_f32_16x16x32_bf16 v[164:167], a[248:251], a[128:131], v[164:167]
s_add_u32 m0, s61, 82064
ds_read_b64_tr_b16 a[148:149], v238 offset:51200
v_mfma_f32_16x16x32_bf16 v[164:167], v[120:123], a[132:135], v[164:167]
buffer_load_dwordx4 v242, s[40:43], s87 offen offset:1152  lds
v_mfma_f32_16x16x32_bf16 v[168:171], a[252:255], a[132:135], v[168:171]
ds_read_b64_tr_b16 a[150:151], v238 offset:51328
v_mfma_f32_16x16x32_bf16 v[168:171], v[108:111], a[128:131], v[168:171]
s_waitcnt lgkmcnt(4)
v_mfma_f32_16x16x32_bf16 v[220:223], v[124:127], a[136:139], v[220:223]
ds_read_b64_tr_b16 a[128:129], v238 offset:33872
v_mfma_f32_16x16x32_bf16 v[220:223], v[140:143], a[140:143], v[220:223]
v_mfma_f32_16x16x32_bf16 v[224:227], v[144:147], a[140:143], v[224:227]
ds_read_b64_tr_b16 a[130:131], v238 offset:34000
v_mfma_f32_16x16x32_bf16 v[224:227], v[128:131], a[136:139], v[224:227]
v_mfma_f32_16x16x32_bf16 v[228:231], v[132:135], a[136:139], v[228:231]
s_add_u32 m0, s62, 41088
ds_read_b64_tr_b16 a[132:133], v238 offset:34384
v_mfma_f32_16x16x32_bf16 v[228:231], v[148:151], a[140:143], v[228:231]
buffer_load_dwordx4 v254, s[28:31], s78 offen offset:0  lds
v_mfma_f32_16x16x32_bf16 v[232:235], v[152:155], a[140:143], v[232:235]
ds_read_b64_tr_b16 a[134:135], v238 offset:34512
v_mul_f32_dpp v220, v250, v220 row_newbcast:0 row_mask:0xf bank_mask:0xf
v_mfma_f32_16x16x32_bf16 v[232:235], v[136:139], a[136:139], v[232:235]
s_waitcnt lgkmcnt(4)
v_mul_f32_dpp v221, v250, v221 row_newbcast:1 row_mask:0xf bank_mask:0xf
v_mul_f32_dpp v222, v250, v222 row_newbcast:2 row_mask:0xf bank_mask:0xf
v_mfma_f32_16x16x32_bf16 v[188:191], a[156:159], a[144:147], 0
ds_read_b64_tr_b16 a[136:137], v238 offset:42304
v_mul_f32_dpp v223, v250, v223 row_newbcast:3 row_mask:0xf bank_mask:0xf
v_mfma_f32_16x16x32_bf16 v[188:191], a[172:175], a[148:151], v[188:191]
v_cvt_pk_bf16_f32 v220, v204, v220
v_cvt_pk_bf16_f32 v221, v205, v221
v_mfma_f32_16x16x32_bf16 v[192:195], a[176:179], a[148:151], 0
ds_read_b64_tr_b16 a[138:139], v238 offset:42432
v_cvt_pk_bf16_f32 v222, v206, v222
v_mfma_f32_16x16x32_bf16 v[192:195], a[160:163], a[144:147], v[192:195]
v_cvt_pk_bf16_f32 v223, v207, v223
v_mul_f32_dpp v224, v251, v224 row_newbcast:0 row_mask:0xf bank_mask:0xf
v_mfma_f32_16x16x32_bf16 v[196:199], a[164:167], a[144:147], 0
ds_read_b64_tr_b16 a[140:141], v238 offset:42816
v_mul_f32_dpp v225, v251, v225 row_newbcast:1 row_mask:0xf bank_mask:0xf
v_mfma_f32_16x16x32_bf16 v[196:199], a[180:183], a[148:151], v[196:199]
v_mul_f32_dpp v226, v251, v226 row_newbcast:2 row_mask:0xf bank_mask:0xf
v_mul_f32_dpp v227, v251, v227 row_newbcast:3 row_mask:0xf bank_mask:0xf
v_mfma_f32_16x16x32_bf16 v[200:203], a[184:187], a[148:151], 0
ds_read_b64_tr_b16 a[142:143], v238 offset:42944
v_cvt_pk_bf16_f32 v224, v208, v224
v_mfma_f32_16x16x32_bf16 v[200:203], a[168:171], a[144:147], v[200:203]
s_waitcnt lgkmcnt(4)
v_cvt_pk_bf16_f32 v225, v209, v225
v_cvt_pk_bf16_f32 v226, v210, v226
v_mfma_f32_16x16x32_bf16 v[172:175], v[100:103], a[128:131], v[172:175]
ds_read_b64_tr_b16 a[144:145], v238 offset:50704
v_cvt_pk_bf16_f32 v227, v211, v227
v_mfma_f32_16x16x32_bf16 v[172:175], v[112:115], a[132:135], v[172:175]
v_mul_f32_dpp v228, v252, v228 row_newbcast:0 row_mask:0xf bank_mask:0xf
v_mul_f32_dpp v229, v252, v229 row_newbcast:1 row_mask:0xf bank_mask:0xf
v_mfma_f32_16x16x32_bf16 v[176:179], v[116:119], a[132:135], v[176:179]
s_add_u32 m0, s61, 90496
ds_read_b64_tr_b16 a[146:147], v238 offset:50832
v_mul_f32_dpp v230, v252, v230 row_newbcast:2 row_mask:0xf bank_mask:0xf
v_mfma_f32_16x16x32_bf16 v[176:179], v[104:107], a[128:131], v[176:179]
buffer_load_dwordx4 v242, s[40:43], s89 offen offset:128  lds
v_mfma_f32_16x16x32_bf16 v[180:183], a[248:251], a[128:131], v[180:183]
ds_read_b64_tr_b16 a[148:149], v238 offset:51216
v_mul_f32_dpp v231, v252, v231 row_newbcast:3 row_mask:0xf bank_mask:0xf
v_mfma_f32_16x16x32_bf16 v[180:183], v[120:123], a[132:135], v[180:183]
.Lpad6_%=:
s_cbranch_vccnz .Lret1_%=
v_cvt_pk_bf16_f32 v228, v212, v228
v_cvt_pk_bf16_f32 v229, v213, v229
v_mfma_f32_16x16x32_bf16 v[184:187], a[252:255], a[132:135], v[184:187]
ds_read_b64_tr_b16 a[150:151], v238 offset:51344
v_cvt_pk_bf16_f32 v230, v214, v230
v_mfma_f32_16x16x32_bf16 v[184:187], v[108:111], a[128:131], v[184:187]
s_waitcnt lgkmcnt(4)
v_cvt_pk_bf16_f32 v231, v215, v231
v_mul_f32_dpp v232, v253, v232 row_newbcast:0 row_mask:0xf bank_mask:0xf
v_mfma_f32_16x16x32_bf16 v[156:159], v[124:127], a[136:139], v[156:159]
ds_read_b64_tr_b16 a[128:129], v238 offset:59136
v_mul_f32_dpp v233, v253, v233 row_newbcast:1 row_mask:0xf bank_mask:0xf
v_mfma_f32_16x16x32_bf16 v[156:159], v[140:143], a[140:143], v[156:159]
v_mul_f32_dpp v234, v253, v234 row_newbcast:2 row_mask:0xf bank_mask:0xf
v_mul_f32_dpp v235, v253, v235 row_newbcast:3 row_mask:0xf bank_mask:0xf
v_mfma_f32_16x16x32_bf16 v[160:163], v[144:147], a[140:143], v[160:163]
ds_read_b64_tr_b16 a[130:131], v238 offset:59264
v_cvt_pk_bf16_f32 v232, v216, v232
)"
R"(
v_mfma_f32_16x16x32_bf16 v[160:163], v[128:131], a[136:139], v[160:163]
v_cvt_pk_bf16_f32 v233, v217, v233
v_cvt_pk_bf16_f32 v234, v218, v234
v_mfma_f32_16x16x32_bf16 v[164:167], v[132:135], a[136:139], v[164:167]
s_add_u32 m0, s62, 41120
ds_read_b64_tr_b16 a[132:133], v238 offset:59648
v_cvt_pk_bf16_f32 v235, v219, v235
v_mfma_f32_16x16x32_bf16 v[164:167], v[148:151], a[140:143], v[164:167]
buffer_load_dwordx4 v254, s[28:31], s78 offen offset:1024  lds
v_mfma_f32_16x16x32_bf16 v[168:171], v[152:155], a[140:143], v[168:171]
ds_read_b64_tr_b16 a[134:135], v238 offset:59776
v_mul_f32_dpp v156, v250, v156 row_newbcast:0 row_mask:0xf bank_mask:0xf
v_mfma_f32_16x16x32_bf16 v[168:171], v[136:139], a[136:139], v[168:171]
s_waitcnt lgkmcnt(4)
ds_write_b64 v244, v[220:221] offset:0
v_mfma_f32_16x16x32_bf16 v[204:207], a[156:159], a[144:147], 0
ds_read_b64_tr_b16 a[136:137], v238 offset:42320
v_mfma_f32_16x16x32_bf16 v[204:207], a[172:175], a[148:151], v[204:207]
ds_write_b64 v244, v[222:223] offset:8
v_mfma_f32_16x16x32_bf16 v[208:211], a[176:179], a[148:151], 0
ds_read_b64_tr_b16 a[138:139], v238 offset:42448
v_mfma_f32_16x16x32_bf16 v[208:211], a[160:163], a[144:147], v[208:211]
ds_write_b64 v244, v[224:225] offset:64
v_mfma_f32_16x16x32_bf16 v[212:215], a[164:167], a[144:147], 0
ds_read_b64_tr_b16 a[140:141], v238 offset:42832
v_mfma_f32_16x16x32_bf16 v[212:215], a[180:183], a[148:151], v[212:215]
ds_write_b64 v244, v[226:227] offset:72
v_mfma_f32_16x16x32_bf16 v[216:219], a[184:187], a[148:151], 0
ds_read_b64_tr_b16 a[142:143], v238 offset:42960
v_mfma_f32_16x16x32_bf16 v[216:219], a[168:171], a[144:147], v[216:219]
s_waitcnt lgkmcnt(8)
ds_write_b64 v244, v[228:229] offset:128
v_mfma_f32_16x16x32_bf16 v[188:191], a[188:191], a[128:131], v[188:191]
ds_read_b64_tr_b16 a[144:145], v238 offset:50752
v_mfma_f32_16x16x32_bf16 v[188:191], a[204:207], a[132:135], v[188:191]
ds_write_b64 v244, v[230:231] offset:136
v_mfma_f32_16x16x32_bf16 v[192:195], a[208:211], a[132:135], v[192:195]
s_add_u32 m0, s61, 90512
ds_read_b64_tr_b16 a[146:147], v238 offset:50880
v_mfma_f32_16x16x32_bf16 v[192:195], a[192:195], a[128:131], v[192:195]
buffer_load_dwordx4 v242, s[40:43], s89 offen offset:1152  lds
v_mfma_f32_16x16x32_bf16 v[196:199], a[196:199], a[128:131], v[196:199]
ds_read_b64_tr_b16 a[148:149], v238 offset:51264
v_mul_f32_dpp v157, v250, v157 row_newbcast:1 row_mask:0xf bank_mask:0xf
v_mfma_f32_16x16x32_bf16 v[196:199], a[212:215], a[132:135], v[196:199]
ds_write_b64 v244, v[232:233] offset:192
v_mfma_f32_16x16x32_bf16 v[200:203], a[216:219], a[132:135], v[200:203]
ds_read_b64_tr_b16 a[150:151], v238 offset:51392
v_mfma_f32_16x16x32_bf16 v[200:203], a[200:203], a[128:131], v[200:203]
s_waitcnt lgkmcnt(7)
ds_write_b64 v244, v[234:235] offset:200
v_mfma_f32_16x16x32_bf16 v[172:175], v[124:127], a[136:139], v[172:175]
ds_read_b64_tr_b16 a[128:129], v238 offset:59152
v_mfma_f32_16x16x32_bf16 v[172:175], v[140:143], a[140:143], v[172:175]
v_mul_f32_dpp v158, v250, v158 row_newbcast:2 row_mask:0xf bank_mask:0xf
v_mul_f32_dpp v159, v250, v159 row_newbcast:3 row_mask:0xf bank_mask:0xf
v_mfma_f32_16x16x32_bf16 v[176:179], v[144:147], a[140:143], v[176:179]
s_add_u32 m0, s61, 98944
ds_read_b64_tr_b16 a[130:131], v238 offset:59280
v_mul_f32_dpp v160, v251, v160 row_newbcast:0 row_mask:0xf bank_mask:0xf
v_mfma_f32_16x16x32_bf16 v[176:179], v[128:131], a[136:139], v[176:179]
buffer_load_dwordx4 v242, s[40:43], s91 offen offset:128  lds
v_mfma_f32_16x16x32_bf16 v[180:183], v[132:135], a[136:139], v[180:183]
ds_read_b64_tr_b16 a[132:133], v238 offset:59664
v_mul_f32_dpp v161, v251, v161 row_newbcast:1 row_mask:0xf bank_mask:0xf
v_mfma_f32_16x16x32_bf16 v[180:183], v[148:151], a[140:143], v[180:183]
v_mul_f32_dpp v162, v251, v162 row_newbcast:2 row_mask:0xf bank_mask:0xf
v_mul_f32_dpp v163, v251, v163 row_newbcast:3 row_mask:0xf bank_mask:0xf
v_mfma_f32_16x16x32_bf16 v[184:187], v[152:155], a[140:143], v[184:187]
s_waitcnt vmcnt(12) lgkmcnt(4)
ds_read_b64_tr_b16 a[134:135], v238 offset:59792
v_mul_f32_dpp v164, v252, v164 row_newbcast:0 row_mask:0xf bank_mask:0xf
v_mfma_f32_16x16x32_bf16 v[184:187], v[136:139], a[136:139], v[184:187]
s_barrier
v_mfma_f32_16x16x32_bf16 v[220:223], a[156:159], a[144:147], 0
ds_read_b64_tr_b16 a[136:137], v239 offset:0
v_mul_f32_dpp v165, v252, v165 row_newbcast:1 row_mask:0xf bank_mask:0xf
v_mfma_f32_16x16x32_bf16 v[220:223], a[172:175], a[148:151], v[220:223]
v_mul_f32_dpp v166, v252, v166 row_newbcast:2 row_mask:0xf bank_mask:0xf
v_mul_f32_dpp v167, v252, v167 row_newbcast:3 row_mask:0xf bank_mask:0xf
v_mfma_f32_16x16x32_bf16 v[224:227], a[176:179], a[148:151], 0
s_add_u32 m0, s62, 41152
ds_read_b64_tr_b16 a[138:139], v239 offset:128
v_mul_f32_dpp v168, v253, v168 row_newbcast:0 row_mask:0xf bank_mask:0xf
v_mfma_f32_16x16x32_bf16 v[224:227], a[160:163], a[144:147], v[224:227]
buffer_load_dwordx4 v254, s[28:31], s78 offen offset:2048  lds
v_mfma_f32_16x16x32_bf16 v[228:231], a[164:167], a[144:147], 0
ds_read_b64_tr_b16 a[140:141], v239 offset:512
v_mul_f32_dpp v169, v253, v169 row_newbcast:1 row_mask:0xf bank_mask:0xf
v_mfma_f32_16x16x32_bf16 v[228:231], a[180:183], a[148:151], v[228:231]
v_mul_f32_dpp v170, v253, v170 row_newbcast:2 row_mask:0xf bank_mask:0xf
v_mul_f32_dpp v171, v253, v171 row_newbcast:3 row_mask:0xf bank_mask:0xf
v_mfma_f32_16x16x32_bf16 v[232:235], a[184:187], a[148:151], 0
ds_read_b64_tr_b16 a[142:143], v239 offset:640
v_mul_f32_dpp v172, v250, v172 row_newbcast:0 row_mask:0xf bank_mask:0xf
v_mfma_f32_16x16x32_bf16 v[232:235], a[168:171], a[144:147], v[232:235]
s_waitcnt lgkmcnt(4)
v_mul_f32_dpp v173, v250, v173 row_newbcast:1 row_mask:0xf bank_mask:0xf
v_mul_f32_dpp v174, v250, v174 row_newbcast:2 row_mask:0xf bank_mask:0xf
v_mfma_f32_16x16x32_bf16 v[204:207], a[188:191], a[128:131], v[204:207]
ds_read_b64_tr_b16 a[144:145], v238 offset:50768
v_mul_f32_dpp v175, v250, v175 row_newbcast:3 row_mask:0xf bank_mask:0xf
v_mfma_f32_16x16x32_bf16 v[204:207], a[204:207], a[132:135], v[204:207]
v_cvt_pk_bf16_f32 v172, v156, v172
v_cvt_pk_bf16_f32 v173, v157, v173
v_mfma_f32_16x16x32_bf16 v[208:211], a[208:211], a[132:135], v[208:211]
ds_read_b64_tr_b16 a[146:147], v238 offset:50896
v_cvt_pk_bf16_f32 v174, v158, v174
v_mfma_f32_16x16x32_bf16 v[208:211], a[192:195], a[128:131], v[208:211]
v_cvt_pk_bf16_f32 v175, v159, v175
v_mul_f32_dpp v176, v251, v176 row_newbcast:0 row_mask:0xf bank_mask:0xf
v_mfma_f32_16x16x32_bf16 v[212:215], a[196:199], a[128:131], v[212:215]
ds_read_b64_tr_b16 a[148:149], v238 offset:51280
v_mul_f32_dpp v177, v251, v177 row_newbcast:1 row_mask:0xf bank_mask:0xf
v_mfma_f32_16x16x32_bf16 v[212:215], a[212:215], a[132:135], v[212:215]
v_mul_f32_dpp v178, v251, v178 row_newbcast:2 row_mask:0xf bank_mask:0xf
v_mul_f32_dpp v179, v251, v179 row_newbcast:3 row_mask:0xf bank_mask:0xf
v_mfma_f32_16x16x32_bf16 v[216:219], a[216:219], a[132:135], v[216:219]
ds_read_b64_tr_b16 a[150:151], v238 offset:51408
v_cvt_pk_bf16_f32 v176, v160, v176
v_mfma_f32_16x16x32_bf16 v[216:219], a[200:203], a[128:131], v[216:219]
s_waitcnt lgkmcnt(4)
v_cvt_pk_bf16_f32 v177, v161, v177
v_cvt_pk_bf16_f32 v178, v162, v178
v_mfma_f32_16x16x32_bf16 v[188:191], a[220:223], a[136:139], v[188:191]
s_add_u32 m0, s61, 98960
ds_read_b64_tr_b16 a[128:129], v238 offset:59200
v_cvt_pk_bf16_f32 v179, v163, v179
v_mfma_f32_16x16x32_bf16 v[188:191], a[224:227], a[140:143], v[188:191]
buffer_load_dwordx4 v242, s[40:43], s91 offen offset:1152  lds
v_mfma_f32_16x16x32_bf16 v[192:195], v[12:15], a[140:143], v[192:195]
ds_read_b64_tr_b16 a[130:131], v238 offset:59328
v_mul_f32_dpp v180, v252, v180 row_newbcast:0 row_mask:0xf bank_mask:0xf
v_mfma_f32_16x16x32_bf16 v[192:195], v[0:3], a[136:139], v[192:195]
v_mul_f32_dpp v181, v252, v181 row_newbcast:1 row_mask:0xf bank_mask:0xf
v_mul_f32_dpp v182, v252, v182 row_newbcast:2 row_mask:0xf bank_mask:0xf
v_mfma_f32_16x16x32_bf16 v[196:199], v[4:7], a[136:139], v[196:199]
ds_read_b64_tr_b16 a[132:133], v238 offset:59712
v_mul_f32_dpp v183, v252, v183 row_newbcast:3 row_mask:0xf bank_mask:0xf
v_mfma_f32_16x16x32_bf16 v[196:199], v[16:19], a[140:143], v[196:199]
v_cvt_pk_bf16_f32 v180, v164, v180
v_cvt_pk_bf16_f32 v181, v165, v181
v_mfma_f32_16x16x32_bf16 v[200:203], v[20:23], a[140:143], v[200:203]
ds_read_b64_tr_b16 a[134:135], v238 offset:59840
v_cvt_pk_bf16_f32 v182, v166, v182
v_mfma_f32_16x16x32_bf16 v[200:203], v[8:11], a[136:139], v[200:203]
s_waitcnt lgkmcnt(4)
v_cvt_pk_bf16_f32 v183, v167, v183
v_mul_f32_dpp v184, v253, v184 row_newbcast:0 row_mask:0xf bank_mask:0xf
v_mfma_f32_16x16x32_bf16 v[156:159], a[156:159], a[144:147], 0
ds_read_b64_tr_b16 a[136:137], v239 offset:16
v_mul_f32_dpp v185, v253, v185 row_newbcast:1 row_mask:0xf bank_mask:0xf
v_mfma_f32_16x16x32_bf16 v[156:159], a[172:175], a[148:151], v[156:159]
v_mul_f32_dpp v186, v253, v186 row_newbcast:2 row_mask:0xf bank_mask:0xf
v_mul_f32_dpp v187, v253, v187 row_newbcast:3 row_mask:0xf bank_mask:0xf
v_mfma_f32_16x16x32_bf16 v[160:163], a[176:179], a[148:151], 0
ds_read_b64_tr_b16 a[138:139], v239 offset:144
v_cvt_pk_bf16_f32 v184, v168, v184
v_mfma_f32_16x16x32_bf16 v[160:163], a[160:163], a[144:147], v[160:163]
v_cvt_pk_bf16_f32 v185, v169, v185
v_cvt_pk_bf16_f32 v186, v170, v186
v_mfma_f32_16x16x32_bf16 v[164:167], a[164:167], a[144:147], 0
s_add_u32 m0, s62, 41184
ds_read_b64_tr_b16 a[140:141], v239 offset:528
v_cvt_pk_bf16_f32 v187, v171, v187
v_mfma_f32_16x16x32_bf16 v[164:167], a[180:183], a[148:151], v[164:167]
buffer_load_dwordx4 v254, s[28:31], s78 offen offset:3072  lds
v_mfma_f32_16x16x32_bf16 v[168:171], a[184:187], a[148:151], 0
ds_read_b64_tr_b16 a[142:143], v239 offset:656
v_mfma_f32_16x16x32_bf16 v[168:171], a[168:171], a[144:147], v[168:171]
s_waitcnt lgkmcnt(4)
ds_write_b64 v244, v[172:173] offset:4224
v_mfma_f32_16x16x32_bf16 v[220:223], a[188:191], a[128:131], v[220:223]
ds_read_b64_tr_b16 a[144:145], v239 offset:8448
v_mfma_f32_16x16x32_bf16 v[220:223], a[204:207], a[132:135], v[220:223]
ds_write_b64 v244, v[174:175] offset:4232
v_mfma_f32_16x16x32_bf16 v[224:227], a[208:211], a[132:135], v[224:227]
ds_read_b64_tr_b16 a[146:147], v239 offset:8576
v_mfma_f32_16x16x32_bf16 v[224:227], a[192:195], a[128:131], v[224:227]
ds_write_b64 v244, v[176:177] offset:4288
v_mfma_f32_16x16x32_bf16 v[228:231], a[196:199], a[128:131], v[228:231]
ds_read_b64_tr_b16 a[148:149], v239 offset:8960
v_mfma_f32_16x16x32_bf16 v[228:231], a[212:215], a[132:135], v[228:231]
ds_write_b64 v244, v[178:179] offset:4296
v_mfma_f32_16x16x32_bf16 v[232:235], a[216:219], a[132:135], v[232:235]
ds_read_b64_tr_b16 a[150:151], v239 offset:9088
v_mfma_f32_16x16x32_bf16 v[232:235], a[200:203], a[128:131], v[232:235]
s_waitcnt lgkmcnt(8)
ds_write_b64 v244, v[180:181] offset:4352
v_mfma_f32_16x16x32_bf16 v[204:207], a[220:223], a[136:139], v[204:207]
s_add_u32 m0, s61, 107264
ds_read_b64_tr_b16 a[128:129], v238 offset:59216
v_mfma_f32_16x16x32_bf16 v[204:207], a[224:227], a[140:143], v[204:207]
buffer_load_dwordx4 v242, s[40:43], 0 offen offset:256 nt lds
v_mfma_f32_16x16x32_bf16 v[208:211], v[12:15], a[140:143], v[208:211]
ds_read_b64_tr_b16 a[130:131], v238 offset:59344
v_mfma_f32_16x16x32_bf16 v[208:211], v[0:3], a[136:139], v[208:211]
ds_write_b64 v244, v[182:183] offset:4360
v_mfma_f32_16x16x32_bf16 v[212:215], v[4:7], a[136:139], v[212:215]
ds_read_b64_tr_b16 a[132:133], v238 offset:59728
v_mfma_f32_16x16x32_bf16 v[212:215], v[16:19], a[140:143], v[212:215]
ds_write_b64 v244, v[184:185] offset:4416
v_mfma_f32_16x16x32_bf16 v[216:219], v[20:23], a[140:143], v[216:219]
ds_read_b64_tr_b16 a[134:135], v238 offset:59856
v_mfma_f32_16x16x32_bf16 v[216:219], v[8:11], a[136:139], v[216:219]
s_waitcnt lgkmcnt(7)
ds_write_b64 v244, v[186:187] offset:4424
v_mfma_f32_16x16x32_bf16 v[188:191], v[24:27], a[144:147], v[188:191]
ds_read_b64_tr_b16 a[136:137], v239 offset:64
v_mfma_f32_16x16x32_bf16 v[188:191], v[36:39], a[148:151], v[188:191]
v_mfma_f32_16x16x32_bf16 v[192:195], v[40:43], a[148:151], v[192:195]
)"
R"(
ds_read_b64_tr_b16 a[138:139], v239 offset:192
v_mfma_f32_16x16x32_bf16 v[192:195], a[228:231], a[144:147], v[192:195]
v_mfma_f32_16x16x32_bf16 v[196:199], v[28:31], a[144:147], v[196:199]
s_add_u32 m0, s61, 107280
ds_read_b64_tr_b16 a[140:141], v239 offset:576
v_mfma_f32_16x16x32_bf16 v[196:199], a[232:235], a[148:151], v[196:199]
buffer_load_dwordx4 v242, s[40:43], 0 offen offset:1280 nt lds
v_mfma_f32_16x16x32_bf16 v[200:203], v[44:47], a[148:151], v[200:203]
ds_read_b64_tr_b16 a[142:143], v239 offset:704
v_mfma_f32_16x16x32_bf16 v[200:203], v[32:35], a[144:147], v[200:203]
s_waitcnt lgkmcnt(5)
v_mfma_f32_16x16x32_bf16 v[156:159], a[188:191], a[128:131], v[156:159]
ds_read_b64_tr_b16 a[144:145], v239 offset:8464
v_mfma_f32_16x16x32_bf16 v[156:159], a[204:207], a[132:135], v[156:159]
v_mfma_f32_16x16x32_bf16 v[160:163], a[208:211], a[132:135], v[160:163]
ds_read_b64_tr_b16 a[146:147], v239 offset:8592
v_mfma_f32_16x16x32_bf16 v[160:163], a[192:195], a[128:131], v[160:163]
v_mfma_f32_16x16x32_bf16 v[164:167], a[196:199], a[128:131], v[164:167]
s_add_u32 m0, s61, 115712
ds_read_b64_tr_b16 a[148:149], v239 offset:8976
v_mfma_f32_16x16x32_bf16 v[164:167], a[212:215], a[132:135], v[164:167]
buffer_load_dwordx4 v242, s[40:43], s79 offen offset:256 nt lds
v_mfma_f32_16x16x32_bf16 v[168:171], a[216:219], a[132:135], v[168:171]
s_waitcnt vmcnt(11) lgkmcnt(3)
ds_read_b64_tr_b16 a[150:151], v239 offset:9104
v_mfma_f32_16x16x32_bf16 v[168:171], a[200:203], a[128:131], v[168:171]
s_barrier
v_mfma_f32_16x16x32_bf16 v[220:223], a[220:223], a[136:139], v[220:223]
ds_read_b64_tr_b16 a[128:129], v238 offset:0
v_mfma_f32_16x16x32_bf16 v[220:223], a[224:227], a[140:143], v[220:223]
v_mfma_f32_16x16x32_bf16 v[224:227], v[12:15], a[140:143], v[224:227]
ds_read_b64_tr_b16 a[130:131], v238 offset:128
v_mfma_f32_16x16x32_bf16 v[224:227], v[0:3], a[136:139], v[224:227]
v_mfma_f32_16x16x32_bf16 v[228:231], v[4:7], a[136:139], v[228:231]
s_add_u32 m0, s61, 115728
ds_read_b64_tr_b16 a[132:133], v238 offset:512
v_mfma_f32_16x16x32_bf16 v[228:231], v[16:19], a[140:143], v[228:231]
buffer_load_dwordx4 v242, s[40:43], s79 offen offset:1280 nt lds
v_mfma_f32_16x16x32_bf16 v[232:235], v[20:23], a[140:143], v[232:235]
ds_read_b64_tr_b16 a[134:135], v238 offset:640
v_mfma_f32_16x16x32_bf16 v[232:235], v[8:11], a[136:139], v[232:235]
s_waitcnt lgkmcnt(4)
v_mfma_f32_16x16x32_bf16 v[204:207], v[24:27], a[144:147], v[204:207]
ds_read_b64_tr_b16 a[136:137], v239 offset:80
v_mfma_f32_16x16x32_bf16 v[204:207], v[36:39], a[148:151], v[204:207]
v_mfma_f32_16x16x32_bf16 v[208:211], v[40:43], a[148:151], v[208:211]
ds_read_b64_tr_b16 a[138:139], v239 offset:208
v_mfma_f32_16x16x32_bf16 v[208:211], a[228:231], a[144:147], v[208:211]
v_mfma_f32_16x16x32_bf16 v[212:215], v[28:31], a[144:147], v[212:215]
ds_read_b64_tr_b16 a[140:141], v239 offset:592
v_mfma_f32_16x16x32_bf16 v[212:215], a[232:235], a[148:151], v[212:215]
ds_read_b64_tr_b16 a[152:153], v245 offset:0
v_mfma_f32_16x16x32_bf16 v[216:219], v[44:47], a[148:151], v[216:219]
ds_read_b64_tr_b16 a[142:143], v239 offset:720
v_mfma_f32_16x16x32_bf16 v[216:219], v[32:35], a[144:147], v[216:219]
s_waitcnt lgkmcnt(5)
ds_read_b64_tr_b16 a[154:155], v245 offset:1056
v_mfma_f32_16x16x32_bf16 v[188:191], v[48:51], a[128:131], v[188:191]
s_add_u32 m0, s61, 124160
ds_read_b64_tr_b16 a[144:145], v239 offset:8512
v_mfma_f32_16x16x32_bf16 v[188:191], v[60:63], a[132:135], v[188:191]
buffer_load_dwordx4 v242, s[40:43], s81 offen offset:256 nt lds
v_mfma_f32_16x16x32_bf16 v[192:195], v[64:67], a[132:135], v[192:195]
ds_read_b64_tr_b16 a[146:147], v239 offset:8640
v_mfma_f32_16x16x32_bf16 v[192:195], v[52:55], a[128:131], v[192:195]
v_mfma_f32_16x16x32_bf16 v[196:199], v[56:59], a[128:131], v[196:199]
ds_read_b64_tr_b16 a[148:149], v239 offset:9024
v_mfma_f32_16x16x32_bf16 v[196:199], v[68:71], a[132:135], v[196:199]
v_mfma_f32_16x16x32_bf16 v[200:203], v[72:75], a[132:135], v[200:203]
ds_read_b64_tr_b16 a[150:151], v239 offset:9152
v_mfma_f32_16x16x32_bf16 v[200:203], a[236:239], a[128:131], v[200:203]
s_waitcnt lgkmcnt(4)
buffer_store_dwordx4 a[152:155], v243, s[48:51], 0 offen offset:0 sc1
v_mfma_f32_16x16x32_bf16 v[156:159], a[220:223], a[136:139], v[156:159]
ds_read_b64_tr_b16 a[128:129], v238 offset:16
v_mfma_f32_16x16x32_bf16 v[156:159], a[224:227], a[140:143], v[156:159]
v_mfma_f32_16x16x32_bf16 v[160:163], v[12:15], a[140:143], v[160:163]
ds_read_b64_tr_b16 a[130:131], v238 offset:144
v_mfma_f32_16x16x32_bf16 v[160:163], v[0:3], a[136:139], v[160:163]
v_mfma_f32_16x16x32_bf16 v[164:167], v[4:7], a[136:139], v[164:167]
ds_read_b64_tr_b16 a[132:133], v238 offset:528
v_mfma_f32_16x16x32_bf16 v[164:167], v[16:19], a[140:143], v[164:167]
v_mfma_f32_16x16x32_bf16 v[168:171], v[20:23], a[140:143], v[168:171]
ds_read_b64_tr_b16 a[134:135], v238 offset:656
v_mfma_f32_16x16x32_bf16 v[168:171], v[8:11], a[136:139], v[168:171]
s_waitcnt lgkmcnt(4)
v_mfma_f32_16x16x32_bf16 v[220:223], v[24:27], a[144:147], v[220:223]
s_add_u32 m0, s61, 124176
ds_read_b64_tr_b16 a[136:137], v238 offset:8448
v_mfma_f32_16x16x32_bf16 v[220:223], v[36:39], a[148:151], v[220:223]
buffer_load_dwordx4 v242, s[40:43], s81 offen offset:1280 nt lds
v_mfma_f32_16x16x32_bf16 v[224:227], v[40:43], a[148:151], v[224:227]
ds_read_b64_tr_b16 a[138:139], v238 offset:8576
v_mfma_f32_16x16x32_bf16 v[224:227], a[228:231], a[144:147], v[224:227]
v_mfma_f32_16x16x32_bf16 v[228:231], v[28:31], a[144:147], v[228:231]
ds_read_b64_tr_b16 a[140:141], v238 offset:8960
v_mfma_f32_16x16x32_bf16 v[228:231], a[232:235], a[148:151], v[228:231]
v_mfma_f32_16x16x32_bf16 v[232:235], v[44:47], a[148:151], v[232:235]
ds_read_b64_tr_b16 a[142:143], v238 offset:9088
v_mfma_f32_16x16x32_bf16 v[232:235], v[32:35], a[144:147], v[232:235]
s_waitcnt lgkmcnt(4)
v_mfma_f32_16x16x32_bf16 v[204:207], v[48:51], a[128:131], v[204:207]
ds_read_b64_tr_b16 a[144:145], v239 offset:8528
v_mfma_f32_16x16x32_bf16 v[204:207], v[60:63], a[132:135], v[204:207]
v_mfma_f32_16x16x32_bf16 v[208:211], v[64:67], a[132:135], v[208:211]
ds_read_b64_tr_b16 a[146:147], v239 offset:8656
v_mfma_f32_16x16x32_bf16 v[208:211], v[52:55], a[128:131], v[208:211]
ds_read_b64_tr_b16 a[152:153], v245 offset:32
v_mfma_f32_16x16x32_bf16 v[212:215], v[56:59], a[128:131], v[212:215]
s_add_u32 m0, s61, 132608
ds_read_b64_tr_b16 a[148:149], v239 offset:9040
v_mfma_f32_16x16x32_bf16 v[212:215], v[68:71], a[132:135], v[212:215]
buffer_load_dwordx4 v242, s[40:43], s83 offen offset:256 nt lds
v_mfma_f32_16x16x32_bf16 v[216:219], v[72:75], a[132:135], v[216:219]
ds_read_b64_tr_b16 a[150:151], v239 offset:9168
v_mfma_f32_16x16x32_bf16 v[216:219], a[236:239], a[128:131], v[216:219]
s_waitcnt lgkmcnt(5)
ds_read_b64_tr_b16 a[154:155], v245 offset:1088
v_mfma_f32_16x16x32_bf16 v[188:191], a[240:243], a[136:139], v[188:191]
ds_read_b64_tr_b16 a[128:129], v238 offset:64
v_mfma_f32_16x16x32_bf16 v[188:191], v[88:91], a[140:143], v[188:191]
v_mfma_f32_16x16x32_bf16 v[192:195], a[244:247], a[140:143], v[192:195]
ds_read_b64_tr_b16 a[130:131], v238 offset:192
v_mfma_f32_16x16x32_bf16 v[192:195], v[76:79], a[136:139], v[192:195]
v_mfma_f32_16x16x32_bf16 v[196:199], v[80:83], a[136:139], v[196:199]
ds_read_b64_tr_b16 a[132:133], v238 offset:576
v_mfma_f32_16x16x32_bf16 v[196:199], v[92:95], a[140:143], v[196:199]
v_mfma_f32_16x16x32_bf16 v[200:203], v[96:99], a[140:143], v[200:203]
ds_read_b64_tr_b16 a[134:135], v238 offset:704
v_mfma_f32_16x16x32_bf16 v[200:203], v[84:87], a[136:139], v[200:203]
s_waitcnt lgkmcnt(4)
buffer_store_dwordx4 a[152:155], v243, s[48:51], s72 offen offset:0 sc1
v_mfma_f32_16x16x32_bf16 v[156:159], v[24:27], a[144:147], v[156:159]
ds_read_b64_tr_b16 a[136:137], v238 offset:8464
v_mfma_f32_16x16x32_bf16 v[156:159], v[36:39], a[148:151], v[156:159]
v_mfma_f32_16x16x32_bf16 v[160:163], v[40:43], a[148:151], v[160:163]
ds_read_b64_tr_b16 a[138:139], v238 offset:8592
v_mfma_f32_16x16x32_bf16 v[160:163], a[228:231], a[144:147], v[160:163]
v_mfma_f32_16x16x32_bf16 v[164:167], v[28:31], a[144:147], v[164:167]
ds_read_b64_tr_b16 a[140:141], v238 offset:8976
v_mfma_f32_16x16x32_bf16 v[164:167], a[232:235], a[148:151], v[164:167]
v_mfma_f32_16x16x32_bf16 v[168:171], v[44:47], a[148:151], v[168:171]
s_waitcnt vmcnt(10) lgkmcnt(3)
ds_read_b64_tr_b16 a[142:143], v238 offset:9104
v_mfma_f32_16x16x32_bf16 v[168:171], v[32:35], a[144:147], v[168:171]
s_barrier
v_mfma_f32_16x16x32_bf16 v[220:223], v[48:51], a[128:131], v[220:223]
s_add_u32 m0, s61, 132624
ds_read_b64_tr_b16 a[144:145], v238 offset:16896
v_mfma_f32_16x16x32_bf16 v[220:223], v[60:63], a[132:135], v[220:223]
buffer_load_dwordx4 v242, s[40:43], s83 offen offset:1280 nt lds
v_mfma_f32_16x16x32_bf16 v[224:227], v[64:67], a[132:135], v[224:227]
ds_read_b64_tr_b16 a[146:147], v238 offset:17024
v_mfma_f32_16x16x32_bf16 v[224:227], v[52:55], a[128:131], v[224:227]
v_mfma_f32_16x16x32_bf16 v[228:231], v[56:59], a[128:131], v[228:231]
ds_read_b64_tr_b16 a[148:149], v238 offset:17408
v_mfma_f32_16x16x32_bf16 v[228:231], v[68:71], a[132:135], v[228:231]
v_mfma_f32_16x16x32_bf16 v[232:235], v[72:75], a[132:135], v[232:235]
ds_read_b64_tr_b16 a[150:151], v238 offset:17536
v_mfma_f32_16x16x32_bf16 v[232:235], a[236:239], a[128:131], v[232:235]
s_waitcnt lgkmcnt(4)
v_mfma_f32_16x16x32_bf16 v[204:207], a[240:243], a[136:139], v[204:207]
ds_read_b64_tr_b16 a[128:129], v238 offset:80
v_mfma_f32_16x16x32_bf16 v[204:207], v[88:91], a[140:143], v[204:207]
ds_read_b64_tr_b16 a[152:153], v245 offset:64
v_mfma_f32_16x16x32_bf16 v[208:211], a[244:247], a[140:143], v[208:211]
ds_read_b64_tr_b16 a[130:131], v238 offset:208
v_mfma_f32_16x16x32_bf16 v[208:211], v[76:79], a[136:139], v[208:211]
ds_read_b64_tr_b16 a[154:155], v245 offset:1120
v_mfma_f32_16x16x32_bf16 v[212:215], v[80:83], a[136:139], v[212:215]
s_add_u32 m0, s61, 141056
ds_read_b64_tr_b16 a[132:133], v238 offset:592
v_mfma_f32_16x16x32_bf16 v[212:215], v[92:95], a[140:143], v[212:215]
buffer_load_dwordx4 v242, s[40:43], s85 offen offset:256 nt lds
v_mfma_f32_16x16x32_bf16 v[216:219], v[96:99], a[140:143], v[216:219]
ds_read_b64_tr_b16 a[134:135], v238 offset:720
v_mfma_f32_16x16x32_bf16 v[216:219], v[84:87], a[136:139], v[216:219]
s_waitcnt lgkmcnt(6)
v_mfma_f32_16x16x32_bf16 v[188:191], v[100:103], a[144:147], v[188:191]
ds_read_b64_tr_b16 a[136:137], v238 offset:8512
v_mfma_f32_16x16x32_bf16 v[188:191], v[112:115], a[148:151], v[188:191]
v_mfma_f32_16x16x32_bf16 v[192:195], v[116:119], a[148:151], v[192:195]
ds_read_b64_tr_b16 a[138:139], v238 offset:8640
v_mfma_f32_16x16x32_bf16 v[192:195], v[104:107], a[144:147], v[192:195]
s_waitcnt lgkmcnt(4)
buffer_store_dwordx4 a[152:155], v243, s[48:51], s73 offen offset:0 sc1
v_mfma_f32_16x16x32_bf16 v[196:199], a[248:251], a[144:147], v[196:199]
ds_read_b64_tr_b16 a[140:141], v238 offset:9024
v_mfma_f32_16x16x32_bf16 v[196:199], v[120:123], a[148:151], v[196:199]
v_mfma_f32_16x16x32_bf16 v[200:203], a[252:255], a[148:151], v[200:203]
ds_read_b64_tr_b16 a[142:143], v238 offset:9152
v_mfma_f32_16x16x32_bf16 v[200:203], v[108:111], a[144:147], v[200:203]
s_waitcnt lgkmcnt(4)
v_mfma_f32_16x16x32_bf16 v[156:159], v[48:51], a[128:131], v[156:159]
ds_read_b64_tr_b16 a[144:145], v238 offset:16912
v_mfma_f32_16x16x32_bf16 v[156:159], v[60:63], a[132:135], v[156:159]
v_mfma_f32_16x16x32_bf16 v[160:163], v[64:67], a[132:135], v[160:163]
ds_read_b64_tr_b16 a[146:147], v238 offset:17040
v_mfma_f32_16x16x32_bf16 v[160:163], v[52:55], a[128:131], v[160:163]
v_mfma_f32_16x16x32_bf16 v[164:167], v[56:59], a[128:131], v[164:167]
ds_read_b64_tr_b16 a[148:149], v238 offset:17424
v_mfma_f32_16x16x32_bf16 v[164:167], v[68:71], a[132:135], v[164:167]
s_waitcnt vmcnt(12)
ds_read_b128 a[96:99], v240 offset:36864
v_mfma_f32_16x16x32_bf16 v[168:171], v[72:75], a[132:135], v[168:171]
ds_read_b64_tr_b16 a[150:151], v238 offset:17552
v_mfma_f32_16x16x32_bf16 v[168:171], a[236:239], a[128:131], v[168:171]
s_waitcnt lgkmcnt(5)
ds_read_b128 a[100:103], v240 offset:36880
v_mfma_f32_16x16x32_bf16 v[220:223], a[240:243], a[136:139], v[220:223]
)"
R"(
ds_read_b64_tr_b16 a[128:129], v238 offset:25344
v_mfma_f32_16x16x32_bf16 v[220:223], v[88:91], a[140:143], v[220:223]
ds_read_b128 a[104:107], v240 offset:36896
v_mfma_f32_16x16x32_bf16 v[224:227], a[244:247], a[140:143], v[224:227]
ds_read_b64_tr_b16 a[130:131], v238 offset:25472
v_mfma_f32_16x16x32_bf16 v[224:227], v[76:79], a[136:139], v[224:227]
ds_read_b128 a[108:111], v240 offset:36912
v_mfma_f32_16x16x32_bf16 v[228:231], v[80:83], a[136:139], v[228:231]
ds_read_b64_tr_b16 a[132:133], v238 offset:25856
v_mfma_f32_16x16x32_bf16 v[228:231], v[92:95], a[140:143], v[228:231]
ds_read_b128 a[112:115], v240 offset:36928
v_mfma_f32_16x16x32_bf16 v[232:235], v[96:99], a[140:143], v[232:235]
ds_read_b64_tr_b16 a[134:135], v238 offset:25984
v_mfma_f32_16x16x32_bf16 v[232:235], v[84:87], a[136:139], v[232:235]
s_waitcnt lgkmcnt(8)
ds_read_b128 a[116:119], v240 offset:36944
v_mfma_f32_16x16x32_bf16 v[204:207], v[100:103], a[144:147], v[204:207]
ds_read_b64_tr_b16 a[136:137], v238 offset:8528
v_mfma_f32_16x16x32_bf16 v[204:207], v[112:115], a[148:151], v[204:207]
ds_read_b128 a[120:123], v240 offset:36960
v_mfma_f32_16x16x32_bf16 v[208:211], v[116:119], a[148:151], v[208:211]
ds_read_b64_tr_b16 a[138:139], v238 offset:8656
v_mfma_f32_16x16x32_bf16 v[208:211], v[104:107], a[144:147], v[208:211]
ds_read_b128 a[124:127], v240 offset:36976
v_mfma_f32_16x16x32_bf16 v[212:215], a[248:251], a[144:147], v[212:215]
s_add_u32 m0, s61, 141072
ds_read_b64_tr_b16 a[140:141], v238 offset:9040
v_mfma_f32_16x16x32_bf16 v[212:215], v[120:123], a[148:151], v[212:215]
buffer_load_dwordx4 v242, s[40:43], s85 offen offset:1280 nt lds
v_mfma_f32_16x16x32_bf16 v[216:219], a[252:255], a[148:151], v[216:219]
ds_read_b64_tr_b16 a[142:143], v238 offset:9168
v_mfma_f32_16x16x32_bf16 v[216:219], v[108:111], a[144:147], v[216:219]
s_waitcnt lgkmcnt(7)
ds_read_b64_tr_b16 a[152:153], v245 offset:96
v_mfma_f32_16x16x32_bf16 v[188:191], v[124:127], a[128:131], v[188:191]
ds_read_b64_tr_b16 a[144:145], v238 offset:16960
v_mfma_f32_16x16x32_bf16 v[188:191], v[140:143], a[132:135], v[188:191]
ds_read_b64_tr_b16 a[154:155], v245 offset:1152
v_mfma_f32_16x16x32_bf16 v[192:195], v[144:147], a[132:135], v[192:195]
ds_read_b64_tr_b16 a[146:147], v238 offset:17088
v_mfma_f32_16x16x32_bf16 v[192:195], v[128:131], a[128:131], v[192:195]
v_mfma_f32_16x16x32_bf16 v[196:199], v[132:135], a[128:131], v[196:199]
ds_read_b64_tr_b16 a[148:149], v238 offset:17472
v_mfma_f32_16x16x32_bf16 v[196:199], v[148:151], a[132:135], v[196:199]
v_mfma_f32_16x16x32_bf16 v[200:203], v[152:155], a[132:135], v[200:203]
ds_read_b64_tr_b16 a[150:151], v238 offset:17600
v_mul_f32_dpp v188, v250, v188 row_newbcast:0 row_mask:0xf bank_mask:0xf
v_mfma_f32_16x16x32_bf16 v[200:203], v[136:139], a[128:131], v[200:203]
s_waitcnt lgkmcnt(6)
v_mul_f32_dpp v189, v250, v189 row_newbcast:1 row_mask:0xf bank_mask:0xf
v_mul_f32_dpp v190, v250, v190 row_newbcast:2 row_mask:0xf bank_mask:0xf
v_mfma_f32_16x16x32_bf16 v[156:159], a[240:243], a[136:139], v[156:159]
ds_read_b64_tr_b16 a[128:129], v238 offset:25360
v_mul_f32_dpp v191, v250, v191 row_newbcast:3 row_mask:0xf bank_mask:0xf
v_mfma_f32_16x16x32_bf16 v[156:159], v[88:91], a[140:143], v[156:159]
s_waitcnt lgkmcnt(4)
buffer_store_dwordx4 a[152:155], v243, s[48:51], s74 offen offset:0 sc1
v_mul_f32_dpp v192, v251, v192 row_newbcast:0 row_mask:0xf bank_mask:0xf
v_mul_f32_dpp v193, v251, v193 row_newbcast:1 row_mask:0xf bank_mask:0xf
v_mul_f32_dpp v194, v251, v194 row_newbcast:2 row_mask:0xf bank_mask:0xf
v_mfma_f32_16x16x32_bf16 v[160:163], a[244:247], a[140:143], v[160:163]
ds_read_b64_tr_b16 a[130:131], v238 offset:25488
v_mul_f32_dpp v195, v251, v195 row_newbcast:3 row_mask:0xf bank_mask:0xf
v_mfma_f32_16x16x32_bf16 v[160:163], v[76:79], a[136:139], v[160:163]
v_mul_f32_dpp v196, v252, v196 row_newbcast:0 row_mask:0xf bank_mask:0xf
v_mul_f32_dpp v197, v252, v197 row_newbcast:1 row_mask:0xf bank_mask:0xf
v_mfma_f32_16x16x32_bf16 v[164:167], v[80:83], a[136:139], v[164:167]
s_add_u32 m0, s61, 149504
ds_read_b64_tr_b16 a[132:133], v238 offset:25872
v_mul_f32_dpp v198, v252, v198 row_newbcast:2 row_mask:0xf bank_mask:0xf
v_mfma_f32_16x16x32_bf16 v[164:167], v[92:95], a[140:143], v[164:167]
buffer_load_dwordx4 v242, s[40:43], s87 offen offset:256 nt lds
v_mfma_f32_16x16x32_bf16 v[168:171], v[96:99], a[140:143], v[168:171]
s_waitcnt vmcnt(11) lgkmcnt(3)
ds_read_b64_tr_b16 a[134:135], v238 offset:26000
v_mul_f32_dpp v199, v252, v199 row_newbcast:3 row_mask:0xf bank_mask:0xf
v_mfma_f32_16x16x32_bf16 v[168:171], v[84:87], a[136:139], v[168:171]
s_barrier
v_mfma_f32_16x16x32_bf16 v[220:223], v[100:103], a[144:147], v[220:223]
ds_read_b64_tr_b16 a[136:137], v238 offset:33792
v_mul_f32_dpp v200, v253, v200 row_newbcast:0 row_mask:0xf bank_mask:0xf
v_mfma_f32_16x16x32_bf16 v[220:223], v[112:115], a[148:151], v[220:223]
v_mul_f32_dpp v201, v253, v201 row_newbcast:1 row_mask:0xf bank_mask:0xf
v_mul_f32_dpp v202, v253, v202 row_newbcast:2 row_mask:0xf bank_mask:0xf
v_mfma_f32_16x16x32_bf16 v[224:227], v[116:119], a[148:151], v[224:227]
ds_read_b64_tr_b16 a[138:139], v238 offset:33920
v_mul_f32_dpp v203, v253, v203 row_newbcast:3 row_mask:0xf bank_mask:0xf
v_mfma_f32_16x16x32_bf16 v[224:227], v[104:107], a[144:147], v[224:227]
.Lpad7_%=:
s_cbranch_vccnz .Lret1_%=
v_mfma_f32_16x16x32_bf16 v[228:231], a[248:251], a[144:147], v[228:231]
s_add_u32 m0, s61, 149520
ds_read_b64_tr_b16 a[140:141], v238 offset:34304
v_mfma_f32_16x16x32_bf16 v[228:231], v[120:123], a[148:151], v[228:231]
buffer_load_dwordx4 v242, s[40:43], s87 offen offset:1280 nt lds
v_mfma_f32_16x16x32_bf16 v[232:235], a[252:255], a[148:151], v[232:235]
ds_read_b64_tr_b16 a[142:143], v238 offset:34432
v_mfma_f32_16x16x32_bf16 v[232:235], v[108:111], a[144:147], v[232:235]
s_waitcnt lgkmcnt(4)
ds_read_b64_tr_b16 a[152:153], v245 offset:128
v_mfma_f32_16x16x32_bf16 v[204:207], v[124:127], a[128:131], v[204:207]
ds_read_b64_tr_b16 a[144:145], v238 offset:16976
v_mfma_f32_16x16x32_bf16 v[204:207], v[140:143], a[132:135], v[204:207]
ds_read_b64_tr_b16 a[154:155], v245 offset:1184
v_mfma_f32_16x16x32_bf16 v[208:211], v[144:147], a[132:135], v[208:211]
ds_read_b64_tr_b16 a[146:147], v238 offset:17104
v_mfma_f32_16x16x32_bf16 v[208:211], v[128:131], a[128:131], v[208:211]
v_mfma_f32_16x16x32_bf16 v[212:215], v[132:135], a[128:131], v[212:215]
ds_read_b64_tr_b16 a[148:149], v238 offset:17488
v_mfma_f32_16x16x32_bf16 v[212:215], v[148:151], a[132:135], v[212:215]
v_mfma_f32_16x16x32_bf16 v[216:219], v[152:155], a[132:135], v[216:219]
ds_read_b64_tr_b16 a[150:151], v238 offset:17616
v_mul_f32_dpp v204, v250, v204 row_newbcast:0 row_mask:0xf bank_mask:0xf
v_mfma_f32_16x16x32_bf16 v[216:219], v[136:139], a[128:131], v[216:219]
s_waitcnt lgkmcnt(6)
v_mul_f32_dpp v205, v250, v205 row_newbcast:1 row_mask:0xf bank_mask:0xf
v_mul_f32_dpp v206, v250, v206 row_newbcast:2 row_mask:0xf bank_mask:0xf
v_mfma_f32_16x16x32_bf16 v[172:175], a[156:159], a[136:139], 0
ds_read_b64_tr_b16 a[128:129], v238 offset:25408
v_mul_f32_dpp v207, v250, v207 row_newbcast:3 row_mask:0xf bank_mask:0xf
v_mfma_f32_16x16x32_bf16 v[172:175], a[172:175], a[140:143], v[172:175]
s_waitcnt lgkmcnt(4)
buffer_store_dwordx4 a[152:155], v243, s[48:51], s75 offen offset:0 sc1
v_cvt_pk_bf16_f32 v204, v188, v204
v_cvt_pk_bf16_f32 v205, v189, v205
v_cvt_pk_bf16_f32 v206, v190, v206
v_mfma_f32_16x16x32_bf16 v[176:179], a[176:179], a[140:143], 0
ds_read_b64_tr_b16 a[130:131], v238 offset:25536
v_cvt_pk_bf16_f32 v207, v191, v207
v_mfma_f32_16x16x32_bf16 v[176:179], a[160:163], a[136:139], v[176:179]
v_mul_f32_dpp v208, v251, v208 row_newbcast:0 row_mask:0xf bank_mask:0xf
v_mul_f32_dpp v209, v251, v209 row_newbcast:1 row_mask:0xf bank_mask:0xf
v_mfma_f32_16x16x32_bf16 v[180:183], a[164:167], a[136:139], 0
ds_read_b64_tr_b16 a[132:133], v238 offset:25920
v_mul_f32_dpp v210, v251, v210 row_newbcast:2 row_mask:0xf bank_mask:0xf
v_mfma_f32_16x16x32_bf16 v[180:183], a[180:183], a[140:143], v[180:183]
v_mul_f32_dpp v211, v251, v211 row_newbcast:3 row_mask:0xf bank_mask:0xf
v_cvt_pk_bf16_f32 v208, v192, v208
v_mfma_f32_16x16x32_bf16 v[184:187], a[184:187], a[140:143], 0
ds_read_b64_tr_b16 a[134:135], v238 offset:26048
v_cvt_pk_bf16_f32 v209, v193, v209
v_mfma_f32_16x16x32_bf16 v[184:187], a[168:171], a[136:139], v[184:187]
s_waitcnt lgkmcnt(4)
v_cvt_pk_bf16_f32 v210, v194, v210
v_cvt_pk_bf16_f32 v211, v195, v211
v_mfma_f32_16x16x32_bf16 v[156:159], v[100:103], a[144:147], v[156:159]
ds_read_b64_tr_b16 a[136:137], v238 offset:33808
v_mul_f32_dpp v212, v252, v212 row_newbcast:0 row_mask:0xf bank_mask:0xf
v_mfma_f32_16x16x32_bf16 v[156:159], v[112:115], a[148:151], v[156:159]
v_mul_f32_dpp v213, v252, v213 row_newbcast:1 row_mask:0xf bank_mask:0xf
v_mul_f32_dpp v214, v252, v214 row_newbcast:2 row_mask:0xf bank_mask:0xf
v_mfma_f32_16x16x32_bf16 v[160:163], v[116:119], a[148:151], v[160:163]
s_add_u32 m0, s61, 73472
ds_read_b64_tr_b16 a[138:139], v238 offset:33936
v_mul_f32_dpp v215, v252, v215 row_newbcast:3 row_mask:0xf bank_mask:0xf
v_mfma_f32_16x16x32_bf16 v[160:163], v[104:107], a[144:147], v[160:163]
buffer_load_dwordx4 v242, s[40:43], s89 offen offset:256 nt lds
v_mfma_f32_16x16x32_bf16 v[164:167], a[248:251], a[144:147], v[164:167]
ds_read_b64_tr_b16 a[140:141], v238 offset:34320
v_cvt_pk_bf16_f32 v212, v196, v212
v_mfma_f32_16x16x32_bf16 v[164:167], v[120:123], a[148:151], v[164:167]
v_cvt_pk_bf16_f32 v213, v197, v213
v_cvt_pk_bf16_f32 v214, v198, v214
v_mfma_f32_16x16x32_bf16 v[168:171], a[252:255], a[148:151], v[168:171]
ds_read_b64_tr_b16 a[142:143], v238 offset:34448
v_cvt_pk_bf16_f32 v215, v199, v215
v_mfma_f32_16x16x32_bf16 v[168:171], v[108:111], a[144:147], v[168:171]
s_waitcnt lgkmcnt(4)
v_mul_f32_dpp v216, v253, v216 row_newbcast:0 row_mask:0xf bank_mask:0xf
v_mul_f32_dpp v217, v253, v217 row_newbcast:1 row_mask:0xf bank_mask:0xf
v_mfma_f32_16x16x32_bf16 v[220:223], v[124:127], a[128:131], v[220:223]
ds_read_b64_tr_b16 a[144:145], v238 offset:42240
v_mul_f32_dpp v218, v253, v218 row_newbcast:2 row_mask:0xf bank_mask:0xf
v_mfma_f32_16x16x32_bf16 v[220:223], v[140:143], a[132:135], v[220:223]
v_mul_f32_dpp v219, v253, v219 row_newbcast:3 row_mask:0xf bank_mask:0xf
v_cvt_pk_bf16_f32 v216, v200, v216
v_mfma_f32_16x16x32_bf16 v[224:227], v[144:147], a[132:135], v[224:227]
ds_read_b64_tr_b16 a[146:147], v238 offset:42368
v_cvt_pk_bf16_f32 v217, v201, v217
v_mfma_f32_16x16x32_bf16 v[224:227], v[128:131], a[128:131], v[224:227]
ds_read_b64_tr_b16 a[152:153], v245 offset:160
v_cvt_pk_bf16_f32 v218, v202, v218
v_mfma_f32_16x16x32_bf16 v[228:231], v[132:135], a[128:131], v[228:231]
ds_read_b64_tr_b16 a[148:149], v238 offset:42752
v_cvt_pk_bf16_f32 v219, v203, v219
v_mfma_f32_16x16x32_bf16 v[228:231], v[148:151], a[132:135], v[228:231]
ds_read_b64_tr_b16 a[154:155], v245 offset:1216
v_mul_f32_dpp v220, v250, v220 row_newbcast:0 row_mask:0xf bank_mask:0xf
v_mfma_f32_16x16x32_bf16 v[232:235], v[152:155], a[132:135], v[232:235]
s_add_u32 m0, s61, 73488
ds_read_b64_tr_b16 a[150:151], v238 offset:42880
v_mul_f32_dpp v221, v250, v221 row_newbcast:1 row_mask:0xf bank_mask:0xf
v_mfma_f32_16x16x32_bf16 v[232:235], v[136:139], a[128:131], v[232:235]
s_waitcnt lgkmcnt(6)
buffer_load_dwordx4 v242, s[40:43], s89 offen offset:1280 nt lds
v_mfma_f32_16x16x32_bf16 v[188:191], a[156:159], a[136:139], 0
ds_read_b64_tr_b16 a[128:129], v238 offset:25424
v_mul_f32_dpp v222, v250, v222 row_newbcast:2 row_mask:0xf bank_mask:0xf
v_mfma_f32_16x16x32_bf16 v[188:191], a[172:175], a[140:143], v[188:191]
ds_write_b64 v244, v[204:205] offset:36864
v_mfma_f32_16x16x32_bf16 v[192:195], a[176:179], a[140:143], 0
ds_read_b64_tr_b16 a[130:131], v238 offset:25552
v_mfma_f32_16x16x32_bf16 v[192:195], a[160:163], a[136:139], v[192:195]
ds_write_b64 v244, v[206:207] offset:36872
v_mfma_f32_16x16x32_bf16 v[196:199], a[164:167], a[136:139], 0
ds_read_b64_tr_b16 a[132:133], v238 offset:25936
v_mfma_f32_16x16x32_bf16 v[196:199], a[180:183], a[140:143], v[196:199]
s_waitcnt lgkmcnt(6)
)"
R"(
buffer_store_dwordx4 a[152:155], v243, s[48:51], s76 offen offset:0 sc1
v_mul_f32_dpp v223, v250, v223 row_newbcast:3 row_mask:0xf bank_mask:0xf
v_mul_f32_dpp v224, v251, v224 row_newbcast:0 row_mask:0xf bank_mask:0xf
v_mul_f32_dpp v225, v251, v225 row_newbcast:1 row_mask:0xf bank_mask:0xf
v_mfma_f32_16x16x32_bf16 v[200:203], a[184:187], a[140:143], 0
ds_read_b64_tr_b16 a[134:135], v238 offset:26064
v_mul_f32_dpp v226, v251, v226 row_newbcast:2 row_mask:0xf bank_mask:0xf
v_mfma_f32_16x16x32_bf16 v[200:203], a[168:171], a[136:139], v[200:203]
s_waitcnt lgkmcnt(6)
ds_write_b64 v244, v[208:209] offset:36928
v_mfma_f32_16x16x32_bf16 v[172:175], a[188:191], a[144:147], v[172:175]
ds_read_b64_tr_b16 a[136:137], v238 offset:33856
v_mfma_f32_16x16x32_bf16 v[172:175], a[204:207], a[148:151], v[172:175]
ds_write_b64 v244, v[210:211] offset:36936
v_mfma_f32_16x16x32_bf16 v[176:179], a[208:211], a[148:151], v[176:179]
ds_read_b64_tr_b16 a[138:139], v238 offset:33984
v_mfma_f32_16x16x32_bf16 v[176:179], a[192:195], a[144:147], v[176:179]
ds_write_b64 v244, v[212:213] offset:36992
v_mfma_f32_16x16x32_bf16 v[180:183], a[196:199], a[144:147], v[180:183]
ds_read_b64_tr_b16 a[140:141], v238 offset:34368
v_mfma_f32_16x16x32_bf16 v[180:183], a[212:215], a[148:151], v[180:183]
ds_write_b64 v244, v[214:215] offset:37000
v_mfma_f32_16x16x32_bf16 v[184:187], a[216:219], a[148:151], v[184:187]
ds_read_b64_tr_b16 a[142:143], v238 offset:34496
v_mfma_f32_16x16x32_bf16 v[184:187], a[200:203], a[144:147], v[184:187]
s_waitcnt lgkmcnt(8)
ds_write_b64 v244, v[216:217] offset:37056
v_mfma_f32_16x16x32_bf16 v[156:159], v[124:127], a[128:131], v[156:159]
s_add_u32 m0, s61, 81920
ds_read_b64_tr_b16 a[144:145], v238 offset:42256
v_mfma_f32_16x16x32_bf16 v[156:159], v[140:143], a[132:135], v[156:159]
buffer_load_dwordx4 v242, s[40:43], s91 offen offset:256 nt lds
v_mfma_f32_16x16x32_bf16 v[160:163], v[144:147], a[132:135], v[160:163]
ds_read_b64_tr_b16 a[146:147], v238 offset:42384
v_mul_f32_dpp v227, v251, v227 row_newbcast:3 row_mask:0xf bank_mask:0xf
v_mfma_f32_16x16x32_bf16 v[160:163], v[128:131], a[128:131], v[160:163]
ds_write_b64 v244, v[218:219] offset:37064
v_mfma_f32_16x16x32_bf16 v[164:167], v[132:135], a[128:131], v[164:167]
ds_read_b64_tr_b16 a[148:149], v238 offset:42768
v_mfma_f32_16x16x32_bf16 v[164:167], v[148:151], a[132:135], v[164:167]
v_mul_f32_dpp v228, v252, v228 row_newbcast:0 row_mask:0xf bank_mask:0xf
v_mul_f32_dpp v229, v252, v229 row_newbcast:1 row_mask:0xf bank_mask:0xf
v_mfma_f32_16x16x32_bf16 v[168:171], v[152:155], a[132:135], v[168:171]
s_waitcnt vmcnt(11) lgkmcnt(5)
ds_read_b64_tr_b16 a[150:151], v238 offset:42896
v_mul_f32_dpp v230, v252, v230 row_newbcast:2 row_mask:0xf bank_mask:0xf
v_mfma_f32_16x16x32_bf16 v[168:171], v[136:139], a[128:131], v[168:171]
s_barrier
v_mfma_f32_16x16x32_bf16 v[204:207], a[156:159], a[136:139], 0
s_add_u32 m0, s61, 81936
ds_read_b64_tr_b16 a[128:129], v238 offset:50688
v_mul_f32_dpp v231, v252, v231 row_newbcast:3 row_mask:0xf bank_mask:0xf
v_mfma_f32_16x16x32_bf16 v[204:207], a[172:175], a[140:143], v[204:207]
buffer_load_dwordx4 v242, s[40:43], s91 offen offset:1280 nt lds
v_mfma_f32_16x16x32_bf16 v[208:211], a[176:179], a[140:143], 0
ds_read_b64_tr_b16 a[130:131], v238 offset:50816
v_mul_f32_dpp v232, v253, v232 row_newbcast:0 row_mask:0xf bank_mask:0xf
v_mfma_f32_16x16x32_bf16 v[208:211], a[160:163], a[136:139], v[208:211]
ds_read_b64_tr_b16 a[152:153], v245 offset:192
v_mul_f32_dpp v233, v253, v233 row_newbcast:1 row_mask:0xf bank_mask:0xf
v_mfma_f32_16x16x32_bf16 v[212:215], a[164:167], a[136:139], 0
ds_read_b64_tr_b16 a[132:133], v238 offset:51200
v_mul_f32_dpp v234, v253, v234 row_newbcast:2 row_mask:0xf bank_mask:0xf
v_mfma_f32_16x16x32_bf16 v[212:215], a[180:183], a[140:143], v[212:215]
ds_read_b64_tr_b16 a[154:155], v245 offset:1248
v_mul_f32_dpp v235, v253, v235 row_newbcast:3 row_mask:0xf bank_mask:0xf
v_mfma_f32_16x16x32_bf16 v[216:219], a[184:187], a[140:143], 0
ds_read_b64_tr_b16 a[134:135], v238 offset:51328
v_mul_f32_dpp v156, v250, v156 row_newbcast:0 row_mask:0xf bank_mask:0xf
v_mfma_f32_16x16x32_bf16 v[216:219], a[168:171], a[136:139], v[216:219]
s_waitcnt lgkmcnt(6)
v_mul_f32_dpp v157, v250, v157 row_newbcast:1 row_mask:0xf bank_mask:0xf
v_mul_f32_dpp v158, v250, v158 row_newbcast:2 row_mask:0xf bank_mask:0xf
v_mfma_f32_16x16x32_bf16 v[188:191], a[188:191], a[144:147], v[188:191]
ds_read_b64_tr_b16 a[136:137], v238 offset:33872
v_mul_f32_dpp v159, v250, v159 row_newbcast:3 row_mask:0xf bank_mask:0xf
v_mfma_f32_16x16x32_bf16 v[188:191], a[204:207], a[148:151], v[188:191]
v_cvt_pk_bf16_f32 v156, v220, v156
v_cvt_pk_bf16_f32 v157, v221, v157
v_mfma_f32_16x16x32_bf16 v[192:195], a[208:211], a[148:151], v[192:195]
ds_read_b64_tr_b16 a[138:139], v238 offset:34000
v_cvt_pk_bf16_f32 v158, v222, v158
v_mfma_f32_16x16x32_bf16 v[192:195], a[192:195], a[144:147], v[192:195]
v_cvt_pk_bf16_f32 v159, v223, v159
v_mul_f32_dpp v160, v251, v160 row_newbcast:0 row_mask:0xf bank_mask:0xf
v_mfma_f32_16x16x32_bf16 v[196:199], a[196:199], a[144:147], v[196:199]
ds_read_b64_tr_b16 a[140:141], v238 offset:34384
v_mul_f32_dpp v161, v251, v161 row_newbcast:1 row_mask:0xf bank_mask:0xf
v_mfma_f32_16x16x32_bf16 v[196:199], a[212:215], a[148:151], v[196:199]
s_waitcnt lgkmcnt(4)
buffer_store_dwordx4 a[152:155], v243, s[48:51], s77 offen offset:0 sc1
v_mul_f32_dpp v162, v251, v162 row_newbcast:2 row_mask:0xf bank_mask:0xf
v_mul_f32_dpp v163, v251, v163 row_newbcast:3 row_mask:0xf bank_mask:0xf
v_cvt_pk_bf16_f32 v160, v224, v160
v_mfma_f32_16x16x32_bf16 v[200:203], a[216:219], a[148:151], v[200:203]
ds_read_b64_tr_b16 a[142:143], v238 offset:34512
v_cvt_pk_bf16_f32 v161, v225, v161
v_mfma_f32_16x16x32_bf16 v[200:203], a[200:203], a[144:147], v[200:203]
s_waitcnt lgkmcnt(4)
v_cvt_pk_bf16_f32 v162, v226, v162
v_cvt_pk_bf16_f32 v163, v227, v163
v_mfma_f32_16x16x32_bf16 v[172:175], a[220:223], a[128:131], v[172:175]
ds_read_b64_tr_b16 a[144:145], v238 offset:42304
v_mul_f32_dpp v164, v252, v164 row_newbcast:0 row_mask:0xf bank_mask:0xf
v_mfma_f32_16x16x32_bf16 v[172:175], a[224:227], a[132:135], v[172:175]
v_mul_f32_dpp v165, v252, v165 row_newbcast:1 row_mask:0xf bank_mask:0xf
v_mul_f32_dpp v166, v252, v166 row_newbcast:2 row_mask:0xf bank_mask:0xf
v_mfma_f32_16x16x32_bf16 v[176:179], v[12:15], a[132:135], v[176:179]
ds_read_b64_tr_b16 a[146:147], v238 offset:42432
v_mul_f32_dpp v167, v252, v167 row_newbcast:3 row_mask:0xf bank_mask:0xf
v_mfma_f32_16x16x32_bf16 v[176:179], v[0:3], a[128:131], v[176:179]
v_cvt_pk_bf16_f32 v164, v228, v164
v_cvt_pk_bf16_f32 v165, v229, v165
v_mfma_f32_16x16x32_bf16 v[180:183], v[4:7], a[128:131], v[180:183]
ds_read_b64_tr_b16 a[148:149], v238 offset:42816
v_cvt_pk_bf16_f32 v166, v230, v166
v_mfma_f32_16x16x32_bf16 v[180:183], v[16:19], a[132:135], v[180:183]
v_cvt_pk_bf16_f32 v167, v231, v167
v_mul_f32_dpp v168, v253, v168 row_newbcast:0 row_mask:0xf bank_mask:0xf
v_mfma_f32_16x16x32_bf16 v[184:187], v[20:23], a[132:135], v[184:187]
ds_read_b64_tr_b16 a[150:151], v238 offset:42944
v_mul_f32_dpp v169, v253, v169 row_newbcast:1 row_mask:0xf bank_mask:0xf
v_mfma_f32_16x16x32_bf16 v[184:187], v[8:11], a[128:131], v[184:187]
s_waitcnt lgkmcnt(4)
v_mul_f32_dpp v170, v253, v170 row_newbcast:2 row_mask:0xf bank_mask:0xf
v_mul_f32_dpp v171, v253, v171 row_newbcast:3 row_mask:0xf bank_mask:0xf
v_mfma_f32_16x16x32_bf16 v[220:223], a[156:159], a[136:139], 0
s_add_u32 m0, s61, 90240
ds_read_b64_tr_b16 a[128:129], v238 offset:50704
v_cvt_pk_bf16_f32 v168, v232, v168
v_mfma_f32_16x16x32_bf16 v[220:223], a[172:175], a[140:143], v[220:223]
buffer_load_dwordx4 v242, s[40:43], 0 offen offset:384 nt lds
v_mfma_f32_16x16x32_bf16 v[224:227], a[176:179], a[140:143], 0
ds_read_b64_tr_b16 a[130:131], v238 offset:50832
v_cvt_pk_bf16_f32 v169, v233, v169
v_mfma_f32_16x16x32_bf16 v[224:227], a[160:163], a[136:139], v[224:227]
v_cvt_pk_bf16_f32 v170, v234, v170
v_cvt_pk_bf16_f32 v171, v235, v171
v_mfma_f32_16x16x32_bf16 v[228:231], a[164:167], a[136:139], 0
ds_read_b64_tr_b16 a[132:133], v238 offset:51216
v_mfma_f32_16x16x32_bf16 v[228:231], a[180:183], a[140:143], v[228:231]
ds_write_b64 v244, v[156:157] offset:41088
v_mfma_f32_16x16x32_bf16 v[232:235], a[184:187], a[140:143], 0
ds_read_b64_tr_b16 a[134:135], v238 offset:51344
v_mfma_f32_16x16x32_bf16 v[232:235], a[168:171], a[136:139], v[232:235]
s_waitcnt lgkmcnt(5)
ds_read_b64_tr_b16 a[152:153], v245 offset:224
v_mfma_f32_16x16x32_bf16 v[204:207], a[188:191], a[144:147], v[204:207]
ds_read_b64_tr_b16 a[136:137], v238 offset:59136
v_mfma_f32_16x16x32_bf16 v[204:207], a[204:207], a[148:151], v[204:207]
ds_read_b64_tr_b16 a[154:155], v245 offset:1280
v_mfma_f32_16x16x32_bf16 v[208:211], a[208:211], a[148:151], v[208:211]
ds_read_b64_tr_b16 a[138:139], v238 offset:59264
v_mfma_f32_16x16x32_bf16 v[208:211], a[192:195], a[144:147], v[208:211]
ds_write_b64 v244, v[158:159] offset:41096
v_mfma_f32_16x16x32_bf16 v[212:215], a[196:199], a[144:147], v[212:215]
s_add_u32 m0, s61, 90256
ds_read_b64_tr_b16 a[140:141], v238 offset:59648
v_mfma_f32_16x16x32_bf16 v[212:215], a[212:215], a[148:151], v[212:215]
buffer_load_dwordx4 v242, s[40:43], 0 offen offset:1408 nt lds
v_mfma_f32_16x16x32_bf16 v[216:219], a[216:219], a[148:151], v[216:219]
ds_read_b64_tr_b16 a[142:143], v238 offset:59776
v_mfma_f32_16x16x32_bf16 v[216:219], a[200:203], a[144:147], v[216:219]
s_waitcnt lgkmcnt(7)
ds_write_b64 v244, v[160:161] offset:41152
v_mfma_f32_16x16x32_bf16 v[188:191], a[220:223], a[128:131], v[188:191]
ds_read_b64_tr_b16 a[144:145], v238 offset:42320
v_mfma_f32_16x16x32_bf16 v[188:191], a[224:227], a[132:135], v[188:191]
s_waitcnt lgkmcnt(6)
buffer_store_dwordx4 a[152:155], v243, s[48:51], s78 offen offset:0 sc1
v_mfma_f32_16x16x32_bf16 v[192:195], v[12:15], a[132:135], v[192:195]
ds_read_b64_tr_b16 a[146:147], v238 offset:42448
v_mfma_f32_16x16x32_bf16 v[192:195], v[0:3], a[128:131], v[192:195]
ds_write_b64 v244, v[162:163] offset:41160
v_mfma_f32_16x16x32_bf16 v[196:199], v[4:7], a[128:131], v[196:199]
ds_read_b64_tr_b16 a[148:149], v238 offset:42832
v_mfma_f32_16x16x32_bf16 v[196:199], v[16:19], a[132:135], v[196:199]
ds_write_b64 v244, v[164:165] offset:41216
v_mfma_f32_16x16x32_bf16 v[200:203], v[20:23], a[132:135], v[200:203]
ds_read_b64_tr_b16 a[150:151], v238 offset:42960
v_mfma_f32_16x16x32_bf16 v[200:203], v[8:11], a[128:131], v[200:203]
s_waitcnt lgkmcnt(7)
ds_write_b64 v244, v[166:167] offset:41224
v_mfma_f32_16x16x32_bf16 v[172:175], v[24:27], a[136:139], v[172:175]
ds_read_b64_tr_b16 a[128:129], v238 offset:50752
v_mfma_f32_16x16x32_bf16 v[172:175], v[36:39], a[140:143], v[172:175]
ds_write_b64 v244, v[168:169] offset:41280
v_mfma_f32_16x16x32_bf16 v[176:179], v[40:43], a[140:143], v[176:179]
ds_read_b64_tr_b16 a[130:131], v238 offset:50880
v_mfma_f32_16x16x32_bf16 v[176:179], a[228:231], a[136:139], v[176:179]
ds_write_b64 v244, v[170:171] offset:41288
v_mfma_f32_16x16x32_bf16 v[180:183], v[28:31], a[136:139], v[180:183]
ds_read_b64_tr_b16 a[132:133], v238 offset:51264
v_mfma_f32_16x16x32_bf16 v[180:183], a[232:235], a[140:143], v[180:183]
v_mfma_f32_16x16x32_bf16 v[184:187], v[44:47], a[140:143], v[184:187]
s_add_u32 m0, s61, 98688
ds_read_b64_tr_b16 a[134:135], v238 offset:51392
v_mfma_f32_16x16x32_bf16 v[184:187], v[32:35], a[136:139], v[184:187]
s_waitcnt lgkmcnt(7)
buffer_load_dwordx4 v242, s[40:43], s79 offen offset:384 nt lds
v_mfma_f32_16x16x32_bf16 v[220:223], a[188:191], a[144:147], v[220:223]
ds_read_b64_tr_b16 a[136:137], v238 offset:59152
v_mfma_f32_16x16x32_bf16 v[220:223], a[204:207], a[148:151], v[220:223]
v_mfma_f32_16x16x32_bf16 v[224:227], a[208:211], a[148:151], v[224:227]
ds_read_b64_tr_b16 a[138:139], v238 offset:59280
v_mfma_f32_16x16x32_bf16 v[224:227], a[192:195], a[144:147], v[224:227]
v_mfma_f32_16x16x32_bf16 v[228:231], a[196:199], a[144:147], v[228:231]
ds_read_b64_tr_b16 a[140:141], v238 offset:59664
v_mfma_f32_16x16x32_bf16 v[228:231], a[212:215], a[148:151], v[228:231]
v_mfma_f32_16x16x32_bf16 v[232:235], a[216:219], a[148:151], v[232:235]
)"
R"(
s_waitcnt vmcnt(11) lgkmcnt(3)
ds_read_b64_tr_b16 a[142:143], v238 offset:59792
v_mfma_f32_16x16x32_bf16 v[232:235], a[200:203], a[144:147], v[232:235]
s_barrier
v_mfma_f32_16x16x32_bf16 v[204:207], a[220:223], a[128:131], v[204:207]
s_add_u32 m0, s61, 98704
ds_read_b64_tr_b16 a[144:145], v239 offset:0
v_mfma_f32_16x16x32_bf16 v[204:207], a[224:227], a[132:135], v[204:207]
buffer_load_dwordx4 v242, s[40:43], s79 offen offset:1408 nt lds
v_mfma_f32_16x16x32_bf16 v[208:211], v[12:15], a[132:135], v[208:211]
ds_read_b64_tr_b16 a[146:147], v239 offset:128
v_mfma_f32_16x16x32_bf16 v[208:211], v[0:3], a[128:131], v[208:211]
ds_read_b64_tr_b16 a[152:153], v245 offset:36864
v_mfma_f32_16x16x32_bf16 v[212:215], v[4:7], a[128:131], v[212:215]
ds_read_b64_tr_b16 a[148:149], v239 offset:512
v_mfma_f32_16x16x32_bf16 v[212:215], v[16:19], a[132:135], v[212:215]
ds_read_b64_tr_b16 a[154:155], v245 offset:37920
v_mfma_f32_16x16x32_bf16 v[216:219], v[20:23], a[132:135], v[216:219]
ds_read_b64_tr_b16 a[150:151], v239 offset:640
v_mfma_f32_16x16x32_bf16 v[216:219], v[8:11], a[128:131], v[216:219]
s_waitcnt lgkmcnt(6)
v_mfma_f32_16x16x32_bf16 v[188:191], v[24:27], a[136:139], v[188:191]
ds_read_b64_tr_b16 a[128:129], v238 offset:50768
v_mfma_f32_16x16x32_bf16 v[188:191], v[36:39], a[140:143], v[188:191]
v_mfma_f32_16x16x32_bf16 v[192:195], v[40:43], a[140:143], v[192:195]
ds_read_b64_tr_b16 a[130:131], v238 offset:50896
v_mfma_f32_16x16x32_bf16 v[192:195], a[228:231], a[136:139], v[192:195]
v_mfma_f32_16x16x32_bf16 v[196:199], v[28:31], a[136:139], v[196:199]
ds_read_b64_tr_b16 a[132:133], v238 offset:51280
v_mfma_f32_16x16x32_bf16 v[196:199], a[232:235], a[140:143], v[196:199]
s_waitcnt lgkmcnt(4)
buffer_store_dwordx4 a[152:155], v243, s[48:51], 0 offen offset:128 sc1
v_mfma_f32_16x16x32_bf16 v[200:203], v[44:47], a[140:143], v[200:203]
ds_read_b64_tr_b16 a[134:135], v238 offset:51408
v_mfma_f32_16x16x32_bf16 v[200:203], v[32:35], a[136:139], v[200:203]
s_waitcnt lgkmcnt(4)
v_mfma_f32_16x16x32_bf16 v[172:175], v[48:51], a[144:147], v[172:175]
ds_read_b64_tr_b16 a[136:137], v238 offset:59200
v_mfma_f32_16x16x32_bf16 v[172:175], v[60:63], a[148:151], v[172:175]
v_mfma_f32_16x16x32_bf16 v[176:179], v[64:67], a[148:151], v[176:179]
ds_read_b64_tr_b16 a[138:139], v238 offset:59328
v_mfma_f32_16x16x32_bf16 v[176:179], v[52:55], a[144:147], v[176:179]
v_mfma_f32_16x16x32_bf16 v[180:183], v[56:59], a[144:147], v[180:183]
s_add_u32 m0, s61, 107136
ds_read_b64_tr_b16 a[140:141], v238 offset:59712
v_mfma_f32_16x16x32_bf16 v[180:183], v[68:71], a[148:151], v[180:183]
buffer_load_dwordx4 v242, s[40:43], s81 offen offset:384 nt lds
v_mfma_f32_16x16x32_bf16 v[184:187], v[72:75], a[148:151], v[184:187]
ds_read_b64_tr_b16 a[142:143], v238 offset:59840
v_mfma_f32_16x16x32_bf16 v[184:187], a[236:239], a[144:147], v[184:187]
s_waitcnt lgkmcnt(4)
v_mfma_f32_16x16x32_bf16 v[220:223], a[220:223], a[128:131], v[220:223]
ds_read_b64_tr_b16 a[144:145], v239 offset:16
v_mfma_f32_16x16x32_bf16 v[220:223], a[224:227], a[132:135], v[220:223]
v_mfma_f32_16x16x32_bf16 v[224:227], v[12:15], a[132:135], v[224:227]
ds_read_b64_tr_b16 a[146:147], v239 offset:144
v_mfma_f32_16x16x32_bf16 v[224:227], v[0:3], a[128:131], v[224:227]
v_mfma_f32_16x16x32_bf16 v[228:231], v[4:7], a[128:131], v[228:231]
ds_read_b64_tr_b16 a[148:149], v239 offset:528
v_mfma_f32_16x16x32_bf16 v[228:231], v[16:19], a[132:135], v[228:231]
v_mfma_f32_16x16x32_bf16 v[232:235], v[20:23], a[132:135], v[232:235]
ds_read_b64_tr_b16 a[150:151], v239 offset:656
v_mfma_f32_16x16x32_bf16 v[232:235], v[8:11], a[128:131], v[232:235]
s_waitcnt lgkmcnt(4)
ds_read_b64_tr_b16 a[152:153], v245 offset:36896
v_mfma_f32_16x16x32_bf16 v[204:207], v[24:27], a[136:139], v[204:207]
s_add_u32 m0, s61, 107152
ds_read_b64_tr_b16 a[128:129], v239 offset:8448
v_mfma_f32_16x16x32_bf16 v[204:207], v[36:39], a[140:143], v[204:207]
buffer_load_dwordx4 v242, s[40:43], s81 offen offset:1408 nt lds
v_mfma_f32_16x16x32_bf16 v[208:211], v[40:43], a[140:143], v[208:211]
ds_read_b64_tr_b16 a[130:131], v239 offset:8576
v_mfma_f32_16x16x32_bf16 v[208:211], a[228:231], a[136:139], v[208:211]
ds_read_b64_tr_b16 a[154:155], v245 offset:37952
v_mfma_f32_16x16x32_bf16 v[212:215], v[28:31], a[136:139], v[212:215]
ds_read_b64_tr_b16 a[132:133], v239 offset:8960
v_mfma_f32_16x16x32_bf16 v[212:215], a[232:235], a[140:143], v[212:215]
v_mfma_f32_16x16x32_bf16 v[216:219], v[44:47], a[140:143], v[216:219]
ds_read_b64_tr_b16 a[134:135], v239 offset:9088
v_mfma_f32_16x16x32_bf16 v[216:219], v[32:35], a[136:139], v[216:219]
s_waitcnt lgkmcnt(6)
v_mfma_f32_16x16x32_bf16 v[188:191], v[48:51], a[144:147], v[188:191]
ds_read_b64_tr_b16 a[136:137], v238 offset:59216
v_mfma_f32_16x16x32_bf16 v[188:191], v[60:63], a[148:151], v[188:191]
v_mfma_f32_16x16x32_bf16 v[192:195], v[64:67], a[148:151], v[192:195]
ds_read_b64_tr_b16 a[138:139], v238 offset:59344
v_mfma_f32_16x16x32_bf16 v[192:195], v[52:55], a[144:147], v[192:195]
s_waitcnt lgkmcnt(4)
buffer_store_dwordx4 a[152:155], v243, s[48:51], s72 offen offset:128 sc1
v_mfma_f32_16x16x32_bf16 v[196:199], v[56:59], a[144:147], v[196:199]
ds_read_b64_tr_b16 a[140:141], v238 offset:59728
v_mfma_f32_16x16x32_bf16 v[196:199], v[68:71], a[148:151], v[196:199]
v_mfma_f32_16x16x32_bf16 v[200:203], v[72:75], a[148:151], v[200:203]
ds_read_b64_tr_b16 a[142:143], v238 offset:59856
v_mfma_f32_16x16x32_bf16 v[200:203], a[236:239], a[144:147], v[200:203]
s_waitcnt lgkmcnt(4)
v_mfma_f32_16x16x32_bf16 v[172:175], a[240:243], a[128:131], v[172:175]
ds_read_b64_tr_b16 a[144:145], v239 offset:64
v_mfma_f32_16x16x32_bf16 v[172:175], v[88:91], a[132:135], v[172:175]
v_mfma_f32_16x16x32_bf16 v[176:179], a[244:247], a[132:135], v[176:179]
s_add_u32 m0, s61, 115584
ds_read_b64_tr_b16 a[146:147], v239 offset:192
v_mfma_f32_16x16x32_bf16 v[176:179], v[76:79], a[128:131], v[176:179]
buffer_load_dwordx4 v242, s[40:43], s83 offen offset:384 nt lds
v_mfma_f32_16x16x32_bf16 v[180:183], v[80:83], a[128:131], v[180:183]
ds_read_b64_tr_b16 a[148:149], v239 offset:576
v_mfma_f32_16x16x32_bf16 v[180:183], v[92:95], a[132:135], v[180:183]
v_mfma_f32_16x16x32_bf16 v[184:187], v[96:99], a[132:135], v[184:187]
ds_read_b64_tr_b16 a[150:151], v239 offset:704
v_mfma_f32_16x16x32_bf16 v[184:187], v[84:87], a[128:131], v[184:187]
s_waitcnt lgkmcnt(4)
v_mfma_f32_16x16x32_bf16 v[220:223], v[24:27], a[136:139], v[220:223]
ds_read_b64_tr_b16 a[128:129], v239 offset:8464
v_mfma_f32_16x16x32_bf16 v[220:223], v[36:39], a[140:143], v[220:223]
v_mfma_f32_16x16x32_bf16 v[224:227], v[40:43], a[140:143], v[224:227]
s_add_u32 m0, s61, 115600
ds_read_b64_tr_b16 a[130:131], v239 offset:8592
v_mfma_f32_16x16x32_bf16 v[224:227], a[228:231], a[136:139], v[224:227]
buffer_load_dwordx4 v242, s[40:43], s83 offen offset:1408 nt lds
v_mfma_f32_16x16x32_bf16 v[228:231], v[28:31], a[136:139], v[228:231]
ds_read_b64_tr_b16 a[132:133], v239 offset:8976
v_mfma_f32_16x16x32_bf16 v[228:231], a[232:235], a[140:143], v[228:231]
ds_read_b64_tr_b16 a[152:153], v245 offset:36928
v_mfma_f32_16x16x32_bf16 v[232:235], v[44:47], a[140:143], v[232:235]
s_waitcnt vmcnt(12) lgkmcnt(4)
ds_read_b64_tr_b16 a[134:135], v239 offset:9104
v_mfma_f32_16x16x32_bf16 v[232:235], v[32:35], a[136:139], v[232:235]
s_barrier
v_mfma_f32_16x16x32_bf16 v[204:207], v[48:51], a[144:147], v[204:207]
ds_read_b64_tr_b16 a[136:137], v238 offset:0
v_mfma_f32_16x16x32_bf16 v[204:207], v[60:63], a[148:151], v[204:207]
ds_read_b64_tr_b16 a[154:155], v245 offset:37984
v_mfma_f32_16x16x32_bf16 v[208:211], v[64:67], a[148:151], v[208:211]
ds_read_b64_tr_b16 a[138:139], v238 offset:128
v_mfma_f32_16x16x32_bf16 v[208:211], v[52:55], a[144:147], v[208:211]
v_mfma_f32_16x16x32_bf16 v[212:215], v[56:59], a[144:147], v[212:215]
ds_read_b64_tr_b16 a[140:141], v238 offset:512
v_mfma_f32_16x16x32_bf16 v[212:215], v[68:71], a[148:151], v[212:215]
v_mfma_f32_16x16x32_bf16 v[216:219], v[72:75], a[148:151], v[216:219]
ds_read_b64_tr_b16 a[142:143], v238 offset:640
v_mfma_f32_16x16x32_bf16 v[216:219], a[236:239], a[144:147], v[216:219]
s_waitcnt lgkmcnt(5)
v_mfma_f32_16x16x32_bf16 v[188:191], a[240:243], a[128:131], v[188:191]
ds_read_b64_tr_b16 a[144:145], v239 offset:80
v_mfma_f32_16x16x32_bf16 v[188:191], v[88:91], a[132:135], v[188:191]
s_waitcnt lgkmcnt(4)
buffer_store_dwordx4 a[152:155], v243, s[48:51], s73 offen offset:128 sc1
v_mfma_f32_16x16x32_bf16 v[192:195], a[244:247], a[132:135], v[192:195]
ds_read_b64_tr_b16 a[146:147], v239 offset:208
v_mfma_f32_16x16x32_bf16 v[192:195], v[76:79], a[128:131], v[192:195]
v_mfma_f32_16x16x32_bf16 v[196:199], v[80:83], a[128:131], v[196:199]
ds_read_b64_tr_b16 a[148:149], v239 offset:592
v_mfma_f32_16x16x32_bf16 v[196:199], v[92:95], a[132:135], v[196:199]
v_mfma_f32_16x16x32_bf16 v[200:203], v[96:99], a[132:135], v[200:203]
ds_read_b64_tr_b16 a[150:151], v239 offset:720
v_mfma_f32_16x16x32_bf16 v[200:203], v[84:87], a[128:131], v[200:203]
s_waitcnt lgkmcnt(4)
v_mfma_f32_16x16x32_bf16 v[172:175], v[100:103], a[136:139], v[172:175]
ds_read_b64_tr_b16 a[128:129], v239 offset:8512
v_mfma_f32_16x16x32_bf16 v[172:175], v[112:115], a[140:143], v[172:175]
v_mfma_f32_16x16x32_bf16 v[176:179], v[116:119], a[140:143], v[176:179]
s_add_u32 m0, s61, 124032
ds_read_b64_tr_b16 a[130:131], v239 offset:8640
v_mfma_f32_16x16x32_bf16 v[176:179], v[104:107], a[136:139], v[176:179]
buffer_load_dwordx4 v242, s[40:43], s85 offen offset:384 nt lds
v_mfma_f32_16x16x32_bf16 v[180:183], a[248:251], a[136:139], v[180:183]
ds_read_b64_tr_b16 a[132:133], v239 offset:9024
v_mfma_f32_16x16x32_bf16 v[180:183], v[120:123], a[140:143], v[180:183]
v_mfma_f32_16x16x32_bf16 v[184:187], a[252:255], a[140:143], v[184:187]
ds_read_b64_tr_b16 a[134:135], v239 offset:9152
v_mfma_f32_16x16x32_bf16 v[184:187], v[108:111], a[136:139], v[184:187]
s_waitcnt lgkmcnt(4)
v_mfma_f32_16x16x32_bf16 v[220:223], v[48:51], a[144:147], v[220:223]
ds_read_b64_tr_b16 a[136:137], v238 offset:16
v_mfma_f32_16x16x32_bf16 v[220:223], v[60:63], a[148:151], v[220:223]
v_mfma_f32_16x16x32_bf16 v[224:227], v[64:67], a[148:151], v[224:227]
ds_read_b64_tr_b16 a[138:139], v238 offset:144
v_mfma_f32_16x16x32_bf16 v[224:227], v[52:55], a[144:147], v[224:227]
ds_read_b64_tr_b16 a[152:153], v245 offset:36960
v_mfma_f32_16x16x32_bf16 v[228:231], v[56:59], a[144:147], v[228:231]
ds_read_b64_tr_b16 a[140:141], v238 offset:528
v_mfma_f32_16x16x32_bf16 v[228:231], v[68:71], a[148:151], v[228:231]
ds_read_b64_tr_b16 a[154:155], v245 offset:38016
v_mfma_f32_16x16x32_bf16 v[232:235], v[72:75], a[148:151], v[232:235]
s_add_u32 m0, s61, 124048
ds_read_b64_tr_b16 a[142:143], v238 offset:656
v_mfma_f32_16x16x32_bf16 v[232:235], a[236:239], a[144:147], v[232:235]
s_waitcnt lgkmcnt(6)
buffer_load_dwordx4 v242, s[40:43], s85 offen offset:1408 nt lds
v_mfma_f32_16x16x32_bf16 v[204:207], a[240:243], a[128:131], v[204:207]
ds_read_b64_tr_b16 a[144:145], v238 offset:8448
v_mfma_f32_16x16x32_bf16 v[204:207], v[88:91], a[132:135], v[204:207]
v_mfma_f32_16x16x32_bf16 v[208:211], a[244:247], a[132:135], v[208:211]
ds_read_b64_tr_b16 a[146:147], v238 offset:8576
v_mfma_f32_16x16x32_bf16 v[208:211], v[76:79], a[128:131], v[208:211]
v_mfma_f32_16x16x32_bf16 v[212:215], v[80:83], a[128:131], v[212:215]
ds_read_b64_tr_b16 a[148:149], v238 offset:8960
v_mfma_f32_16x16x32_bf16 v[212:215], v[92:95], a[132:135], v[212:215]
s_waitcnt lgkmcnt(4)
buffer_store_dwordx4 a[152:155], v243, s[48:51], s74 offen offset:128 sc1
v_mfma_f32_16x16x32_bf16 v[216:219], v[96:99], a[132:135], v[216:219]
ds_read_b64_tr_b16 a[150:151], v238 offset:9088
v_mfma_f32_16x16x32_bf16 v[216:219], v[84:87], a[128:131], v[216:219]
s_waitcnt lgkmcnt(4)
v_mfma_f32_16x16x32_bf16 v[188:191], v[100:103], a[136:139], v[188:191]
.Lpad8_%=:
s_cbranch_vccnz .Lret2_%=
ds_read_b64_tr_b16 a[128:129], v239 offset:8528
v_mfma_f32_16x16x32_bf16 v[188:191], v[112:115], a[140:143], v[188:191]
v_mfma_f32_16x16x32_bf16 v[192:195], v[116:119], a[140:143], v[192:195]
ds_read_b64_tr_b16 a[130:131], v239 offset:8656
)"
R"(
v_mfma_f32_16x16x32_bf16 v[192:195], v[104:107], a[136:139], v[192:195]
v_mfma_f32_16x16x32_bf16 v[196:199], a[248:251], a[136:139], v[196:199]
ds_read_b64_tr_b16 a[132:133], v239 offset:9040
v_mfma_f32_16x16x32_bf16 v[196:199], v[120:123], a[140:143], v[196:199]
v_mfma_f32_16x16x32_bf16 v[200:203], a[252:255], a[140:143], v[200:203]
ds_read_b64_tr_b16 a[134:135], v239 offset:9168
v_mfma_f32_16x16x32_bf16 v[200:203], v[108:111], a[136:139], v[200:203]
s_waitcnt lgkmcnt(4)
v_mfma_f32_16x16x32_bf16 v[172:175], v[124:127], a[144:147], v[172:175]
s_add_u32 m0, s61, 132480
ds_read_b64_tr_b16 a[136:137], v238 offset:64
v_mfma_f32_16x16x32_bf16 v[172:175], v[140:143], a[148:151], v[172:175]
buffer_load_dwordx4 v242, s[40:43], s87 offen offset:384 nt lds
v_mfma_f32_16x16x32_bf16 v[176:179], v[144:147], a[148:151], v[176:179]
ds_read_b64_tr_b16 a[138:139], v238 offset:192
v_mfma_f32_16x16x32_bf16 v[176:179], v[128:131], a[144:147], v[176:179]
v_mfma_f32_16x16x32_bf16 v[180:183], v[132:135], a[144:147], v[180:183]
ds_read_b64_tr_b16 a[140:141], v238 offset:576
v_mfma_f32_16x16x32_bf16 v[180:183], v[148:151], a[148:151], v[180:183]
v_mfma_f32_16x16x32_bf16 v[184:187], v[152:155], a[148:151], v[184:187]
ds_read_b64_tr_b16 a[142:143], v238 offset:704
v_mul_f32_dpp v172, v250, v172 row_newbcast:0 row_mask:0xf bank_mask:0xf
v_mfma_f32_16x16x32_bf16 v[184:187], v[136:139], a[144:147], v[184:187]
s_waitcnt lgkmcnt(4)
v_mul_f32_dpp v173, v250, v173 row_newbcast:1 row_mask:0xf bank_mask:0xf
v_mul_f32_dpp v174, v250, v174 row_newbcast:2 row_mask:0xf bank_mask:0xf
s_cmp_eq_u32 s59, 1
s_cbranch_scc1 .LX1%=
v_mfma_f32_16x16x32_bf16 v[220:223], a[240:243], a[128:131], v[220:223]
s_add_u32 m0, s61, 132496
ds_read_b64_tr_b16 a[144:145], v238 offset:8464
v_mul_f32_dpp v175, v250, v175 row_newbcast:3 row_mask:0xf bank_mask:0xf
v_mfma_f32_16x16x32_bf16 v[220:223], v[88:91], a[132:135], v[220:223]
s_add_u32 s36, s36, 0x2000000
s_addc_u32 s37, s37, 0
buffer_load_dwordx4 v242, s[40:43], s87 offen offset:1408 nt lds
v_mfma_f32_16x16x32_bf16 v[224:227], a[244:247], a[132:135], v[224:227]
s_mov_b32 s46, 0
ds_read_b64_tr_b16 a[146:147], v238 offset:8592
v_mul_f32_dpp v176, v251, v176 row_newbcast:0 row_mask:0xf bank_mask:0xf
v_mfma_f32_16x16x32_bf16 v[224:227], v[76:79], a[128:131], v[224:227]
s_mov_b32 s22, 0
ds_read_b64_tr_b16 a[152:153], v245 offset:36992
v_mul_f32_dpp v177, v251, v177 row_newbcast:1 row_mask:0xf bank_mask:0xf
v_mfma_f32_16x16x32_bf16 v[228:231], v[80:83], a[128:131], v[228:231]
s_add_u32 s24, s34, 8448
s_addc_u32 s25, s35, 0
ds_read_b64_tr_b16 a[148:149], v238 offset:8976
v_mul_f32_dpp v178, v251, v178 row_newbcast:2 row_mask:0xf bank_mask:0xf
v_mfma_f32_16x16x32_bf16 v[228:231], v[92:95], a[132:135], v[228:231]
s_mov_b32 s26, 49152
ds_read_b64_tr_b16 a[154:155], v245 offset:38048
v_mul_f32_dpp v179, v251, v179 row_newbcast:3 row_mask:0xf bank_mask:0xf
v_mfma_f32_16x16x32_bf16 v[232:235], v[96:99], a[132:135], v[232:235]
s_waitcnt vmcnt(12) lgkmcnt(5)
ds_read_b64_tr_b16 a[150:151], v238 offset:9104
v_mul_f32_dpp v180, v252, v180 row_newbcast:0 row_mask:0xf bank_mask:0xf
v_mfma_f32_16x16x32_bf16 v[232:235], v[84:87], a[128:131], v[232:235]
s_barrier
v_mfma_f32_16x16x32_bf16 v[204:207], v[100:103], a[136:139], v[204:207]
s_add_u32 s28, s34, 384
s_addc_u32 s29, s35, 0
ds_read_b64_tr_b16 a[128:129], v238 offset:16896
v_mul_f32_dpp v181, v252, v181 row_newbcast:1 row_mask:0xf bank_mask:0xf
v_mfma_f32_16x16x32_bf16 v[204:207], v[112:115], a[140:143], v[204:207]
s_mov_b32 s30, 57344
v_mul_f32_dpp v182, v252, v182 row_newbcast:2 row_mask:0xf bank_mask:0xf
v_mul_f32_dpp v183, v252, v183 row_newbcast:3 row_mask:0xf bank_mask:0xf
v_mfma_f32_16x16x32_bf16 v[208:211], v[116:119], a[140:143], v[208:211]
s_cmp_lg_u64 s[64:65], 0
s_cselect_b32 s70, 1, 0
ds_read_b64_tr_b16 a[130:131], v238 offset:17024
v_mul_f32_dpp v184, v253, v184 row_newbcast:0 row_mask:0xf bank_mask:0xf
v_mfma_f32_16x16x32_bf16 v[208:211], v[104:107], a[136:139], v[208:211]
s_mov_b32 s59, 1
v_mul_f32_dpp v185, v253, v185 row_newbcast:1 row_mask:0xf bank_mask:0xf
v_mul_f32_dpp v186, v253, v186 row_newbcast:2 row_mask:0xf bank_mask:0xf
v_mfma_f32_16x16x32_bf16 v[212:215], a[248:251], a[136:139], v[212:215]
ds_read_b64_tr_b16 a[132:133], v238 offset:17408
v_mul_f32_dpp v187, v253, v187 row_newbcast:3 row_mask:0xf bank_mask:0xf
v_mfma_f32_16x16x32_bf16 v[212:215], v[120:123], a[140:143], v[212:215]
s_waitcnt lgkmcnt(4)
buffer_store_dwordx4 a[152:155], v243, s[48:51], s75 offen offset:128 sc1
v_mov_b32 v254, v242
v_mfma_f32_16x16x32_bf16 v[216:219], a[252:255], a[140:143], v[216:219]
ds_read_b64_tr_b16 a[134:135], v238 offset:17536
v_mfma_f32_16x16x32_bf16 v[216:219], v[108:111], a[136:139], v[216:219]
s_waitcnt lgkmcnt(4)
v_mfma_f32_16x16x32_bf16 v[188:191], v[124:127], a[144:147], v[188:191]
ds_read_b64_tr_b16 a[136:137], v238 offset:80
v_mfma_f32_16x16x32_bf16 v[188:191], v[140:143], a[148:151], v[188:191]
v_mfma_f32_16x16x32_bf16 v[192:195], v[144:147], a[148:151], v[192:195]
ds_read_b64_tr_b16 a[138:139], v238 offset:208
v_mfma_f32_16x16x32_bf16 v[192:195], v[128:131], a[144:147], v[192:195]
v_mfma_f32_16x16x32_bf16 v[196:199], v[132:135], a[144:147], v[196:199]
ds_read_b64_tr_b16 a[140:141], v238 offset:592
v_mfma_f32_16x16x32_bf16 v[196:199], v[148:151], a[148:151], v[196:199]
v_mfma_f32_16x16x32_bf16 v[200:203], v[152:155], a[148:151], v[200:203]
s_add_u32 m0, s61, 140928
ds_read_b64_tr_b16 a[142:143], v238 offset:720
v_mul_f32_dpp v188, v250, v188 row_newbcast:0 row_mask:0xf bank_mask:0xf
v_mfma_f32_16x16x32_bf16 v[200:203], v[136:139], a[144:147], v[200:203]
s_waitcnt lgkmcnt(4)
buffer_load_dwordx4 v242, s[40:43], s89 offen offset:384 nt lds
v_mfma_f32_16x16x32_bf16 v[156:159], a[156:159], a[128:131], 0
ds_read_b64_tr_b16 a[144:145], v238 offset:8512
v_mul_f32_dpp v189, v250, v189 row_newbcast:1 row_mask:0xf bank_mask:0xf
v_mfma_f32_16x16x32_bf16 v[156:159], a[172:175], a[132:135], v[156:159]
v_mul_f32_dpp v190, v250, v190 row_newbcast:2 row_mask:0xf bank_mask:0xf
v_mul_f32_dpp v191, v250, v191 row_newbcast:3 row_mask:0xf bank_mask:0xf
v_mfma_f32_16x16x32_bf16 v[160:163], a[176:179], a[132:135], 0
ds_read_b64_tr_b16 a[146:147], v238 offset:8640
v_cvt_pk_bf16_f32 v188, v172, v188
v_mfma_f32_16x16x32_bf16 v[160:163], a[160:163], a[128:131], v[160:163]
v_cvt_pk_bf16_f32 v189, v173, v189
v_cvt_pk_bf16_f32 v190, v174, v190
v_mfma_f32_16x16x32_bf16 v[164:167], a[164:167], a[128:131], 0
ds_read_b64_tr_b16 a[148:149], v238 offset:9024
v_cvt_pk_bf16_f32 v191, v175, v191
v_mfma_f32_16x16x32_bf16 v[164:167], a[180:183], a[132:135], v[164:167]
v_mul_f32_dpp v192, v251, v192 row_newbcast:0 row_mask:0xf bank_mask:0xf
v_mul_f32_dpp v193, v251, v193 row_newbcast:1 row_mask:0xf bank_mask:0xf
v_mfma_f32_16x16x32_bf16 v[168:171], a[184:187], a[132:135], 0
ds_read_b64_tr_b16 a[150:151], v238 offset:9152
v_mul_f32_dpp v194, v251, v194 row_newbcast:2 row_mask:0xf bank_mask:0xf
v_mfma_f32_16x16x32_bf16 v[168:171], a[168:171], a[128:131], v[168:171]
s_waitcnt lgkmcnt(4)
ds_read_b64_tr_b16 a[152:153], v245 offset:37024
v_mul_f32_dpp v195, v251, v195 row_newbcast:3 row_mask:0xf bank_mask:0xf
v_mfma_f32_16x16x32_bf16 v[220:223], v[100:103], a[136:139], v[220:223]
ds_read_b64_tr_b16 a[128:129], v238 offset:16912
v_cvt_pk_bf16_f32 v192, v176, v192
v_mfma_f32_16x16x32_bf16 v[220:223], v[112:115], a[140:143], v[220:223]
ds_read_b64_tr_b16 a[154:155], v245 offset:38080
v_cvt_pk_bf16_f32 v193, v177, v193
v_mfma_f32_16x16x32_bf16 v[224:227], v[116:119], a[140:143], v[224:227]
s_add_u32 m0, s61, 140944
ds_read_b64_tr_b16 a[130:131], v238 offset:17040
v_cvt_pk_bf16_f32 v194, v178, v194
v_mfma_f32_16x16x32_bf16 v[224:227], v[104:107], a[136:139], v[224:227]
buffer_load_dwordx4 v242, s[40:43], s89 offen offset:1408 nt lds
v_mfma_f32_16x16x32_bf16 v[228:231], a[248:251], a[136:139], v[228:231]
ds_read_b64_tr_b16 a[132:133], v238 offset:17424
v_cvt_pk_bf16_f32 v195, v179, v195
v_mfma_f32_16x16x32_bf16 v[228:231], v[120:123], a[140:143], v[228:231]
v_mul_f32_dpp v196, v252, v196 row_newbcast:0 row_mask:0xf bank_mask:0xf
v_mul_f32_dpp v197, v252, v197 row_newbcast:1 row_mask:0xf bank_mask:0xf
v_mfma_f32_16x16x32_bf16 v[232:235], a[252:255], a[140:143], v[232:235]
ds_read_b64_tr_b16 a[134:135], v238 offset:17552
v_mul_f32_dpp v198, v252, v198 row_newbcast:2 row_mask:0xf bank_mask:0xf
v_mfma_f32_16x16x32_bf16 v[232:235], v[108:111], a[136:139], v[232:235]
s_waitcnt lgkmcnt(6)
v_mul_f32_dpp v199, v252, v199 row_newbcast:3 row_mask:0xf bank_mask:0xf
v_cvt_pk_bf16_f32 v196, v180, v196
v_mfma_f32_16x16x32_bf16 v[204:207], v[124:127], a[144:147], v[204:207]
ds_read_b64_tr_b16 a[136:137], v238 offset:25344
v_cvt_pk_bf16_f32 v197, v181, v197
v_mfma_f32_16x16x32_bf16 v[204:207], v[140:143], a[148:151], v[204:207]
s_waitcnt lgkmcnt(4)
buffer_store_dwordx4 a[152:155], v243, s[48:51], s76 offen offset:128 sc1
v_cvt_pk_bf16_f32 v198, v182, v198
v_cvt_pk_bf16_f32 v199, v183, v199
v_mul_f32_dpp v200, v253, v200 row_newbcast:0 row_mask:0xf bank_mask:0xf
v_mfma_f32_16x16x32_bf16 v[208:211], v[144:147], a[148:151], v[208:211]
ds_read_b64_tr_b16 a[138:139], v238 offset:25472
v_mul_f32_dpp v201, v253, v201 row_newbcast:1 row_mask:0xf bank_mask:0xf
v_mfma_f32_16x16x32_bf16 v[208:211], v[128:131], a[144:147], v[208:211]
v_mul_f32_dpp v202, v253, v202 row_newbcast:2 row_mask:0xf bank_mask:0xf
v_mul_f32_dpp v203, v253, v203 row_newbcast:3 row_mask:0xf bank_mask:0xf
v_mfma_f32_16x16x32_bf16 v[212:215], v[132:135], a[144:147], v[212:215]
ds_read_b64_tr_b16 a[140:141], v238 offset:25856
v_cvt_pk_bf16_f32 v200, v184, v200
v_mfma_f32_16x16x32_bf16 v[212:215], v[148:151], a[148:151], v[212:215]
v_cvt_pk_bf16_f32 v201, v185, v201
v_cvt_pk_bf16_f32 v202, v186, v202
v_mfma_f32_16x16x32_bf16 v[216:219], v[152:155], a[148:151], v[216:219]
ds_read_b64_tr_b16 a[142:143], v238 offset:25984
v_cvt_pk_bf16_f32 v203, v187, v203
v_mfma_f32_16x16x32_bf16 v[216:219], v[136:139], a[144:147], v[216:219]
s_waitcnt lgkmcnt(4)
ds_write_b64 v244, v[188:189] offset:0
v_mfma_f32_16x16x32_bf16 v[172:175], a[156:159], a[128:131], 0
ds_read_b64_tr_b16 a[144:145], v238 offset:8528
v_mfma_f32_16x16x32_bf16 v[172:175], a[172:175], a[132:135], v[172:175]
ds_write_b64 v244, v[190:191] offset:8
v_mfma_f32_16x16x32_bf16 v[176:179], a[176:179], a[132:135], 0
ds_read_b64_tr_b16 a[146:147], v238 offset:8656
v_mfma_f32_16x16x32_bf16 v[176:179], a[160:163], a[128:131], v[176:179]
ds_write_b64 v244, v[192:193] offset:64
v_mfma_f32_16x16x32_bf16 v[180:183], a[164:167], a[128:131], 0
s_add_u32 m0, s61, 149376
ds_read_b64_tr_b16 a[148:149], v238 offset:9040
v_mfma_f32_16x16x32_bf16 v[180:183], a[180:183], a[132:135], v[180:183]
buffer_load_dwordx4 v242, s[40:43], s91 offen offset:384 nt lds
v_mfma_f32_16x16x32_bf16 v[184:187], a[184:187], a[132:135], 0
ds_read_b64_tr_b16 a[150:151], v238 offset:9168
v_mul_f32_dpp v204, v250, v204 row_newbcast:0 row_mask:0xf bank_mask:0xf
v_mfma_f32_16x16x32_bf16 v[184:187], a[168:171], a[128:131], v[184:187]
s_waitcnt lgkmcnt(7)
ds_write_b64 v244, v[194:195] offset:72
v_mfma_f32_16x16x32_bf16 v[156:159], a[188:191], a[136:139], v[156:159]
ds_read_b64_tr_b16 a[128:129], v238 offset:16960
v_mul_f32_dpp v205, v250, v205 row_newbcast:1 row_mask:0xf bank_mask:0xf
v_mfma_f32_16x16x32_bf16 v[156:159], a[204:207], a[140:143], v[156:159]
ds_write_b64 v244, v[196:197] offset:128
v_mfma_f32_16x16x32_bf16 v[160:163], a[208:211], a[140:143], v[160:163]
ds_read_b64_tr_b16 a[130:131], v238 offset:17088
v_mul_f32_dpp v206, v250, v206 row_newbcast:2 row_mask:0xf bank_mask:0xf
v_mfma_f32_16x16x32_bf16 v[160:163], a[192:195], a[136:139], v[160:163]
ds_write_b64 v244, v[198:199] offset:136
v_mfma_f32_16x16x32_bf16 v[164:167], a[196:199], a[136:139], v[164:167]
s_add_u32 m0, s61, 149392
ds_read_b64_tr_b16 a[132:133], v238 offset:17472
v_mul_f32_dpp v207, v250, v207 row_newbcast:3 row_mask:0xf bank_mask:0xf
v_mfma_f32_16x16x32_bf16 v[164:167], a[212:215], a[140:143], v[164:167]
buffer_load_dwordx4 v242, s[40:43], s91 offen offset:1408 nt lds
)"
R"(
v_mfma_f32_16x16x32_bf16 v[168:171], a[216:219], a[140:143], v[168:171]
ds_read_b64_tr_b16 a[134:135], v238 offset:17600
v_mul_f32_dpp v208, v251, v208 row_newbcast:0 row_mask:0xf bank_mask:0xf
v_mfma_f32_16x16x32_bf16 v[168:171], a[200:203], a[136:139], v[168:171]
s_waitcnt lgkmcnt(7)
ds_read_b64_tr_b16 a[152:153], v245 offset:37056
v_mul_f32_dpp v209, v251, v209 row_newbcast:1 row_mask:0xf bank_mask:0xf
v_mfma_f32_16x16x32_bf16 v[220:223], v[124:127], a[144:147], v[220:223]
s_add_u32 s40, s40, 0x2000000
s_addc_u32 s41, s41, 0
ds_read_b64_tr_b16 a[136:137], v238 offset:25360
v_mul_f32_dpp v210, v251, v210 row_newbcast:2 row_mask:0xf bank_mask:0xf
v_mfma_f32_16x16x32_bf16 v[220:223], v[140:143], a[148:151], v[220:223]
ds_read_b64_tr_b16 a[154:155], v245 offset:38112
v_mul_f32_dpp v211, v251, v211 row_newbcast:3 row_mask:0xf bank_mask:0xf
v_mfma_f32_16x16x32_bf16 v[224:227], v[144:147], a[148:151], v[224:227]
ds_read_b64_tr_b16 a[138:139], v238 offset:25488
v_mul_f32_dpp v212, v252, v212 row_newbcast:0 row_mask:0xf bank_mask:0xf
v_mfma_f32_16x16x32_bf16 v[224:227], v[128:131], a[144:147], v[224:227]
ds_write_b64 v244, v[200:201] offset:192
v_mfma_f32_16x16x32_bf16 v[228:231], v[132:135], a[144:147], v[228:231]
ds_read_b64_tr_b16 a[140:141], v238 offset:25872
v_mul_f32_dpp v213, v252, v213 row_newbcast:1 row_mask:0xf bank_mask:0xf
v_mfma_f32_16x16x32_bf16 v[228:231], v[148:151], a[148:151], v[228:231]
ds_write_b64 v244, v[202:203] offset:200
v_mfma_f32_16x16x32_bf16 v[232:235], v[152:155], a[148:151], v[232:235]
s_waitcnt vmcnt(12) lgkmcnt(7)
ds_read_b64_tr_b16 a[142:143], v238 offset:26000
v_mul_f32_dpp v214, v252, v214 row_newbcast:2 row_mask:0xf bank_mask:0xf
v_mfma_f32_16x16x32_bf16 v[232:235], v[136:139], a[144:147], v[232:235]
s_barrier
v_mfma_f32_16x16x32_bf16 v[188:191], a[156:159], a[128:131], 0
ds_read_b64_tr_b16 a[144:145], v238 offset:33792
v_mul_f32_dpp v215, v252, v215 row_newbcast:3 row_mask:0xf bank_mask:0xf
v_mfma_f32_16x16x32_bf16 v[188:191], a[172:175], a[132:135], v[188:191]
s_waitcnt lgkmcnt(6)
buffer_store_dwordx4 a[152:155], v243, s[48:51], s77 offen offset:128 sc1
v_mul_f32_dpp v216, v253, v216 row_newbcast:0 row_mask:0xf bank_mask:0xf
v_mul_f32_dpp v217, v253, v217 row_newbcast:1 row_mask:0xf bank_mask:0xf
v_mul_f32_dpp v218, v253, v218 row_newbcast:2 row_mask:0xf bank_mask:0xf
v_mfma_f32_16x16x32_bf16 v[192:195], a[176:179], a[132:135], 0
ds_read_b64_tr_b16 a[146:147], v238 offset:33920
v_mul_f32_dpp v219, v253, v219 row_newbcast:3 row_mask:0xf bank_mask:0xf
v_mfma_f32_16x16x32_bf16 v[192:195], a[160:163], a[128:131], v[192:195]
v_mul_f32_dpp v220, v250, v220 row_newbcast:0 row_mask:0xf bank_mask:0xf
v_mul_f32_dpp v221, v250, v221 row_newbcast:1 row_mask:0xf bank_mask:0xf
v_mfma_f32_16x16x32_bf16 v[196:199], a[164:167], a[128:131], 0
ds_read_b64_tr_b16 a[148:149], v238 offset:34304
v_mul_f32_dpp v222, v250, v222 row_newbcast:2 row_mask:0xf bank_mask:0xf
v_mfma_f32_16x16x32_bf16 v[196:199], a[180:183], a[132:135], v[196:199]
v_mul_f32_dpp v223, v250, v223 row_newbcast:3 row_mask:0xf bank_mask:0xf
v_cvt_pk_bf16_f32 v220, v204, v220
v_mfma_f32_16x16x32_bf16 v[200:203], a[184:187], a[132:135], 0
ds_read_b64_tr_b16 a[150:151], v238 offset:34432
v_cvt_pk_bf16_f32 v221, v205, v221
v_mfma_f32_16x16x32_bf16 v[200:203], a[168:171], a[128:131], v[200:203]
s_waitcnt lgkmcnt(4)
v_cvt_pk_bf16_f32 v222, v206, v222
v_cvt_pk_bf16_f32 v223, v207, v223
v_mfma_f32_16x16x32_bf16 v[172:175], a[188:191], a[136:139], v[172:175]
ds_read_b64_tr_b16 a[128:129], v238 offset:16976
v_mul_f32_dpp v224, v251, v224 row_newbcast:0 row_mask:0xf bank_mask:0xf
v_mfma_f32_16x16x32_bf16 v[172:175], a[204:207], a[140:143], v[172:175]
v_mul_f32_dpp v225, v251, v225 row_newbcast:1 row_mask:0xf bank_mask:0xf
v_mul_f32_dpp v226, v251, v226 row_newbcast:2 row_mask:0xf bank_mask:0xf
v_mfma_f32_16x16x32_bf16 v[176:179], a[208:211], a[140:143], v[176:179]
ds_read_b64_tr_b16 a[130:131], v238 offset:17104
v_mul_f32_dpp v227, v251, v227 row_newbcast:3 row_mask:0xf bank_mask:0xf
v_mfma_f32_16x16x32_bf16 v[176:179], a[192:195], a[136:139], v[176:179]
v_cvt_pk_bf16_f32 v224, v208, v224
v_cvt_pk_bf16_f32 v225, v209, v225
v_mfma_f32_16x16x32_bf16 v[180:183], a[196:199], a[136:139], v[180:183]
s_add_u32 m0, s60, 73728
ds_read_b64_tr_b16 a[132:133], v238 offset:17488
v_cvt_pk_bf16_f32 v226, v210, v226
v_mfma_f32_16x16x32_bf16 v[180:183], a[212:215], a[140:143], v[180:183]
buffer_load_dwordx4 v241, s[36:39], 0 offen offset:0 nt lds
v_mfma_f32_16x16x32_bf16 v[184:187], a[216:219], a[140:143], v[184:187]
ds_read_b64_tr_b16 a[134:135], v238 offset:17616
v_cvt_pk_bf16_f32 v227, v211, v227
v_mfma_f32_16x16x32_bf16 v[184:187], a[200:203], a[136:139], v[184:187]
s_waitcnt lgkmcnt(4)
v_mul_f32_dpp v228, v252, v228 row_newbcast:0 row_mask:0xf bank_mask:0xf
v_mul_f32_dpp v229, v252, v229 row_newbcast:1 row_mask:0xf bank_mask:0xf
v_mfma_f32_16x16x32_bf16 v[156:159], a[220:223], a[144:147], v[156:159]
ds_read_b64_tr_b16 a[136:137], v238 offset:25408
v_mul_f32_dpp v230, v252, v230 row_newbcast:2 row_mask:0xf bank_mask:0xf
v_mfma_f32_16x16x32_bf16 v[156:159], a[224:227], a[148:151], v[156:159]
v_mul_f32_dpp v231, v252, v231 row_newbcast:3 row_mask:0xf bank_mask:0xf
v_cvt_pk_bf16_f32 v228, v212, v228
v_mfma_f32_16x16x32_bf16 v[160:163], v[12:15], a[148:151], v[160:163]
ds_read_b64_tr_b16 a[138:139], v238 offset:25536
v_cvt_pk_bf16_f32 v229, v213, v229
v_mfma_f32_16x16x32_bf16 v[160:163], v[0:3], a[144:147], v[160:163]
ds_read_b64_tr_b16 a[152:153], v245 offset:37088
v_cvt_pk_bf16_f32 v230, v214, v230
v_mfma_f32_16x16x32_bf16 v[164:167], v[4:7], a[144:147], v[164:167]
s_add_u32 m0, s60, 73744
ds_read_b64_tr_b16 a[140:141], v238 offset:25920
v_cvt_pk_bf16_f32 v231, v215, v231
v_mfma_f32_16x16x32_bf16 v[164:167], v[16:19], a[148:151], v[164:167]
buffer_load_dwordx4 v241, s[36:39], 0 offen offset:1024 nt lds
v_mfma_f32_16x16x32_bf16 v[168:171], v[20:23], a[148:151], v[168:171]
ds_read_b64_tr_b16 a[142:143], v238 offset:26048
v_mul_f32_dpp v232, v253, v232 row_newbcast:0 row_mask:0xf bank_mask:0xf
v_mfma_f32_16x16x32_bf16 v[168:171], v[8:11], a[144:147], v[168:171]
s_waitcnt lgkmcnt(5)
ds_read_b64_tr_b16 a[154:155], v245 offset:38144
v_mul_f32_dpp v233, v253, v233 row_newbcast:1 row_mask:0xf bank_mask:0xf
v_mfma_f32_16x16x32_bf16 v[204:207], a[156:159], a[128:131], 0
ds_read_b64_tr_b16 a[144:145], v238 offset:33808
v_mul_f32_dpp v234, v253, v234 row_newbcast:2 row_mask:0xf bank_mask:0xf
v_mfma_f32_16x16x32_bf16 v[204:207], a[172:175], a[132:135], v[204:207]
v_mul_f32_dpp v235, v253, v235 row_newbcast:3 row_mask:0xf bank_mask:0xf
v_cvt_pk_bf16_f32 v232, v216, v232
v_mfma_f32_16x16x32_bf16 v[208:211], a[176:179], a[132:135], 0
ds_read_b64_tr_b16 a[146:147], v238 offset:33936
v_cvt_pk_bf16_f32 v233, v217, v233
v_mfma_f32_16x16x32_bf16 v[208:211], a[160:163], a[128:131], v[208:211]
v_cvt_pk_bf16_f32 v234, v218, v234
v_cvt_pk_bf16_f32 v235, v219, v235
v_mfma_f32_16x16x32_bf16 v[212:215], a[164:167], a[128:131], 0
ds_read_b64_tr_b16 a[148:149], v238 offset:34320
v_mfma_f32_16x16x32_bf16 v[212:215], a[180:183], a[132:135], v[212:215]
ds_write_b64 v244, v[220:221] offset:4224
v_mfma_f32_16x16x32_bf16 v[216:219], a[184:187], a[132:135], 0
ds_read_b64_tr_b16 a[150:151], v238 offset:34448
v_mfma_f32_16x16x32_bf16 v[216:219], a[168:171], a[128:131], v[216:219]
s_waitcnt lgkmcnt(5)
buffer_store_dwordx4 a[152:155], v243, s[48:51], s78 offen offset:128 sc1
v_mfma_f32_16x16x32_bf16 v[188:191], a[188:191], a[136:139], v[188:191]
ds_read_b64_tr_b16 a[128:129], v238 offset:42240
v_mfma_f32_16x16x32_bf16 v[188:191], a[204:207], a[140:143], v[188:191]
ds_write_b64 v244, v[222:223] offset:4232
v_mfma_f32_16x16x32_bf16 v[192:195], a[208:211], a[140:143], v[192:195]
ds_read_b64_tr_b16 a[130:131], v238 offset:42368
v_mfma_f32_16x16x32_bf16 v[192:195], a[192:195], a[136:139], v[192:195]
ds_write_b64 v244, v[224:225] offset:4288
v_mfma_f32_16x16x32_bf16 v[196:199], a[196:199], a[136:139], v[196:199]
ds_read_b64_tr_b16 a[132:133], v238 offset:42752
v_mfma_f32_16x16x32_bf16 v[196:199], a[212:215], a[140:143], v[196:199]
ds_write_b64 v244, v[226:227] offset:4296
v_mfma_f32_16x16x32_bf16 v[200:203], a[216:219], a[140:143], v[200:203]
ds_read_b64_tr_b16 a[134:135], v238 offset:42880
v_mfma_f32_16x16x32_bf16 v[200:203], a[200:203], a[136:139], v[200:203]
s_waitcnt lgkmcnt(7)
ds_write_b64 v244, v[228:229] offset:4352
v_mfma_f32_16x16x32_bf16 v[172:175], a[220:223], a[144:147], v[172:175]
s_add_u32 m0, s60, 73760
ds_read_b64_tr_b16 a[136:137], v238 offset:25424
v_mfma_f32_16x16x32_bf16 v[172:175], a[224:227], a[148:151], v[172:175]
buffer_load_dwordx4 v241, s[36:39], 0 offen offset:2048 nt lds
v_mfma_f32_16x16x32_bf16 v[176:179], v[12:15], a[148:151], v[176:179]
ds_read_b64_tr_b16 a[138:139], v238 offset:25552
v_mfma_f32_16x16x32_bf16 v[176:179], v[0:3], a[144:147], v[176:179]
ds_write_b64 v244, v[230:231] offset:4360
v_mfma_f32_16x16x32_bf16 v[180:183], v[4:7], a[144:147], v[180:183]
ds_read_b64_tr_b16 a[140:141], v238 offset:25936
v_mfma_f32_16x16x32_bf16 v[180:183], v[16:19], a[148:151], v[180:183]
ds_write_b64 v244, v[232:233] offset:4416
v_mfma_f32_16x16x32_bf16 v[184:187], v[20:23], a[148:151], v[184:187]
ds_read_b64_tr_b16 a[142:143], v238 offset:26064
v_mfma_f32_16x16x32_bf16 v[184:187], v[8:11], a[144:147], v[184:187]
s_waitcnt lgkmcnt(7)
ds_write_b64 v244, v[234:235] offset:4424
v_mfma_f32_16x16x32_bf16 v[156:159], v[24:27], a[128:131], v[156:159]
s_add_u32 m0, s60, 73776
ds_read_b64_tr_b16 a[144:145], v238 offset:33856
v_mfma_f32_16x16x32_bf16 v[156:159], v[36:39], a[132:135], v[156:159]
buffer_load_dwordx4 v241, s[36:39], 0 offen offset:3072 nt lds
v_mfma_f32_16x16x32_bf16 v[160:163], v[40:43], a[132:135], v[160:163]
ds_read_b64_tr_b16 a[146:147], v238 offset:33984
v_mfma_f32_16x16x32_bf16 v[160:163], a[228:231], a[128:131], v[160:163]
ds_read_b64_tr_b16 a[152:153], v245 offset:0
v_mfma_f32_16x16x32_bf16 v[164:167], v[28:31], a[128:131], v[164:167]
ds_read_b64_tr_b16 a[148:149], v238 offset:34368
v_mfma_f32_16x16x32_bf16 v[164:167], a[232:235], a[132:135], v[164:167]
ds_read_b64_tr_b16 a[154:155], v245 offset:1056
v_mfma_f32_16x16x32_bf16 v[168:171], v[44:47], a[132:135], v[168:171]
ds_read_b64_tr_b16 a[150:151], v238 offset:34496
v_mfma_f32_16x16x32_bf16 v[168:171], v[32:35], a[128:131], v[168:171]
s_waitcnt lgkmcnt(7)
v_mfma_f32_16x16x32_bf16 v[204:207], a[188:191], a[136:139], v[204:207]
ds_read_b64_tr_b16 a[128:129], v238 offset:42256
v_mfma_f32_16x16x32_bf16 v[204:207], a[204:207], a[140:143], v[204:207]
v_mfma_f32_16x16x32_bf16 v[208:211], a[208:211], a[140:143], v[208:211]
ds_read_b64_tr_b16 a[130:131], v238 offset:42384
v_mfma_f32_16x16x32_bf16 v[208:211], a[192:195], a[136:139], v[208:211]
v_mfma_f32_16x16x32_bf16 v[212:215], a[196:199], a[136:139], v[212:215]
ds_read_b64_tr_b16 a[132:133], v238 offset:42768
v_mfma_f32_16x16x32_bf16 v[212:215], a[212:215], a[140:143], v[212:215]
s_waitcnt lgkmcnt(4)
buffer_store_dwordx4 a[152:155], v243, s[48:51], 0 offen offset:256 sc1
v_mfma_f32_16x16x32_bf16 v[216:219], a[216:219], a[140:143], v[216:219]
s_waitcnt vmcnt(13) lgkmcnt(3)
ds_read_b64_tr_b16 a[134:135], v238 offset:42896
v_mfma_f32_16x16x32_bf16 v[216:219], a[200:203], a[136:139], v[216:219]
s_barrier
v_mfma_f32_16x16x32_bf16 v[188:191], a[220:223], a[144:147], v[188:191]
ds_read_b64_tr_b16 a[136:137], v238 offset:50688
v_mfma_f32_16x16x32_bf16 v[188:191], a[224:227], a[148:151], v[188:191]
v_mfma_f32_16x16x32_bf16 v[192:195], v[12:15], a[148:151], v[192:195]
ds_read_b64_tr_b16 a[138:139], v238 offset:50816
v_mfma_f32_16x16x32_bf16 v[192:195], v[0:3], a[144:147], v[192:195]
v_mfma_f32_16x16x32_bf16 v[196:199], v[4:7], a[144:147], v[196:199]
ds_read_b64_tr_b16 a[140:141], v238 offset:51200
v_mfma_f32_16x16x32_bf16 v[196:199], v[16:19], a[148:151], v[196:199]
v_mfma_f32_16x16x32_bf16 v[200:203], v[20:23], a[148:151], v[200:203]
ds_read_b64_tr_b16 a[142:143], v238 offset:51328
v_mfma_f32_16x16x32_bf16 v[200:203], v[8:11], a[144:147], v[200:203]
)"
R"(
s_waitcnt lgkmcnt(4)
v_mfma_f32_16x16x32_bf16 v[172:175], v[24:27], a[128:131], v[172:175]
s_add_u32 m0, s60, 90624
ds_read_b64_tr_b16 a[144:145], v238 offset:33872
v_mfma_f32_16x16x32_bf16 v[172:175], v[36:39], a[132:135], v[172:175]
buffer_load_dwordx4 v241, s[36:39], s75 offen offset:0 nt lds
v_mfma_f32_16x16x32_bf16 v[176:179], v[40:43], a[132:135], v[176:179]
ds_read_b64_tr_b16 a[146:147], v238 offset:34000
v_mfma_f32_16x16x32_bf16 v[176:179], a[228:231], a[128:131], v[176:179]
v_mfma_f32_16x16x32_bf16 v[180:183], v[28:31], a[128:131], v[180:183]
ds_read_b64_tr_b16 a[148:149], v238 offset:34384
v_mfma_f32_16x16x32_bf16 v[180:183], a[232:235], a[132:135], v[180:183]
ds_read_b64_tr_b16 a[152:153], v245 offset:32
v_mfma_f32_16x16x32_bf16 v[184:187], v[44:47], a[132:135], v[184:187]
ds_read_b64_tr_b16 a[150:151], v238 offset:34512
v_mfma_f32_16x16x32_bf16 v[184:187], v[32:35], a[128:131], v[184:187]
s_waitcnt lgkmcnt(5)
ds_read_b64_tr_b16 a[154:155], v245 offset:1088
v_mfma_f32_16x16x32_bf16 v[156:159], v[48:51], a[136:139], v[156:159]
ds_read_b64_tr_b16 a[128:129], v238 offset:42304
v_mfma_f32_16x16x32_bf16 v[156:159], v[60:63], a[140:143], v[156:159]
v_mfma_f32_16x16x32_bf16 v[160:163], v[64:67], a[140:143], v[160:163]
ds_read_b64_tr_b16 a[130:131], v238 offset:42432
v_mfma_f32_16x16x32_bf16 v[160:163], v[52:55], a[136:139], v[160:163]
v_mfma_f32_16x16x32_bf16 v[164:167], v[56:59], a[136:139], v[164:167]
s_add_u32 m0, s60, 90640
ds_read_b64_tr_b16 a[132:133], v238 offset:42816
v_mfma_f32_16x16x32_bf16 v[164:167], v[68:71], a[140:143], v[164:167]
buffer_load_dwordx4 v241, s[36:39], s75 offen offset:1024 nt lds
v_mfma_f32_16x16x32_bf16 v[168:171], v[72:75], a[140:143], v[168:171]
ds_read_b64_tr_b16 a[134:135], v238 offset:42944
v_mfma_f32_16x16x32_bf16 v[168:171], a[236:239], a[136:139], v[168:171]
s_waitcnt lgkmcnt(4)
buffer_store_dwordx4 a[152:155], v243, s[48:51], s72 offen offset:256 sc1
v_mfma_f32_16x16x32_bf16 v[204:207], a[220:223], a[144:147], v[204:207]
ds_read_b64_tr_b16 a[136:137], v238 offset:50704
v_mfma_f32_16x16x32_bf16 v[204:207], a[224:227], a[148:151], v[204:207]
v_mfma_f32_16x16x32_bf16 v[208:211], v[12:15], a[148:151], v[208:211]
ds_read_b64_tr_b16 a[138:139], v238 offset:50832
v_mfma_f32_16x16x32_bf16 v[208:211], v[0:3], a[144:147], v[208:211]
v_mfma_f32_16x16x32_bf16 v[212:215], v[4:7], a[144:147], v[212:215]
ds_read_b64_tr_b16 a[140:141], v238 offset:51216
v_mfma_f32_16x16x32_bf16 v[212:215], v[16:19], a[148:151], v[212:215]
v_mfma_f32_16x16x32_bf16 v[216:219], v[20:23], a[148:151], v[216:219]
ds_read_b64_tr_b16 a[142:143], v238 offset:51344
v_mfma_f32_16x16x32_bf16 v[216:219], v[8:11], a[144:147], v[216:219]
s_waitcnt lgkmcnt(4)
v_mfma_f32_16x16x32_bf16 v[188:191], v[24:27], a[128:131], v[188:191]
ds_read_b64_tr_b16 a[144:145], v238 offset:59136
v_mfma_f32_16x16x32_bf16 v[188:191], v[36:39], a[132:135], v[188:191]
v_mfma_f32_16x16x32_bf16 v[192:195], v[40:43], a[132:135], v[192:195]
ds_read_b64_tr_b16 a[146:147], v238 offset:59264
v_mfma_f32_16x16x32_bf16 v[192:195], a[228:231], a[128:131], v[192:195]
v_mfma_f32_16x16x32_bf16 v[196:199], v[28:31], a[128:131], v[196:199]
ds_read_b64_tr_b16 a[148:149], v238 offset:59648
v_mfma_f32_16x16x32_bf16 v[196:199], a[232:235], a[132:135], v[196:199]
v_mfma_f32_16x16x32_bf16 v[200:203], v[44:47], a[132:135], v[200:203]
s_add_u32 m0, s60, 90656
ds_read_b64_tr_b16 a[150:151], v238 offset:59776
v_mfma_f32_16x16x32_bf16 v[200:203], v[32:35], a[128:131], v[200:203]
s_waitcnt lgkmcnt(4)
buffer_load_dwordx4 v241, s[36:39], s75 offen offset:2048 nt lds
v_mfma_f32_16x16x32_bf16 v[172:175], v[48:51], a[136:139], v[172:175]
ds_read_b64_tr_b16 a[128:129], v238 offset:42320
v_mfma_f32_16x16x32_bf16 v[172:175], v[60:63], a[140:143], v[172:175]
ds_read_b64_tr_b16 a[152:153], v245 offset:64
v_mfma_f32_16x16x32_bf16 v[176:179], v[64:67], a[140:143], v[176:179]
ds_read_b64_tr_b16 a[130:131], v238 offset:42448
v_mfma_f32_16x16x32_bf16 v[176:179], v[52:55], a[136:139], v[176:179]
ds_read_b64_tr_b16 a[154:155], v245 offset:1120
v_mfma_f32_16x16x32_bf16 v[180:183], v[56:59], a[136:139], v[180:183]
ds_read_b64_tr_b16 a[132:133], v238 offset:42832
v_mfma_f32_16x16x32_bf16 v[180:183], v[68:71], a[140:143], v[180:183]
v_mfma_f32_16x16x32_bf16 v[184:187], v[72:75], a[140:143], v[184:187]
s_waitcnt vmcnt(11) lgkmcnt(5)
ds_read_b64_tr_b16 a[134:135], v238 offset:42960
v_mfma_f32_16x16x32_bf16 v[184:187], a[236:239], a[136:139], v[184:187]
s_barrier
v_mfma_f32_16x16x32_bf16 v[156:159], a[240:243], a[144:147], v[156:159]
ds_read_b64_tr_b16 a[136:137], v239 offset:0
v_mfma_f32_16x16x32_bf16 v[156:159], v[88:91], a[148:151], v[156:159]
v_mfma_f32_16x16x32_bf16 v[160:163], a[244:247], a[148:151], v[160:163]
ds_read_b64_tr_b16 a[138:139], v239 offset:128
v_mfma_f32_16x16x32_bf16 v[160:163], v[76:79], a[144:147], v[160:163]
s_waitcnt lgkmcnt(4)
buffer_store_dwordx4 a[152:155], v243, s[48:51], s73 offen offset:256 sc1
v_mfma_f32_16x16x32_bf16 v[164:167], v[80:83], a[144:147], v[164:167]
ds_read_b64_tr_b16 a[140:141], v239 offset:512
v_mfma_f32_16x16x32_bf16 v[164:167], v[92:95], a[148:151], v[164:167]
v_mfma_f32_16x16x32_bf16 v[168:171], v[96:99], a[148:151], v[168:171]
ds_read_b64_tr_b16 a[142:143], v239 offset:640
v_mfma_f32_16x16x32_bf16 v[168:171], v[84:87], a[144:147], v[168:171]
s_waitcnt lgkmcnt(4)
v_mfma_f32_16x16x32_bf16 v[204:207], v[24:27], a[128:131], v[204:207]
ds_read_b64_tr_b16 a[144:145], v239 offset:8448
v_mfma_f32_16x16x32_bf16 v[204:207], v[36:39], a[132:135], v[204:207]
v_mfma_f32_16x16x32_bf16 v[208:211], v[40:43], a[132:135], v[208:211]
ds_read_b64_tr_b16 a[146:147], v239 offset:8576
v_mfma_f32_16x16x32_bf16 v[208:211], a[228:231], a[128:131], v[208:211]
.Lpad9_%=:
s_cbranch_vccnz .Lret2_%=
v_mfma_f32_16x16x32_bf16 v[212:215], v[28:31], a[128:131], v[212:215]
ds_read_b64_tr_b16 a[148:149], v239 offset:8960
v_mfma_f32_16x16x32_bf16 v[212:215], a[232:235], a[132:135], v[212:215]
v_mfma_f32_16x16x32_bf16 v[216:219], v[44:47], a[132:135], v[216:219]
s_add_u32 m0, s60, 90672
ds_read_b64_tr_b16 a[150:151], v239 offset:9088
v_mfma_f32_16x16x32_bf16 v[216:219], v[32:35], a[128:131], v[216:219]
s_waitcnt lgkmcnt(4)
buffer_load_dwordx4 v241, s[36:39], s75 offen offset:3072 nt lds
v_mfma_f32_16x16x32_bf16 v[156:159], v[100:103], a[136:139], v[156:159]
ds_read_b64_tr_b16 a[128:129], v238 offset:59152
v_mfma_f32_16x16x32_bf16 v[156:159], v[112:115], a[140:143], v[156:159]
v_mfma_f32_16x16x32_bf16 v[160:163], v[116:119], a[140:143], v[160:163]
ds_read_b64_tr_b16 a[130:131], v238 offset:59280
v_mfma_f32_16x16x32_bf16 v[160:163], v[104:107], a[136:139], v[160:163]
v_mfma_f32_16x16x32_bf16 v[164:167], a[248:251], a[136:139], v[164:167]
ds_read_b64_tr_b16 a[132:133], v238 offset:59664
v_mfma_f32_16x16x32_bf16 v[164:167], v[120:123], a[140:143], v[164:167]
ds_read_b64_tr_b16 a[152:153], v245 offset:96
v_mfma_f32_16x16x32_bf16 v[168:171], a[252:255], a[140:143], v[168:171]
ds_read_b64_tr_b16 a[134:135], v238 offset:59792
v_mfma_f32_16x16x32_bf16 v[168:171], v[108:111], a[136:139], v[168:171]
s_waitcnt lgkmcnt(5)
ds_read_b64_tr_b16 a[154:155], v245 offset:1152
v_mfma_f32_16x16x32_bf16 v[156:159], v[124:127], a[144:147], v[156:159]
ds_read_b64_tr_b16 a[136:137], v239 offset:16
v_mfma_f32_16x16x32_bf16 v[156:159], v[140:143], a[148:151], v[156:159]
v_mfma_f32_16x16x32_bf16 v[160:163], v[144:147], a[148:151], v[160:163]
ds_read_b64_tr_b16 a[138:139], v239 offset:144
v_mfma_f32_16x16x32_bf16 v[160:163], v[128:131], a[144:147], v[160:163]
v_mfma_f32_16x16x32_bf16 v[164:167], v[132:135], a[144:147], v[164:167]
ds_read_b64_tr_b16 a[140:141], v239 offset:528
v_mfma_f32_16x16x32_bf16 v[164:167], v[148:151], a[148:151], v[164:167]
v_mfma_f32_16x16x32_bf16 v[168:171], v[152:155], a[148:151], v[168:171]
ds_read_b64_tr_b16 a[142:143], v239 offset:656
v_mul_f32_dpp v156, v250, v156 row_newbcast:0 row_mask:0xf bank_mask:0xf
v_mfma_f32_16x16x32_bf16 v[168:171], v[136:139], a[144:147], v[168:171]
s_waitcnt lgkmcnt(4)
buffer_store_dwordx4 a[152:155], v243, s[48:51], s74 offen offset:256 sc1
v_mul_f32_dpp v157, v250, v157 row_newbcast:1 row_mask:0xf bank_mask:0xf
v_mul_f32_dpp v158, v250, v158 row_newbcast:2 row_mask:0xf bank_mask:0xf
v_mul_f32_dpp v159, v250, v159 row_newbcast:3 row_mask:0xf bank_mask:0xf
v_mfma_f32_16x16x32_bf16 v[172:175], a[240:243], a[128:131], v[172:175]
ds_read_b64_tr_b16 a[144:145], v239 offset:8464
v_mul_f32_dpp v160, v251, v160 row_newbcast:0 row_mask:0xf bank_mask:0xf
v_mfma_f32_16x16x32_bf16 v[172:175], v[88:91], a[132:135], v[172:175]
v_mul_f32_dpp v161, v251, v161 row_newbcast:1 row_mask:0xf bank_mask:0xf
v_mul_f32_dpp v162, v251, v162 row_newbcast:2 row_mask:0xf bank_mask:0xf
v_mfma_f32_16x16x32_bf16 v[176:179], a[244:247], a[132:135], v[176:179]
ds_read_b64_tr_b16 a[146:147], v239 offset:8592
v_mul_f32_dpp v163, v251, v163 row_newbcast:3 row_mask:0xf bank_mask:0xf
v_mfma_f32_16x16x32_bf16 v[176:179], v[76:79], a[128:131], v[176:179]
v_mul_f32_dpp v164, v252, v164 row_newbcast:0 row_mask:0xf bank_mask:0xf
v_mul_f32_dpp v165, v252, v165 row_newbcast:1 row_mask:0xf bank_mask:0xf
v_mfma_f32_16x16x32_bf16 v[180:183], v[80:83], a[128:131], v[180:183]
ds_read_b64_tr_b16 a[148:149], v239 offset:8976
v_mul_f32_dpp v166, v252, v166 row_newbcast:2 row_mask:0xf bank_mask:0xf
v_mfma_f32_16x16x32_bf16 v[180:183], v[92:95], a[132:135], v[180:183]
s_add_u32 m0, s60, 107520
v_mul_f32_dpp v167, v252, v167 row_newbcast:3 row_mask:0xf bank_mask:0xf
v_mul_f32_dpp v168, v253, v168 row_newbcast:0 row_mask:0xf bank_mask:0xf
v_mfma_f32_16x16x32_bf16 v[184:187], v[96:99], a[132:135], v[184:187]
s_waitcnt vmcnt(8) lgkmcnt(3)
ds_read_b64_tr_b16 a[150:151], v239 offset:9104
v_mul_f32_dpp v169, v253, v169 row_newbcast:1 row_mask:0xf bank_mask:0xf
v_mfma_f32_16x16x32_bf16 v[184:187], v[84:87], a[128:131], v[184:187]
s_barrier
v_mfma_f32_16x16x32_bf16 v[172:175], v[100:103], a[136:139], v[172:175]
buffer_load_dwordx4 v241, s[36:39], s79 offen offset:0 nt lds
v_mfma_f32_16x16x32_bf16 v[172:175], v[112:115], a[140:143], v[172:175]
ds_read_b128 a[128:131], v236 offset:0
v_mul_f32_dpp v170, v253, v170 row_newbcast:2 row_mask:0xf bank_mask:0xf
v_mfma_f32_16x16x32_bf16 v[176:179], v[116:119], a[140:143], v[176:179]
v_mul_f32_dpp v171, v253, v171 row_newbcast:3 row_mask:0xf bank_mask:0xf
v_mfma_f32_16x16x32_bf16 v[176:179], v[104:107], a[136:139], v[176:179]
v_mfma_f32_16x16x32_bf16 v[180:183], a[248:251], a[136:139], v[180:183]
v_mfma_f32_16x16x32_bf16 v[180:183], v[120:123], a[140:143], v[180:183]
ds_read_b128 a[132:135], v236 offset:16
v_mfma_f32_16x16x32_bf16 v[184:187], a[252:255], a[140:143], v[184:187]
v_mfma_f32_16x16x32_bf16 v[184:187], v[108:111], a[136:139], v[184:187]
s_waitcnt lgkmcnt(2)
v_mfma_f32_16x16x32_bf16 v[172:175], v[124:127], a[144:147], v[172:175]
v_mfma_f32_16x16x32_bf16 v[172:175], v[140:143], a[148:151], v[172:175]
ds_read_b128 a[136:139], v236 offset:32
v_mfma_f32_16x16x32_bf16 v[176:179], v[144:147], a[148:151], v[176:179]
ds_read_b64_tr_b16 a[152:153], v245 offset:128
v_mfma_f32_16x16x32_bf16 v[176:179], v[128:131], a[144:147], v[176:179]
s_add_u32 m0, s60, 107536
ds_read_b64_tr_b16 a[154:155], v245 offset:1184
v_mfma_f32_16x16x32_bf16 v[180:183], v[132:135], a[144:147], v[180:183]
buffer_load_dwordx4 v241, s[36:39], s79 offen offset:1024 nt lds
v_mfma_f32_16x16x32_bf16 v[180:183], v[148:151], a[148:151], v[180:183]
ds_read_b128 a[140:143], v236 offset:48
v_mul_f32_dpp v172, v250, v172 row_newbcast:0 row_mask:0xf bank_mask:0xf
v_mfma_f32_16x16x32_bf16 v[184:187], v[152:155], a[148:151], v[184:187]
v_mul_f32_dpp v173, v250, v173 row_newbcast:1 row_mask:0xf bank_mask:0xf
v_mul_f32_dpp v174, v250, v174 row_newbcast:2 row_mask:0xf bank_mask:0xf
v_mfma_f32_16x16x32_bf16 v[184:187], v[136:139], a[144:147], v[184:187]
s_waitcnt lgkmcnt(4)
v_mul_f32_dpp v175, v250, v175 row_newbcast:3 row_mask:0xf bank_mask:0xf
v_mul_f32_dpp v176, v251, v176 row_newbcast:0 row_mask:0xf bank_mask:0xf
v_mfma_f32_16x16x32_bf16 v[220:223], a[128:131], a[0:3], 0
v_mul_f32_dpp v177, v251, v177 row_newbcast:1 row_mask:0xf bank_mask:0xf
)"
R"(
v_mul_f32_dpp v178, v251, v178 row_newbcast:2 row_mask:0xf bank_mask:0xf
v_mfma_f32_16x16x32_bf16 v[220:223], a[132:135], a[4:7], v[220:223]
ds_read_b128 a[144:147], v236 offset:64
v_mul_f32_dpp v179, v251, v179 row_newbcast:3 row_mask:0xf bank_mask:0xf
v_mfma_f32_16x16x32_bf16 v[224:227], a[132:135], a[36:39], 0
v_mul_f32_dpp v180, v252, v180 row_newbcast:0 row_mask:0xf bank_mask:0xf
v_mul_f32_dpp v181, v252, v181 row_newbcast:1 row_mask:0xf bank_mask:0xf
v_mfma_f32_16x16x32_bf16 v[224:227], a[128:131], a[32:35], v[224:227]
s_waitcnt lgkmcnt(2)
buffer_store_dwordx4 a[152:155], v243, s[48:51], s75 offen offset:256 sc1
v_mul_f32_dpp v182, v252, v182 row_newbcast:2 row_mask:0xf bank_mask:0xf
v_mul_f32_dpp v183, v252, v183 row_newbcast:3 row_mask:0xf bank_mask:0xf
v_mul_f32_dpp v184, v253, v184 row_newbcast:0 row_mask:0xf bank_mask:0xf
v_mfma_f32_16x16x32_bf16 v[228:231], a[128:131], a[64:67], 0
v_mul_f32_dpp v185, v253, v185 row_newbcast:1 row_mask:0xf bank_mask:0xf
v_mul_f32_dpp v186, v253, v186 row_newbcast:2 row_mask:0xf bank_mask:0xf
v_mfma_f32_16x16x32_bf16 v[228:231], a[132:135], a[68:71], v[228:231]
ds_read_b128 a[148:151], v236 offset:80
v_mul_f32_dpp v187, v253, v187 row_newbcast:3 row_mask:0xf bank_mask:0xf
v_mfma_f32_16x16x32_bf16 v[232:235], a[132:135], a[100:103], 0
v_cvt_pk_bf16_f32 v172, v156, v172
v_cvt_pk_bf16_f32 v173, v157, v173
v_mfma_f32_16x16x32_bf16 v[232:235], a[128:131], a[96:99], v[232:235]
s_waitcnt lgkmcnt(2)
ds_write_b64 v244, v[172:173] offset:36864
v_mfma_f32_16x16x32_bf16 v[232:235], a[136:139], a[104:107], v[232:235]
v_cvt_pk_bf16_f32 v174, v158, v174
v_cvt_pk_bf16_f32 v175, v159, v175
v_mfma_f32_16x16x32_bf16 v[232:235], a[140:143], a[108:111], v[232:235]
ds_read_b128 a[128:131], v236 offset:96
v_cvt_pk_bf16_f32 v176, v160, v176
v_mfma_f32_16x16x32_bf16 v[228:231], a[140:143], a[76:79], v[228:231]
ds_write_b64 v244, v[174:175] offset:36872
v_mfma_f32_16x16x32_bf16 v[228:231], a[136:139], a[72:75], v[228:231]
s_add_u32 m0, s60, 107552
v_cvt_pk_bf16_f32 v177, v161, v177
v_cvt_pk_bf16_f32 v178, v162, v178
v_mfma_f32_16x16x32_bf16 v[224:227], a[136:139], a[40:43], v[224:227]
buffer_load_dwordx4 v241, s[36:39], s79 offen offset:2048 nt lds
v_mfma_f32_16x16x32_bf16 v[224:227], a[140:143], a[44:47], v[224:227]
ds_read_b128 a[132:135], v236 offset:112
v_cvt_pk_bf16_f32 v179, v163, v179
v_mfma_f32_16x16x32_bf16 v[220:223], a[140:143], a[12:15], v[220:223]
ds_write_b64 v244, v[176:177] offset:36928
v_mfma_f32_16x16x32_bf16 v[220:223], a[136:139], a[8:11], v[220:223]
s_waitcnt lgkmcnt(5)
ds_write_b64 v244, v[178:179] offset:36936
v_mfma_f32_16x16x32_bf16 v[220:223], a[144:147], a[16:19], v[220:223]
ds_read_b64_tr_b16 a[136:137], v238 offset:50768
v_mfma_f32_16x16x32_bf16 v[220:223], a[148:151], a[20:23], v[220:223]
v_cvt_pk_bf16_f32 v180, v164, v180
v_cvt_pk_bf16_f32 v181, v165, v181
v_mfma_f32_16x16x32_bf16 v[224:227], a[148:151], a[52:55], v[224:227]
ds_read_b64_tr_b16 a[138:139], v238 offset:50896
v_cvt_pk_bf16_f32 v182, v166, v182
v_mfma_f32_16x16x32_bf16 v[224:227], a[144:147], a[48:51], v[224:227]
ds_write_b64 v244, v[180:181] offset:36992
v_mfma_f32_16x16x32_bf16 v[228:231], a[144:147], a[80:83], v[228:231]
ds_read_b64_tr_b16 a[140:141], v238 offset:51280
v_mfma_f32_16x16x32_bf16 v[228:231], a[148:151], a[84:87], v[228:231]
ds_read_b64_tr_b16 a[152:153], v245 offset:160
v_cvt_pk_bf16_f32 v183, v167, v183
v_mfma_f32_16x16x32_bf16 v[232:235], a[148:151], a[116:119], v[232:235]
ds_read_b64_tr_b16 a[142:143], v238 offset:51408
v_cvt_pk_bf16_f32 v184, v168, v184
v_mfma_f32_16x16x32_bf16 v[232:235], a[144:147], a[112:115], v[232:235]
s_waitcnt lgkmcnt(8)
ds_read_b64_tr_b16 a[154:155], v245 offset:1216
v_cvt_pk_bf16_f32 v185, v169, v185
v_mfma_f32_16x16x32_bf16 v[232:235], a[128:131], a[120:123], v[232:235]
s_add_u32 m0, s60, 107568
ds_read_b64_tr_b16 a[144:145], v238 offset:50752
v_cvt_pk_bf16_f32 v186, v170, v186
v_mfma_f32_16x16x32_bf16 v[232:235], a[132:135], a[124:127], v[232:235]
buffer_load_dwordx4 v241, s[36:39], s79 offen offset:3072 nt lds
v_mfma_f32_16x16x32_bf16 v[228:231], a[132:135], a[92:95], v[228:231]
ds_read_b64_tr_b16 a[146:147], v238 offset:50880
v_cvt_pk_bf16_f32 v187, v171, v187
v_mfma_f32_16x16x32_bf16 v[228:231], a[128:131], a[88:91], v[228:231]
ds_write_b64 v244, v[182:183] offset:37000
v_mfma_f32_16x16x32_bf16 v[224:227], a[128:131], a[56:59], v[224:227]
ds_read_b64_tr_b16 a[148:149], v238 offset:51264
v_mfma_f32_16x16x32_bf16 v[224:227], a[132:135], a[60:63], v[224:227]
v_max3_f32 v249, v232, v233, v234
v_max_f32 v249, v249, v235
v_mfma_f32_16x16x32_bf16 v[220:223], a[132:135], a[28:31], v[220:223]
ds_read_b64_tr_b16 a[150:151], v238 offset:51392
v_max3_f32 v248, v228, v229, v230
v_mfma_f32_16x16x32_bf16 v[220:223], a[128:131], a[24:27], v[220:223]
s_waitcnt lgkmcnt(5)
buffer_store_dwordx4 a[152:155], v243, s[48:51], s76 offen offset:256 sc1
v_max_f32 v248, v248, v231
v_mfma_f32_16x16x32_bf16 v[204:207], v[48:51], a[136:139], v[204:207]
ds_read_b64_tr_b16 a[128:129], v238 offset:59200
v_max3_f32 v247, v224, v225, v226
v_mfma_f32_16x16x32_bf16 v[204:207], v[60:63], a[140:143], v[204:207]
v_max_f32 v247, v247, v227
v_permlane32_swap_b32 v248, v249
v_mfma_f32_16x16x32_bf16 v[208:211], v[64:67], a[140:143], v[208:211]
ds_read_b64_tr_b16 a[130:131], v238 offset:59328
v_max3_f32 v246, v220, v221, v222
v_mfma_f32_16x16x32_bf16 v[208:211], v[52:55], a[136:139], v[208:211]
v_max_f32 v246, v246, v223
v_max_f32 v248, v248, v249
v_mfma_f32_16x16x32_bf16 v[212:215], v[56:59], a[136:139], v[212:215]
ds_read_b64_tr_b16 a[132:133], v238 offset:59712
v_permlane32_swap_b32 v246, v247
v_mfma_f32_16x16x32_bf16 v[212:215], v[68:71], a[140:143], v[212:215]
v_max_f32 v246, v246, v247
v_mfma_f32_16x16x32_bf16 v[216:219], v[72:75], a[140:143], v[216:219]
ds_read_b64_tr_b16 a[134:135], v238 offset:59840
v_permlane16_swap_b32 v246, v248
v_mfma_f32_16x16x32_bf16 v[216:219], a[236:239], a[136:139], v[216:219]
s_waitcnt lgkmcnt(4)
v_max_f32 v246, v246, v248
v_mul_f32 v246, s57, v246
v_mfma_f32_16x16x32_bf16 v[188:191], v[48:51], a[144:147], v[188:191]
v_add_f32 v246, 0xc0400000, v246
v_mov_b32 v248, v246
v_mfma_f32_16x16x32_bf16 v[188:191], v[60:63], a[148:151], v[188:191]
ds_read_b128 a[136:139], v236 offset:512
v_permlane16_swap_b32 v246, v248
v_mfma_f32_16x16x32_bf16 v[192:195], v[64:67], a[148:151], v[192:195]
v_mov_b32 v247, v246
v_mov_b32 v249, v248
v_mfma_f32_16x16x32_bf16 v[192:195], v[52:55], a[144:147], v[192:195]
v_permlane32_swap_b32 v246, v247
v_permlane32_swap_b32 v248, v249
v_mfma_f32_16x16x32_bf16 v[196:199], v[56:59], a[144:147], v[196:199]
ds_write_b64 v244, v[184:185] offset:37056
v_mfma_f32_16x16x32_bf16 v[196:199], v[68:71], a[148:151], v[196:199]
ds_read_b128 a[140:143], v236 offset:528
v_mfma_f32_16x16x32_bf16 v[200:203], v[72:75], a[148:151], v[200:203]
ds_write_b64 v244, v[186:187] offset:37064
v_mfma_f32_16x16x32_bf16 v[200:203], a[236:239], a[144:147], v[200:203]
s_waitcnt lgkmcnt(4)
v_fma_f32 v220, v220, s56, v246
v_fma_f32 v221, v221, s56, v246
v_mfma_f32_16x16x32_bf16 v[188:191], a[240:243], a[128:131], v[188:191]
v_exp_f32 v220, v220
v_mfma_f32_16x16x32_bf16 v[188:191], v[88:91], a[132:135], v[188:191]
ds_read_b128 a[144:147], v236 offset:544
v_fma_f32 v222, v222, s56, v246
v_mfma_f32_16x16x32_bf16 v[192:195], a[244:247], a[132:135], v[192:195]
ds_read_b64_tr_b16 a[152:153], v245 offset:192
v_fma_f32 v223, v223, s56, v246
v_mfma_f32_16x16x32_bf16 v[192:195], v[76:79], a[128:131], v[192:195]
ds_read_b64_tr_b16 a[154:155], v245 offset:1248
v_fma_f32 v224, v224, s56, v247
v_mfma_f32_16x16x32_bf16 v[196:199], v[80:83], a[128:131], v[196:199]
v_exp_f32 v221, v221
v_mfma_f32_16x16x32_bf16 v[196:199], v[92:95], a[132:135], v[196:199]
ds_read_b128 a[148:151], v236 offset:560
v_cvt_pk_bf16_f32 v220, v220, v221
v_mfma_f32_16x16x32_bf16 v[200:203], v[96:99], a[132:135], v[200:203]
v_exp_f32 v222, v222
v_mfma_f32_16x16x32_bf16 v[200:203], v[84:87], a[128:131], v[200:203]
s_waitcnt lgkmcnt(5)
v_exp_f32 v223, v223
v_mfma_f32_16x16x32_bf16 v[156:159], a[136:139], a[0:3], 0
v_exp_f32 v224, v224
v_mfma_f32_16x16x32_bf16 v[156:159], a[140:143], a[4:7], v[156:159]
ds_read_b128 a[128:131], v236 offset:576
v_accvgpr_write_b32 a156, v220
v_mfma_f32_16x16x32_bf16 v[160:163], a[140:143], a[36:39], 0
v_cvt_pk_bf16_f32 v222, v222, v223
v_accvgpr_write_b32 a157, v222
v_mfma_f32_16x16x32_bf16 v[160:163], a[136:139], a[32:35], v[160:163]
s_waitcnt lgkmcnt(2)
buffer_store_dwordx4 a[152:155], v243, s[48:51], s77 offen offset:256 sc1
v_fma_f32 v225, v225, s56, v247
v_fma_f32 v226, v226, s56, v247
v_fma_f32 v227, v227, s56, v247
v_mfma_f32_16x16x32_bf16 v[164:167], a[136:139], a[64:67], 0
v_exp_f32 v225, v225
v_mfma_f32_16x16x32_bf16 v[164:167], a[140:143], a[68:71], v[164:167]
ds_read_b128 a[132:135], v236 offset:592
v_cvt_pk_bf16_f32 v224, v224, v225
v_mfma_f32_16x16x32_bf16 v[168:171], a[140:143], a[100:103], 0
v_exp_f32 v226, v226
v_mfma_f32_16x16x32_bf16 v[168:171], a[136:139], a[96:99], v[168:171]
s_waitcnt lgkmcnt(2)
v_exp_f32 v227, v227
v_mfma_f32_16x16x32_bf16 v[168:171], a[144:147], a[104:107], v[168:171]
v_accvgpr_write_b32 a160, v224
v_cvt_pk_bf16_f32 v226, v226, v227
v_mfma_f32_16x16x32_bf16 v[168:171], a[148:151], a[108:111], v[168:171]
ds_read_b128 a[136:139], v236 offset:608
v_accvgpr_write_b32 a161, v226
v_mfma_f32_16x16x32_bf16 v[164:167], a[148:151], a[76:79], v[164:167]
v_fma_f32 v228, v228, s56, v248
v_fma_f32 v229, v229, s56, v248
v_mfma_f32_16x16x32_bf16 v[164:167], a[144:147], a[72:75], v[164:167]
v_exp_f32 v228, v228
v_mfma_f32_16x16x32_bf16 v[160:163], a[144:147], a[40:43], v[160:163]
v_exp_f32 v229, v229
v_mfma_f32_16x16x32_bf16 v[160:163], a[148:151], a[44:47], v[160:163]
ds_read_b128 a[140:143], v236 offset:624
v_fma_f32 v230, v230, s56, v248
v_mfma_f32_16x16x32_bf16 v[156:159], a[148:151], a[12:15], v[156:159]
v_exp_f32 v230, v230
v_mfma_f32_16x16x32_bf16 v[156:159], a[144:147], a[8:11], v[156:159]
s_waitcnt lgkmcnt(2)
v_fma_f32 v231, v231, s56, v248
v_cvt_pk_bf16_f32 v228, v228, v229
v_mfma_f32_16x16x32_bf16 v[156:159], a[128:131], a[16:19], v[156:159]
ds_read_b64_tr_b16 a[144:145], v239 offset:64
v_accvgpr_write_b32 a164, v228
v_mfma_f32_16x16x32_bf16 v[156:159], a[132:135], a[20:23], v[156:159]
v_exp_f32 v231, v231
v_mfma_f32_16x16x32_bf16 v[160:163], a[132:135], a[52:55], v[160:163]
ds_read_b64_tr_b16 a[146:147], v239 offset:192
v_cvt_pk_bf16_f32 v230, v230, v231
v_mfma_f32_16x16x32_bf16 v[160:163], a[128:131], a[48:51], v[160:163]
v_accvgpr_write_b32 a165, v230
v_fma_f32 v232, v232, s56, v249
v_mfma_f32_16x16x32_bf16 v[164:167], a[128:131], a[80:83], v[164:167]
ds_read_b64_tr_b16 a[148:149], v239 offset:576
v_fma_f32 v233, v233, s56, v249
v_mfma_f32_16x16x32_bf16 v[164:167], a[132:135], a[84:87], v[164:167]
ds_read_b64_tr_b16 a[152:153], v245 offset:224
v_fma_f32 v234, v234, s56, v249
v_mfma_f32_16x16x32_bf16 v[168:171], a[132:135], a[116:119], v[168:171]
ds_read_b64_tr_b16 a[150:151], v239 offset:704
v_fma_f32 v235, v235, s56, v249
v_mfma_f32_16x16x32_bf16 v[168:171], a[128:131], a[112:115], v[168:171]
s_waitcnt lgkmcnt(5)
ds_read_b64_tr_b16 a[154:155], v245 offset:1280
v_mfma_f32_16x16x32_bf16 v[168:171], a[136:139], a[120:123], v[168:171]
ds_read_b64_tr_b16 a[128:129], v239 offset:8512
v_mfma_f32_16x16x32_bf16 v[168:171], a[140:143], a[124:127], v[168:171]
v_exp_f32 v232, v232
v_mfma_f32_16x16x32_bf16 v[164:167], a[140:143], a[92:95], v[164:167]
ds_read_b64_tr_b16 a[130:131], v239 offset:8640
v_mfma_f32_16x16x32_bf16 v[164:167], a[136:139], a[88:91], v[164:167]
v_exp_f32 v233, v233
v_mfma_f32_16x16x32_bf16 v[160:163], a[136:139], a[56:59], v[160:163]
ds_read_b64_tr_b16 a[132:133], v239 offset:9024
v_cvt_pk_bf16_f32 v232, v232, v233
v_mfma_f32_16x16x32_bf16 v[160:163], a[140:143], a[60:63], v[160:163]
v_fma_f32 v168, v168, s56, v249
v_fma_f32 v169, v169, s56, v249
v_mfma_f32_16x16x32_bf16 v[156:159], a[140:143], a[28:31], v[156:159]
ds_read_b64_tr_b16 a[134:135], v239 offset:9152
v_fma_f32 v164, v164, s56, v248
v_mfma_f32_16x16x32_bf16 v[156:159], a[136:139], a[24:27], v[156:159]
)"
R"(
s_waitcnt lgkmcnt(4)
buffer_store_dwordx4 a[152:155], v243, s[48:51], s78 offen offset:256 sc1
v_exp_f32 v164, v164
v_fma_f32 v165, v165, s56, v248
v_mfma_f32_16x16x32_bf16 v[188:191], v[100:103], a[144:147], v[188:191]
ds_read_b64_tr_b16 a[136:137], v238 offset:59216
v_fma_f32 v160, v160, s56, v247
v_mfma_f32_16x16x32_bf16 v[188:191], v[112:115], a[148:151], v[188:191]
v_exp_f32 v160, v160
v_mfma_f32_16x16x32_bf16 v[192:195], v[116:119], a[148:151], v[192:195]
ds_read_b64_tr_b16 a[138:139], v238 offset:59344
v_fma_f32 v156, v156, s56, v246
v_mfma_f32_16x16x32_bf16 v[192:195], v[104:107], a[144:147], v[192:195]
v_exp_f32 v156, v156
v_mfma_f32_16x16x32_bf16 v[196:199], a[248:251], a[144:147], v[196:199]
ds_read_b64_tr_b16 a[140:141], v238 offset:59728
v_fma_f32 v157, v157, s56, v246
v_mfma_f32_16x16x32_bf16 v[196:199], v[120:123], a[148:151], v[196:199]
v_exp_f32 v157, v157
v_mfma_f32_16x16x32_bf16 v[200:203], a[252:255], a[148:151], v[200:203]
ds_read_b64_tr_b16 a[142:143], v238 offset:59856
v_fma_f32 v158, v158, s56, v246
v_mfma_f32_16x16x32_bf16 v[200:203], v[108:111], a[144:147], v[200:203]
s_waitcnt lgkmcnt(4)
v_exp_f32 v158, v158
v_mfma_f32_16x16x32_bf16 v[188:191], v[124:127], a[128:131], v[188:191]
ds_read_b64_tr_b16 a[144:145], v239 offset:80
v_fma_f32 v159, v159, s56, v246
v_mfma_f32_16x16x32_bf16 v[188:191], v[140:143], a[132:135], v[188:191]
v_exp_f32 v159, v159
v_mfma_f32_16x16x32_bf16 v[192:195], v[144:147], a[132:135], v[192:195]
ds_read_b64_tr_b16 a[146:147], v239 offset:208
v_cvt_pk_bf16_f32 v156, v156, v157
v_mfma_f32_16x16x32_bf16 v[192:195], v[128:131], a[128:131], v[192:195]
v_exp_f32 v165, v165
v_mfma_f32_16x16x32_bf16 v[196:199], v[132:135], a[128:131], v[196:199]
ds_read_b64_tr_b16 a[148:149], v239 offset:592
v_mul_f32_dpp v188, v250, v188 row_newbcast:0 row_mask:0xf bank_mask:0xf
v_mfma_f32_16x16x32_bf16 v[196:199], v[148:151], a[132:135], v[196:199]
v_mul_f32_dpp v189, v250, v189 row_newbcast:1 row_mask:0xf bank_mask:0xf
v_mul_f32_dpp v190, v250, v190 row_newbcast:2 row_mask:0xf bank_mask:0xf
v_mfma_f32_16x16x32_bf16 v[200:203], v[152:155], a[132:135], v[200:203]
ds_read_b64_tr_b16 a[150:151], v239 offset:720
v_mul_f32_dpp v191, v250, v191 row_newbcast:3 row_mask:0xf bank_mask:0xf
v_mfma_f32_16x16x32_bf16 v[200:203], v[136:139], a[128:131], v[200:203]
s_waitcnt lgkmcnt(4)
v_mul_f32_dpp v192, v251, v192 row_newbcast:0 row_mask:0xf bank_mask:0xf
v_mul_f32_dpp v193, v251, v193 row_newbcast:1 row_mask:0xf bank_mask:0xf
v_mfma_f32_16x16x32_bf16 v[204:207], a[240:243], a[136:139], v[204:207]
ds_read_b64_tr_b16 a[128:129], v239 offset:8528
v_mul_f32_dpp v194, v251, v194 row_newbcast:2 row_mask:0xf bank_mask:0xf
v_mfma_f32_16x16x32_bf16 v[204:207], v[88:91], a[140:143], v[204:207]
v_mul_f32_dpp v195, v251, v195 row_newbcast:3 row_mask:0xf bank_mask:0xf
v_mul_f32_dpp v196, v252, v196 row_newbcast:0 row_mask:0xf bank_mask:0xf
v_mfma_f32_16x16x32_bf16 v[208:211], a[244:247], a[140:143], v[208:211]
ds_read_b64_tr_b16 a[130:131], v239 offset:8656
v_mul_f32_dpp v197, v252, v197 row_newbcast:1 row_mask:0xf bank_mask:0xf
v_mfma_f32_16x16x32_bf16 v[208:211], v[76:79], a[136:139], v[208:211]
v_mul_f32_dpp v198, v252, v198 row_newbcast:2 row_mask:0xf bank_mask:0xf
v_mul_f32_dpp v199, v252, v199 row_newbcast:3 row_mask:0xf bank_mask:0xf
v_mfma_f32_16x16x32_bf16 v[212:215], v[80:83], a[136:139], v[212:215]
ds_read_b64_tr_b16 a[132:133], v239 offset:9040
v_mul_f32_dpp v200, v253, v200 row_newbcast:0 row_mask:0xf bank_mask:0xf
v_mfma_f32_16x16x32_bf16 v[212:215], v[92:95], a[140:143], v[212:215]
s_add_u32 m0, s60, 124416
v_mul_f32_dpp v201, v253, v201 row_newbcast:1 row_mask:0xf bank_mask:0xf
v_mul_f32_dpp v202, v253, v202 row_newbcast:2 row_mask:0xf bank_mask:0xf
v_mfma_f32_16x16x32_bf16 v[216:219], v[96:99], a[140:143], v[216:219]
s_waitcnt vmcnt(9) lgkmcnt(3)
ds_read_b64_tr_b16 a[134:135], v239 offset:9168
v_mul_f32_dpp v203, v253, v203 row_newbcast:3 row_mask:0xf bank_mask:0xf
v_mfma_f32_16x16x32_bf16 v[216:219], v[84:87], a[136:139], v[216:219]
s_barrier
v_mfma_f32_16x16x32_bf16 v[204:207], v[100:103], a[144:147], v[204:207]
buffer_load_dwordx4 v241, s[36:39], s80 offen offset:0 nt lds
v_mfma_f32_16x16x32_bf16 v[204:207], v[112:115], a[148:151], v[204:207]
ds_read_b128 a[136:139], v236 offset:16896
v_accvgpr_write_b32 a158, v156
v_mfma_f32_16x16x32_bf16 v[208:211], v[116:119], a[148:151], v[208:211]
v_exp_f32 v168, v168
v_mfma_f32_16x16x32_bf16 v[208:211], v[104:107], a[144:147], v[208:211]
v_exp_f32 v169, v169
v_mfma_f32_16x16x32_bf16 v[212:215], a[248:251], a[144:147], v[212:215]
v_cvt_pk_bf16_f32 v158, v158, v159
v_accvgpr_write_b32 a159, v158
v_mfma_f32_16x16x32_bf16 v[212:215], v[120:123], a[148:151], v[212:215]
ds_read_b128 a[140:143], v236 offset:16912
v_fma_f32 v161, v161, s56, v247
v_mfma_f32_16x16x32_bf16 v[216:219], a[252:255], a[148:151], v[216:219]
s_add_u32 m0, s60, 124432
v_exp_f32 v161, v161
v_mfma_f32_16x16x32_bf16 v[216:219], v[108:111], a[144:147], v[216:219]
s_waitcnt lgkmcnt(2)
v_fma_f32 v162, v162, s56, v247
v_fma_f32 v163, v163, s56, v247
v_mfma_f32_16x16x32_bf16 v[204:207], v[124:127], a[128:131], v[204:207]
buffer_load_dwordx4 v241, s[36:39], s80 offen offset:1024 nt lds
v_mfma_f32_16x16x32_bf16 v[204:207], v[140:143], a[132:135], v[204:207]
ds_read_b128 a[144:147], v236 offset:16928
v_cvt_pk_bf16_f32 v160, v160, v161
v_mfma_f32_16x16x32_bf16 v[208:211], v[144:147], a[132:135], v[208:211]
v_exp_f32 v162, v162
v_mfma_f32_16x16x32_bf16 v[208:211], v[128:131], a[128:131], v[208:211]
v_exp_f32 v163, v163
v_mfma_f32_16x16x32_bf16 v[212:215], v[132:135], a[128:131], v[212:215]
v_accvgpr_write_b32 a162, v160
v_mul_f32_dpp v204, v250, v204 row_newbcast:0 row_mask:0xf bank_mask:0xf
v_mfma_f32_16x16x32_bf16 v[212:215], v[148:151], a[132:135], v[212:215]
ds_read_b128 a[148:151], v236 offset:16944
v_mul_f32_dpp v205, v250, v205 row_newbcast:1 row_mask:0xf bank_mask:0xf
v_mfma_f32_16x16x32_bf16 v[216:219], v[152:155], a[132:135], v[216:219]
v_mul_f32_dpp v206, v250, v206 row_newbcast:2 row_mask:0xf bank_mask:0xf
v_mul_f32_dpp v207, v250, v207 row_newbcast:3 row_mask:0xf bank_mask:0xf
v_mfma_f32_16x16x32_bf16 v[216:219], v[136:139], a[128:131], v[216:219]
s_waitcnt lgkmcnt(2)
v_cvt_pk_bf16_f32 v204, v188, v204
v_cvt_pk_bf16_f32 v205, v189, v205
v_mfma_f32_16x16x32_bf16 v[172:175], a[136:139], a[0:3], 0
v_cvt_pk_bf16_f32 v206, v190, v206
v_cvt_pk_bf16_f32 v207, v191, v207
v_mfma_f32_16x16x32_bf16 v[172:175], a[140:143], a[4:7], v[172:175]
s_add_u32 m0, s60, 124448
ds_read_b128 a[128:131], v236 offset:16960
v_mul_f32_dpp v208, v251, v208 row_newbcast:0 row_mask:0xf bank_mask:0xf
v_mfma_f32_16x16x32_bf16 v[176:179], a[140:143], a[36:39], 0
buffer_load_dwordx4 v241, s[36:39], s80 offen offset:2048 nt lds
v_mfma_f32_16x16x32_bf16 v[176:179], a[136:139], a[32:35], v[176:179]
v_mul_f32_dpp v209, v251, v209 row_newbcast:1 row_mask:0xf bank_mask:0xf
v_mul_f32_dpp v210, v251, v210 row_newbcast:2 row_mask:0xf bank_mask:0xf
v_mfma_f32_16x16x32_bf16 v[180:183], a[136:139], a[64:67], 0
v_mul_f32_dpp v211, v251, v211 row_newbcast:3 row_mask:0xf bank_mask:0xf
v_cvt_pk_bf16_f32 v208, v192, v208
v_mfma_f32_16x16x32_bf16 v[180:183], a[140:143], a[68:71], v[180:183]
ds_read_b128 a[132:135], v236 offset:16976
v_cvt_pk_bf16_f32 v209, v193, v209
v_mfma_f32_16x16x32_bf16 v[184:187], a[140:143], a[100:103], 0
v_cvt_pk_bf16_f32 v210, v194, v210
v_cvt_pk_bf16_f32 v211, v195, v211
v_mfma_f32_16x16x32_bf16 v[184:187], a[136:139], a[96:99], v[184:187]
s_waitcnt lgkmcnt(2)
v_mul_f32_dpp v212, v252, v212 row_newbcast:0 row_mask:0xf bank_mask:0xf
v_mul_f32_dpp v213, v252, v213 row_newbcast:1 row_mask:0xf bank_mask:0xf
v_mfma_f32_16x16x32_bf16 v[184:187], a[144:147], a[104:107], v[184:187]
v_mul_f32_dpp v214, v252, v214 row_newbcast:2 row_mask:0xf bank_mask:0xf
v_mul_f32_dpp v215, v252, v215 row_newbcast:3 row_mask:0xf bank_mask:0xf
v_mfma_f32_16x16x32_bf16 v[184:187], a[148:151], a[108:111], v[184:187]
s_add_u32 m0, s60, 124464
ds_read_b128 a[136:139], v236 offset:16992
v_cvt_pk_bf16_f32 v212, v196, v212
v_mfma_f32_16x16x32_bf16 v[180:183], a[148:151], a[76:79], v[180:183]
buffer_load_dwordx4 v241, s[36:39], s80 offen offset:3072 nt lds
v_mfma_f32_16x16x32_bf16 v[180:183], a[144:147], a[72:75], v[180:183]
v_cvt_pk_bf16_f32 v213, v197, v213
v_cvt_pk_bf16_f32 v214, v198, v214
v_mfma_f32_16x16x32_bf16 v[176:179], a[144:147], a[40:43], v[176:179]
v_cvt_pk_bf16_f32 v215, v199, v215
v_mul_f32_dpp v216, v253, v216 row_newbcast:0 row_mask:0xf bank_mask:0xf
v_mfma_f32_16x16x32_bf16 v[176:179], a[148:151], a[44:47], v[176:179]
ds_read_b128 a[140:143], v236 offset:17008
v_mul_f32_dpp v217, v253, v217 row_newbcast:1 row_mask:0xf bank_mask:0xf
v_mfma_f32_16x16x32_bf16 v[172:175], a[148:151], a[12:15], v[172:175]
v_mul_f32_dpp v218, v253, v218 row_newbcast:2 row_mask:0xf bank_mask:0xf
v_mul_f32_dpp v219, v253, v219 row_newbcast:3 row_mask:0xf bank_mask:0xf
v_mfma_f32_16x16x32_bf16 v[172:175], a[144:147], a[8:11], v[172:175]
s_waitcnt lgkmcnt(2)
v_cvt_pk_bf16_f32 v216, v200, v216
v_cvt_pk_bf16_f32 v217, v201, v217
v_mfma_f32_16x16x32_bf16 v[172:175], a[128:131], a[16:19], v[172:175]
v_cvt_pk_bf16_f32 v218, v202, v218
v_cvt_pk_bf16_f32 v219, v203, v219
v_mfma_f32_16x16x32_bf16 v[172:175], a[132:135], a[20:23], v[172:175]
ds_read_b128 a[144:147], v236 offset:17408
v_cvt_pk_bf16_f32 v162, v162, v163
v_mfma_f32_16x16x32_bf16 v[176:179], a[132:135], a[52:55], v[176:179]
ds_write_b64 v244, v[204:205] offset:41088
v_mfma_f32_16x16x32_bf16 v[176:179], a[128:131], a[48:51], v[176:179]
ds_write_b64 v244, v[206:207] offset:41096
v_mfma_f32_16x16x32_bf16 v[180:183], a[128:131], a[80:83], v[180:183]
ds_write_b64 v244, v[208:209] offset:41152
v_mfma_f32_16x16x32_bf16 v[180:183], a[132:135], a[84:87], v[180:183]
ds_read_b128 a[148:151], v236 offset:17424
v_mfma_f32_16x16x32_bf16 v[184:187], a[132:135], a[116:119], v[184:187]
s_add_u32 m0, s60, 73728
ds_write_b64 v244, v[210:211] offset:41160
v_mfma_f32_16x16x32_bf16 v[184:187], a[128:131], a[112:115], v[184:187]
s_waitcnt lgkmcnt(6)
buffer_load_dwordx4 v241, s[36:39], s81 offen offset:0 nt lds
v_mfma_f32_16x16x32_bf16 v[184:187], a[136:139], a[120:123], v[184:187]
ds_write_b64 v244, v[212:213] offset:41216
v_mfma_f32_16x16x32_bf16 v[184:187], a[140:143], a[124:127], v[184:187]
ds_read_b128 a[128:131], v236 offset:17440
v_mfma_f32_16x16x32_bf16 v[180:183], a[140:143], a[92:95], v[180:183]
ds_write_b64 v244, v[214:215] offset:41224
v_mfma_f32_16x16x32_bf16 v[180:183], a[136:139], a[88:91], v[180:183]
ds_write_b64 v244, v[216:217] offset:41280
v_mfma_f32_16x16x32_bf16 v[176:179], a[136:139], a[56:59], v[176:179]
ds_write_b64 v244, v[218:219] offset:41288
v_mfma_f32_16x16x32_bf16 v[176:179], a[140:143], a[60:63], v[176:179]
ds_read_b128 a[132:135], v236 offset:17456
v_mfma_f32_16x16x32_bf16 v[172:175], a[140:143], a[28:31], v[172:175]
ds_read_b64_tr_b16 a[152:153], v245 offset:36864
v_accvgpr_write_b32 a163, v162
v_mfma_f32_16x16x32_bf16 v[172:175], a[136:139], a[24:27], v[172:175]
.Lpad10_%=:
s_cbranch_vccnz .Lret2_%=
s_waitcnt lgkmcnt(8)
ds_read_b64_tr_b16 a[154:155], v245 offset:37920
v_fma_f32 v166, v166, s56, v248
v_mfma_f32_16x16x32_bf16 v[188:191], a[144:147], a[0:3], 0
v_exp_f32 v166, v166
v_mfma_f32_16x16x32_bf16 v[188:191], a[148:151], a[4:7], v[188:191]
ds_read_b128 a[136:139], v236 offset:17472
v_fma_f32 v167, v167, s56, v248
v_mfma_f32_16x16x32_bf16 v[192:195], a[148:151], a[36:39], 0
v_exp_f32 v167, v167
v_mfma_f32_16x16x32_bf16 v[192:195], a[144:147], a[32:35], v[192:195]
v_cvt_pk_bf16_f32 v164, v164, v165
v_accvgpr_write_b32 a166, v164
v_mfma_f32_16x16x32_bf16 v[196:199], a[144:147], a[64:67], 0
v_cvt_pk_bf16_f32 v166, v166, v167
v_accvgpr_write_b32 a167, v166
v_mfma_f32_16x16x32_bf16 v[196:199], a[148:151], a[68:71], v[196:199]
ds_read_b128 a[140:143], v236 offset:17488
v_fma_f32 v170, v170, s56, v249
v_mfma_f32_16x16x32_bf16 v[200:203], a[148:151], a[100:103], 0
v_exp_f32 v170, v170
v_mfma_f32_16x16x32_bf16 v[200:203], a[144:147], a[96:99], v[200:203]
)"
R"(
s_waitcnt lgkmcnt(2)
buffer_store_dwordx4 a[152:155], v243, s[48:51], 0 offen offset:384 sc1
v_exp_f32 v234, v234
v_fma_f32 v171, v171, s56, v249
v_mfma_f32_16x16x32_bf16 v[200:203], a[128:131], a[104:107], v[200:203]
ds_read_b64_tr_b16 a[152:153], v245 offset:36896
v_cvt_pk_bf16_f32 v168, v168, v169
v_mfma_f32_16x16x32_bf16 v[200:203], a[132:135], a[108:111], v[200:203]
ds_read_b128 a[144:147], v236 offset:17504
v_accvgpr_write_b32 a170, v168
v_mfma_f32_16x16x32_bf16 v[196:199], a[132:135], a[76:79], v[196:199]
ds_read_b64_tr_b16 a[154:155], v245 offset:37952
v_fma_f32 v172, v172, s56, v246
v_mfma_f32_16x16x32_bf16 v[196:199], a[128:131], a[72:75], v[196:199]
v_exp_f32 v171, v171
v_mfma_f32_16x16x32_bf16 v[192:195], a[128:131], a[40:43], v[192:195]
v_exp_f32 v172, v172
v_mfma_f32_16x16x32_bf16 v[192:195], a[132:135], a[44:47], v[192:195]
s_add_u32 m0, s60, 73744
ds_read_b128 a[148:151], v236 offset:17520
v_cvt_pk_bf16_f32 v170, v170, v171
v_mfma_f32_16x16x32_bf16 v[188:191], a[132:135], a[12:15], v[188:191]
s_waitcnt vmcnt(9) lgkmcnt(4)
v_accvgpr_write_b32 a171, v170
v_fma_f32 v173, v173, s56, v246
v_mfma_f32_16x16x32_bf16 v[188:191], a[128:131], a[8:11], v[188:191]
s_barrier
v_mfma_f32_16x16x32_bf16 v[188:191], a[136:139], a[16:19], v[188:191]
buffer_load_dwordx4 v241, s[36:39], s81 offen offset:1024 nt lds
v_mfma_f32_16x16x32_bf16 v[188:191], a[140:143], a[20:23], v[188:191]
ds_read_b128 a[128:131], v236 offset:33792
v_fma_f32 v174, v174, s56, v246
v_mfma_f32_16x16x32_bf16 v[192:195], a[140:143], a[52:55], v[192:195]
s_waitcnt lgkmcnt(2)
buffer_store_dwordx4 a[152:155], v243, s[48:51], s72 offen offset:384 sc1
v_exp_f32 v173, v173
v_fma_f32 v175, v175, s56, v246
v_mfma_f32_16x16x32_bf16 v[192:195], a[136:139], a[48:51], v[192:195]
v_exp_f32 v174, v174
v_mfma_f32_16x16x32_bf16 v[196:199], a[136:139], a[80:83], v[196:199]
v_exp_f32 v175, v175
v_mfma_f32_16x16x32_bf16 v[196:199], a[140:143], a[84:87], v[196:199]
ds_read_b128 a[132:135], v236 offset:33808
v_cvt_pk_bf16_f32 v172, v172, v173
v_mfma_f32_16x16x32_bf16 v[200:203], a[140:143], a[116:119], v[200:203]
s_add_u32 m0, s60, 73760
ds_read_b64_tr_b16 a[152:153], v245 offset:36928
v_accvgpr_write_b32 a172, v172
v_cvt_pk_bf16_f32 v174, v174, v175
v_accvgpr_write_b32 a173, v174
v_mfma_f32_16x16x32_bf16 v[200:203], a[136:139], a[112:115], v[200:203]
s_waitcnt lgkmcnt(3)
ds_read_b64_tr_b16 a[154:155], v245 offset:37984
v_fma_f32 v176, v176, s56, v247
v_fma_f32 v177, v177, s56, v247
v_fma_f32 v178, v178, s56, v247
v_mfma_f32_16x16x32_bf16 v[200:203], a[144:147], a[120:123], v[200:203]
buffer_load_dwordx4 v241, s[36:39], s81 offen offset:2048 nt lds
v_fma_f32 v179, v179, s56, v247
v_fma_f32 v180, v180, s56, v248
v_mfma_f32_16x16x32_bf16 v[200:203], a[148:151], a[124:127], v[200:203]
ds_read_b128 a[136:139], v236 offset:33824
v_fma_f32 v181, v181, s56, v248
v_fma_f32 v182, v182, s56, v248
v_fma_f32 v183, v183, s56, v248
v_mfma_f32_16x16x32_bf16 v[196:199], a[148:151], a[92:95], v[196:199]
v_exp_f32 v176, v176
v_fma_f32 v184, v184, s56, v249
v_fma_f32 v185, v185, s56, v249
v_mfma_f32_16x16x32_bf16 v[196:199], a[144:147], a[88:91], v[196:199]
v_exp_f32 v177, v177
v_fma_f32 v186, v186, s56, v249
v_fma_f32 v187, v187, s56, v249
v_mfma_f32_16x16x32_bf16 v[192:195], a[144:147], a[56:59], v[192:195]
v_exp_f32 v178, v178
v_cvt_pk_bf16_f32 v176, v176, v177
v_accvgpr_write_b32 a176, v176
v_mfma_f32_16x16x32_bf16 v[192:195], a[148:151], a[60:63], v[192:195]
ds_read_b128 a[140:143], v236 offset:33840
v_fma_f32 v200, v200, s56, v249
v_fma_f32 v201, v201, s56, v249
v_fma_f32 v202, v202, s56, v249
v_mfma_f32_16x16x32_bf16 v[188:191], a[148:151], a[28:31], v[188:191]
v_exp_f32 v179, v179
v_fma_f32 v196, v196, s56, v248
v_fma_f32 v197, v197, s56, v248
v_mfma_f32_16x16x32_bf16 v[188:191], a[144:147], a[24:27], v[188:191]
s_waitcnt lgkmcnt(2)
buffer_store_dwordx4 a[152:155], v243, s[48:51], s73 offen offset:384 sc1
v_exp_f32 v180, v180
v_cvt_pk_bf16_f32 v178, v178, v179
v_accvgpr_write_b32 a177, v178
v_fma_f32 v198, v198, s56, v248
v_mfma_f32_16x16x32_bf16 v[204:207], a[128:131], a[0:3], 0
v_exp_f32 v181, v181
v_fma_f32 v192, v192, s56, v247
v_fma_f32 v193, v193, s56, v247
v_mfma_f32_16x16x32_bf16 v[204:207], a[132:135], a[4:7], v[204:207]
s_add_u32 m0, s60, 73776
ds_read_b128 a[144:147], v236 offset:33856
v_cvt_pk_bf16_f32 v180, v180, v181
v_accvgpr_write_b32 a180, v180
v_fma_f32 v194, v194, s56, v247
v_mfma_f32_16x16x32_bf16 v[208:211], a[132:135], a[36:39], 0
buffer_load_dwordx4 v241, s[36:39], s81 offen offset:3072 nt lds
v_fma_f32 v188, v188, s56, v246
v_fma_f32 v189, v189, s56, v246
v_mfma_f32_16x16x32_bf16 v[208:211], a[128:131], a[32:35], v[208:211]
v_exp_f32 v182, v182
v_fma_f32 v190, v190, s56, v246
v_fma_f32 v191, v191, s56, v246
v_mfma_f32_16x16x32_bf16 v[212:215], a[128:131], a[64:67], 0
v_exp_f32 v183, v183
v_fma_f32 v195, v195, s56, v247
v_fma_f32 v199, v199, s56, v248
v_mfma_f32_16x16x32_bf16 v[212:215], a[132:135], a[68:71], v[212:215]
ds_read_b128 a[148:151], v236 offset:33872
v_cvt_pk_bf16_f32 v182, v182, v183
v_accvgpr_write_b32 a181, v182
v_fma_f32 v203, v203, s56, v249
v_mfma_f32_16x16x32_bf16 v[216:219], a[132:135], a[100:103], 0
v_exp_f32 v184, v184
v_accvgpr_write_b32 a168, v232
v_mfma_f32_16x16x32_bf16 v[216:219], a[128:131], a[96:99], v[216:219]
s_waitcnt lgkmcnt(2)
v_exp_f32 v185, v185
v_mfma_f32_16x16x32_bf16 v[216:219], a[136:139], a[104:107], v[216:219]
v_exp_f32 v186, v186
v_cvt_pk_bf16_f32 v184, v184, v185
v_accvgpr_write_b32 a184, v184
v_mfma_f32_16x16x32_bf16 v[216:219], a[140:143], a[108:111], v[216:219]
ds_read_b128 a[128:131], v236 offset:33888
v_mfma_f32_16x16x32_bf16 v[212:215], a[140:143], a[76:79], v[212:215]
v_exp_f32 v187, v187
v_mfma_f32_16x16x32_bf16 v[212:215], a[136:139], a[72:75], v[212:215]
v_exp_f32 v188, v188
v_cvt_pk_bf16_f32 v186, v186, v187
v_accvgpr_write_b32 a185, v186
v_mfma_f32_16x16x32_bf16 v[208:211], a[136:139], a[40:43], v[208:211]
v_exp_f32 v189, v189
v_mfma_f32_16x16x32_bf16 v[208:211], a[140:143], a[44:47], v[208:211]
ds_read_b128 a[132:135], v236 offset:33904
v_cvt_pk_bf16_f32 v188, v188, v189
v_accvgpr_write_b32 a174, v188
v_mfma_f32_16x16x32_bf16 v[204:207], a[140:143], a[12:15], v[204:207]
v_exp_f32 v190, v190
v_mfma_f32_16x16x32_bf16 v[204:207], a[136:139], a[8:11], v[204:207]
s_waitcnt lgkmcnt(2)
v_exp_f32 v191, v191
v_mfma_f32_16x16x32_bf16 v[204:207], a[144:147], a[16:19], v[204:207]
v_exp_f32 v192, v192
v_cvt_pk_bf16_f32 v190, v190, v191
v_accvgpr_write_b32 a175, v190
v_mfma_f32_16x16x32_bf16 v[204:207], a[148:151], a[20:23], v[204:207]
ds_read_b128 a[136:139], v236 offset:34304
v_mfma_f32_16x16x32_bf16 v[208:211], a[148:151], a[52:55], v[208:211]
v_exp_f32 v193, v193
v_mfma_f32_16x16x32_bf16 v[208:211], a[144:147], a[48:51], v[208:211]
v_exp_f32 v194, v194
v_cvt_pk_bf16_f32 v192, v192, v193
v_accvgpr_write_b32 a178, v192
v_mfma_f32_16x16x32_bf16 v[212:215], a[144:147], a[80:83], v[212:215]
v_exp_f32 v195, v195
v_mfma_f32_16x16x32_bf16 v[212:215], a[148:151], a[84:87], v[212:215]
ds_read_b128 a[140:143], v236 offset:34320
v_cvt_pk_bf16_f32 v194, v194, v195
v_accvgpr_write_b32 a179, v194
v_mfma_f32_16x16x32_bf16 v[216:219], a[148:151], a[116:119], v[216:219]
v_exp_f32 v196, v196
v_mfma_f32_16x16x32_bf16 v[216:219], a[144:147], a[112:115], v[216:219]
s_waitcnt lgkmcnt(2)
ds_read_b64_tr_b16 a[152:153], v245 offset:36960
v_mfma_f32_16x16x32_bf16 v[216:219], a[128:131], a[120:123], v[216:219]
ds_read_b64_tr_b16 a[154:155], v245 offset:38016
v_mfma_f32_16x16x32_bf16 v[216:219], a[132:135], a[124:127], v[216:219]
ds_read_b128 a[144:147], v236 offset:34336
v_mfma_f32_16x16x32_bf16 v[212:215], a[132:135], a[92:95], v[212:215]
v_exp_f32 v197, v197
v_mfma_f32_16x16x32_bf16 v[212:215], a[128:131], a[88:91], v[212:215]
v_exp_f32 v198, v198
v_cvt_pk_bf16_f32 v196, v196, v197
v_accvgpr_write_b32 a182, v196
v_mfma_f32_16x16x32_bf16 v[208:211], a[128:131], a[56:59], v[208:211]
v_exp_f32 v199, v199
v_fma_f32 v216, v216, s56, v249
v_fma_f32 v217, v217, s56, v249
v_mfma_f32_16x16x32_bf16 v[208:211], a[132:135], a[60:63], v[208:211]
ds_read_b128 a[148:151], v236 offset:34352
v_cvt_pk_bf16_f32 v198, v198, v199
v_accvgpr_write_b32 a183, v198
v_fma_f32 v218, v218, s56, v249
v_mfma_f32_16x16x32_bf16 v[204:207], a[132:135], a[28:31], v[204:207]
v_exp_f32 v200, v200
v_fma_f32 v212, v212, s56, v248
v_fma_f32 v213, v213, s56, v248
v_mfma_f32_16x16x32_bf16 v[204:207], a[128:131], a[24:27], v[204:207]
s_waitcnt lgkmcnt(4)
v_exp_f32 v201, v201
v_fma_f32 v214, v214, s56, v248
v_fma_f32 v215, v215, s56, v248
v_mfma_f32_16x16x32_bf16 v[156:159], a[136:139], a[0:3], 0
s_waitcnt lgkmcnt(2)
buffer_store_dwordx4 a[152:155], v243, s[48:51], s74 offen offset:384 sc1
v_exp_f32 v202, v202
v_cvt_pk_bf16_f32 v200, v200, v201
v_accvgpr_write_b32 a186, v200
v_fma_f32 v208, v208, s56, v247
v_mfma_f32_16x16x32_bf16 v[156:159], a[140:143], a[4:7], v[156:159]
ds_read_b128 a[128:131], v236 offset:34368
v_fma_f32 v209, v209, s56, v247
v_fma_f32 v210, v210, s56, v247
v_fma_f32 v211, v211, s56, v247
v_mfma_f32_16x16x32_bf16 v[160:163], a[140:143], a[36:39], 0
v_exp_f32 v203, v203
v_fma_f32 v204, v204, s56, v246
v_fma_f32 v205, v205, s56, v246
v_mfma_f32_16x16x32_bf16 v[160:163], a[136:139], a[32:35], v[160:163]
v_exp_f32 v204, v204
v_cvt_pk_bf16_f32 v202, v202, v203
v_accvgpr_write_b32 a187, v202
v_mfma_f32_16x16x32_bf16 v[164:167], a[136:139], a[64:67], 0
v_exp_f32 v205, v205
v_fma_f32 v206, v206, s56, v246
v_fma_f32 v207, v207, s56, v246
v_mfma_f32_16x16x32_bf16 v[164:167], a[140:143], a[68:71], v[164:167]
ds_read_b128 a[132:135], v236 offset:34384
v_cvt_pk_bf16_f32 v204, v204, v205
v_accvgpr_write_b32 a188, v204
v_fma_f32 v219, v219, s56, v249
v_mfma_f32_16x16x32_bf16 v[168:171], a[140:143], a[100:103], 0
v_exp_f32 v206, v206
v_mfma_f32_16x16x32_bf16 v[168:171], a[136:139], a[96:99], v[168:171]
s_waitcnt lgkmcnt(2)
v_exp_f32 v207, v207
v_mfma_f32_16x16x32_bf16 v[168:171], a[144:147], a[104:107], v[168:171]
v_exp_f32 v208, v208
v_cvt_pk_bf16_f32 v206, v206, v207
v_accvgpr_write_b32 a189, v206
v_mfma_f32_16x16x32_bf16 v[168:171], a[148:151], a[108:111], v[168:171]
ds_read_b128 a[136:139], v236 offset:34400
v_mfma_f32_16x16x32_bf16 v[164:167], a[148:151], a[76:79], v[164:167]
v_exp_f32 v209, v209
v_mfma_f32_16x16x32_bf16 v[164:167], a[144:147], a[72:75], v[164:167]
v_exp_f32 v210, v210
v_cvt_pk_bf16_f32 v208, v208, v209
v_accvgpr_write_b32 a192, v208
v_mfma_f32_16x16x32_bf16 v[160:163], a[144:147], a[40:43], v[160:163]
v_exp_f32 v211, v211
v_mfma_f32_16x16x32_bf16 v[160:163], a[148:151], a[44:47], v[160:163]
s_add_u32 m0, s60, 90624
ds_read_b128 a[140:143], v236 offset:34416
v_cvt_pk_bf16_f32 v210, v210, v211
v_accvgpr_write_b32 a193, v210
v_mfma_f32_16x16x32_bf16 v[156:159], a[148:151], a[12:15], v[156:159]
s_waitcnt vmcnt(8) lgkmcnt(2)
v_exp_f32 v212, v212
v_mfma_f32_16x16x32_bf16 v[156:159], a[144:147], a[8:11], v[156:159]
s_barrier
v_mfma_f32_16x16x32_bf16 v[156:159], a[128:131], a[16:19], v[156:159]
buffer_load_dwordx4 v241, s[36:39], s82 offen offset:0 nt lds
v_mfma_f32_16x16x32_bf16 v[156:159], a[132:135], a[20:23], v[156:159]
ds_read_b128 a[144:147], v236 offset:50688
v_mfma_f32_16x16x32_bf16 v[160:163], a[132:135], a[52:55], v[160:163]
v_exp_f32 v213, v213
v_mfma_f32_16x16x32_bf16 v[160:163], a[128:131], a[48:51], v[160:163]
v_exp_f32 v214, v214
v_cvt_pk_bf16_f32 v212, v212, v213
v_accvgpr_write_b32 a196, v212
v_mfma_f32_16x16x32_bf16 v[164:167], a[128:131], a[80:83], v[164:167]
v_exp_f32 v215, v215
v_mfma_f32_16x16x32_bf16 v[164:167], a[132:135], a[84:87], v[164:167]
ds_read_b128 a[148:151], v236 offset:50704
v_cvt_pk_bf16_f32 v214, v214, v215
v_accvgpr_write_b32 a197, v214
v_mfma_f32_16x16x32_bf16 v[168:171], a[132:135], a[116:119], v[168:171]
s_add_u32 m0, s60, 90640
v_exp_f32 v216, v216
v_mfma_f32_16x16x32_bf16 v[168:171], a[128:131], a[112:115], v[168:171]
s_waitcnt lgkmcnt(2)
v_exp_f32 v217, v217
v_mfma_f32_16x16x32_bf16 v[168:171], a[136:139], a[120:123], v[168:171]
buffer_load_dwordx4 v241, s[36:39], s82 offen offset:1024 nt lds
)"
R"(
v_cvt_pk_bf16_f32 v216, v216, v217
v_accvgpr_write_b32 a200, v216
v_mfma_f32_16x16x32_bf16 v[168:171], a[140:143], a[124:127], v[168:171]
ds_read_b128 a[128:131], v236 offset:50720
v_mfma_f32_16x16x32_bf16 v[164:167], a[140:143], a[92:95], v[164:167]
ds_read_b64_tr_b16 a[152:153], v245 offset:36992
v_mfma_f32_16x16x32_bf16 v[164:167], a[136:139], a[88:91], v[164:167]
ds_read_b64_tr_b16 a[154:155], v245 offset:38048
v_mfma_f32_16x16x32_bf16 v[160:163], a[136:139], a[56:59], v[160:163]
v_exp_f32 v218, v218
v_mfma_f32_16x16x32_bf16 v[160:163], a[140:143], a[60:63], v[160:163]
ds_read_b128 a[132:135], v236 offset:50736
v_fma_f32 v168, v168, s56, v249
v_fma_f32 v169, v169, s56, v249
v_fma_f32 v170, v170, s56, v249
v_mfma_f32_16x16x32_bf16 v[156:159], a[140:143], a[28:31], v[156:159]
s_add_u32 m0, s60, 90656
v_exp_f32 v219, v219
v_fma_f32 v164, v164, s56, v248
v_fma_f32 v165, v165, s56, v248
v_mfma_f32_16x16x32_bf16 v[156:159], a[136:139], a[24:27], v[156:159]
s_waitcnt lgkmcnt(4)
v_exp_f32 v235, v235
v_cvt_pk_bf16_f32 v218, v218, v219
v_mfma_f32_16x16x32_bf16 v[172:175], a[144:147], a[0:3], 0
buffer_load_dwordx4 v241, s[36:39], s82 offen offset:2048 nt lds
v_mfma_f32_16x16x32_bf16 v[172:175], a[148:151], a[4:7], v[172:175]
ds_read_b128 a[136:139], v236 offset:50752
v_accvgpr_write_b32 a201, v218
v_mfma_f32_16x16x32_bf16 v[176:179], a[148:151], a[36:39], 0
v_exp_f32 v164, v164
v_mfma_f32_16x16x32_bf16 v[176:179], a[144:147], a[32:35], v[176:179]
s_waitcnt lgkmcnt(2)
buffer_store_dwordx4 a[152:155], v243, s[48:51], s75 offen offset:384 sc1
v_exp_f32 v165, v165
v_cvt_pk_bf16_f32 v234, v234, v235
v_mfma_f32_16x16x32_bf16 v[180:183], a[144:147], a[64:67], 0
v_exp_f32 v168, v168
v_mfma_f32_16x16x32_bf16 v[180:183], a[148:151], a[68:71], v[180:183]
ds_read_b128 a[140:143], v236 offset:50768
v_accvgpr_write_b32 a169, v234
v_mfma_f32_16x16x32_bf16 v[184:187], a[148:151], a[100:103], 0
s_add_u32 m0, s60, 90672
v_exp_f32 v169, v169
v_mfma_f32_16x16x32_bf16 v[184:187], a[144:147], a[96:99], v[184:187]
s_waitcnt lgkmcnt(2)
v_exp_f32 v170, v170
v_mfma_f32_16x16x32_bf16 v[184:187], a[128:131], a[104:107], v[184:187]
buffer_load_dwordx4 v241, s[36:39], s82 offen offset:3072 nt lds
v_mfma_f32_16x16x32_bf16 v[184:187], a[132:135], a[108:111], v[184:187]
ds_read_b128 a[144:147], v236 offset:50784
v_fma_f32 v156, v156, s56, v246
v_mfma_f32_16x16x32_bf16 v[180:183], a[132:135], a[76:79], v[180:183]
v_exp_f32 v156, v156
v_mfma_f32_16x16x32_bf16 v[180:183], a[128:131], a[72:75], v[180:183]
v_fma_f32 v157, v157, s56, v246
v_fma_f32 v158, v158, s56, v246
v_mfma_f32_16x16x32_bf16 v[176:179], a[128:131], a[40:43], v[176:179]
v_exp_f32 v157, v157
v_mfma_f32_16x16x32_bf16 v[176:179], a[132:135], a[44:47], v[176:179]
ds_read_b128 a[148:151], v236 offset:50800
v_fma_f32 v159, v159, s56, v246
v_mfma_f32_16x16x32_bf16 v[172:175], a[132:135], a[12:15], v[172:175]
v_exp_f32 v158, v158
v_mfma_f32_16x16x32_bf16 v[172:175], a[128:131], a[8:11], v[172:175]
s_waitcnt lgkmcnt(2)
v_exp_f32 v159, v159
v_mfma_f32_16x16x32_bf16 v[172:175], a[136:139], a[16:19], v[172:175]
v_fma_f32 v160, v160, s56, v247
v_fma_f32 v161, v161, s56, v247
v_mfma_f32_16x16x32_bf16 v[172:175], a[140:143], a[20:23], v[172:175]
ds_read_b128 a[128:131], v236 offset:51200
v_fma_f32 v162, v162, s56, v247
v_mfma_f32_16x16x32_bf16 v[176:179], a[140:143], a[52:55], v[176:179]
v_exp_f32 v160, v160
v_mfma_f32_16x16x32_bf16 v[176:179], a[136:139], a[48:51], v[176:179]
v_exp_f32 v161, v161
v_mfma_f32_16x16x32_bf16 v[180:183], a[136:139], a[80:83], v[180:183]
v_exp_f32 v162, v162
v_mfma_f32_16x16x32_bf16 v[180:183], a[140:143], a[84:87], v[180:183]
ds_read_b128 a[132:135], v236 offset:51216
v_fma_f32 v163, v163, s56, v247
v_mfma_f32_16x16x32_bf16 v[184:187], a[140:143], a[116:119], v[184:187]
v_exp_f32 v163, v163
v_mfma_f32_16x16x32_bf16 v[184:187], a[136:139], a[112:115], v[184:187]
s_waitcnt lgkmcnt(2)
v_fma_f32 v166, v166, s56, v248
v_fma_f32 v167, v167, s56, v248
v_mfma_f32_16x16x32_bf16 v[184:187], a[144:147], a[120:123], v[184:187]
v_exp_f32 v166, v166
v_mfma_f32_16x16x32_bf16 v[184:187], a[148:151], a[124:127], v[184:187]
ds_read_b128 a[136:139], v236 offset:51232
v_fma_f32 v171, v171, s56, v249
v_mfma_f32_16x16x32_bf16 v[180:183], a[148:151], a[92:95], v[180:183]
ds_read_b64_tr_b16 a[152:153], v245 offset:37024
v_cvt_pk_bf16_f32 v156, v156, v157
v_mfma_f32_16x16x32_bf16 v[180:183], a[144:147], a[88:91], v[180:183]
ds_read_b64_tr_b16 a[154:155], v245 offset:38080
v_accvgpr_write_b32 a190, v156
v_mfma_f32_16x16x32_bf16 v[176:179], a[144:147], a[56:59], v[176:179]
v_exp_f32 v167, v167
v_mfma_f32_16x16x32_bf16 v[176:179], a[148:151], a[60:63], v[176:179]
ds_read_b128 a[140:143], v236 offset:51248
v_fma_f32 v184, v184, s56, v249
v_mfma_f32_16x16x32_bf16 v[172:175], a[148:151], a[28:31], v[172:175]
v_exp_f32 v171, v171
v_mfma_f32_16x16x32_bf16 v[172:175], a[144:147], a[24:27], v[172:175]
s_waitcnt lgkmcnt(4)
v_exp_f32 v184, v184
v_mfma_f32_16x16x32_bf16 v[188:191], a[128:131], a[0:3], 0
v_fma_f32 v180, v180, s56, v248
v_fma_f32 v176, v176, s56, v247
v_mfma_f32_16x16x32_bf16 v[188:191], a[132:135], a[4:7], v[188:191]
ds_read_b128 a[144:147], v236 offset:51264
v_fma_f32 v177, v177, s56, v247
v_mfma_f32_16x16x32_bf16 v[192:195], a[132:135], a[36:39], 0
v_exp_f32 v176, v176
v_mfma_f32_16x16x32_bf16 v[192:195], a[128:131], a[32:35], v[192:195]
s_waitcnt lgkmcnt(2)
buffer_store_dwordx4 a[152:155], v243, s[48:51], s76 offen offset:384 sc1
v_exp_f32 v177, v177
v_fma_f32 v172, v172, s56, v246
v_mfma_f32_16x16x32_bf16 v[196:199], a[128:131], a[64:67], 0
v_exp_f32 v172, v172
v_mfma_f32_16x16x32_bf16 v[196:199], a[132:135], a[68:71], v[196:199]
ds_read_b128 a[148:151], v236 offset:51280
v_fma_f32 v173, v173, s56, v246
v_mfma_f32_16x16x32_bf16 v[200:203], a[132:135], a[100:103], 0
v_exp_f32 v173, v173
v_mfma_f32_16x16x32_bf16 v[200:203], a[128:131], a[96:99], v[200:203]
s_waitcnt lgkmcnt(2)
v_exp_f32 v180, v180
v_mfma_f32_16x16x32_bf16 v[200:203], a[136:139], a[104:107], v[200:203]
v_fma_f32 v174, v174, s56, v246
v_fma_f32 v175, v175, s56, v246
v_mfma_f32_16x16x32_bf16 v[200:203], a[140:143], a[108:111], v[200:203]
ds_read_b128 a[128:131], v236 offset:51296
v_fma_f32 v178, v178, s56, v247
v_mfma_f32_16x16x32_bf16 v[196:199], a[140:143], a[76:79], v[196:199]
v_exp_f32 v174, v174
v_mfma_f32_16x16x32_bf16 v[196:199], a[136:139], a[72:75], v[196:199]
v_exp_f32 v175, v175
v_mfma_f32_16x16x32_bf16 v[192:195], a[136:139], a[40:43], v[192:195]
v_exp_f32 v178, v178
v_mfma_f32_16x16x32_bf16 v[192:195], a[140:143], a[44:47], v[192:195]
s_add_u32 m0, s60, 107520
ds_read_b128 a[132:135], v236 offset:51312
v_fma_f32 v179, v179, s56, v247
v_mfma_f32_16x16x32_bf16 v[188:191], a[140:143], a[12:15], v[188:191]
s_waitcnt vmcnt(7) lgkmcnt(2)
v_exp_f32 v179, v179
v_mfma_f32_16x16x32_bf16 v[188:191], a[136:139], a[8:11], v[188:191]
s_barrier
v_mfma_f32_16x16x32_bf16 v[188:191], a[144:147], a[16:19], v[188:191]
buffer_load_dwordx4 v241, s[36:39], s83 offen offset:0 nt lds
v_mfma_f32_16x16x32_bf16 v[188:191], a[148:151], a[20:23], v[188:191]
ds_read_b128 a[136:139], v236 offset:0
v_fma_f32 v181, v181, s56, v248
v_mfma_f32_16x16x32_bf16 v[192:195], a[148:151], a[52:55], v[192:195]
v_exp_f32 v181, v181
v_mfma_f32_16x16x32_bf16 v[192:195], a[144:147], a[48:51], v[192:195]
v_fma_f32 v182, v182, s56, v248
v_fma_f32 v183, v183, s56, v248
v_mfma_f32_16x16x32_bf16 v[196:199], a[144:147], a[80:83], v[196:199]
v_exp_f32 v182, v182
v_mfma_f32_16x16x32_bf16 v[196:199], a[148:151], a[84:87], v[196:199]
ds_read_b128 a[140:143], v236 offset:16
v_fma_f32 v185, v185, s56, v249
v_mfma_f32_16x16x32_bf16 v[200:203], a[148:151], a[116:119], v[200:203]
s_add_u32 m0, s60, 107536
v_exp_f32 v183, v183
v_mfma_f32_16x16x32_bf16 v[200:203], a[144:147], a[112:115], v[200:203]
s_waitcnt lgkmcnt(2)
v_exp_f32 v185, v185
v_mfma_f32_16x16x32_bf16 v[200:203], a[128:131], a[120:123], v[200:203]
buffer_load_dwordx4 v241, s[36:39], s83 offen offset:1024 nt lds
v_mfma_f32_16x16x32_bf16 v[200:203], a[132:135], a[124:127], v[200:203]
ds_read_b128 a[144:147], v236 offset:32
v_fma_f32 v186, v186, s56, v249
v_mfma_f32_16x16x32_bf16 v[196:199], a[132:135], a[92:95], v[196:199]
ds_read_b64_tr_b16 a[152:153], v245 offset:37056
v_fma_f32 v187, v187, s56, v249
v_mfma_f32_16x16x32_bf16 v[196:199], a[128:131], a[88:91], v[196:199]
ds_read_b64_tr_b16 a[154:155], v245 offset:38112
v_cvt_pk_bf16_f32 v158, v158, v159
v_mfma_f32_16x16x32_bf16 v[192:195], a[128:131], a[56:59], v[192:195]
v_exp_f32 v186, v186
v_mfma_f32_16x16x32_bf16 v[192:195], a[132:135], a[60:63], v[192:195]
ds_read_b128 a[148:151], v236 offset:48
v_fma_f32 v200, v200, s56, v249
v_mfma_f32_16x16x32_bf16 v[188:191], a[132:135], a[28:31], v[188:191]
s_add_u32 m0, s60, 107552
v_exp_f32 v187, v187
v_mfma_f32_16x16x32_bf16 v[188:191], a[128:131], a[24:27], v[188:191]
s_waitcnt lgkmcnt(4)
v_exp_f32 v200, v200
v_mfma_f32_16x16x32_bf16 v[204:207], a[136:139], a[0:3], 0
buffer_load_dwordx4 v241, s[36:39], s83 offen offset:2048 nt lds
v_mfma_f32_16x16x32_bf16 v[204:207], a[140:143], a[4:7], v[204:207]
ds_read_b128 a[128:131], v236 offset:64
v_fma_f32 v192, v192, s56, v247
v_mfma_f32_16x16x32_bf16 v[208:211], a[140:143], a[36:39], 0
v_exp_f32 v192, v192
v_mfma_f32_16x16x32_bf16 v[208:211], a[136:139], a[32:35], v[208:211]
s_waitcnt lgkmcnt(2)
buffer_store_dwordx4 a[152:155], v243, s[48:51], s77 offen offset:384 sc1
v_fma_f32 v188, v188, s56, v246
v_fma_f32 v189, v189, s56, v246
v_fma_f32 v190, v190, s56, v246
v_mfma_f32_16x16x32_bf16 v[212:215], a[136:139], a[64:67], 0
v_exp_f32 v188, v188
v_mfma_f32_16x16x32_bf16 v[212:215], a[140:143], a[68:71], v[212:215]
ds_read_b128 a[132:135], v236 offset:80
v_fma_f32 v191, v191, s56, v246
v_mfma_f32_16x16x32_bf16 v[216:219], a[140:143], a[100:103], 0
s_add_u32 m0, s60, 107568
v_exp_f32 v189, v189
v_mfma_f32_16x16x32_bf16 v[216:219], a[136:139], a[96:99], v[216:219]
s_waitcnt lgkmcnt(2)
v_exp_f32 v190, v190
v_mfma_f32_16x16x32_bf16 v[216:219], a[144:147], a[104:107], v[216:219]
buffer_load_dwordx4 v241, s[36:39], s83 offen offset:3072 nt lds
v_mfma_f32_16x16x32_bf16 v[216:219], a[148:151], a[108:111], v[216:219]
ds_read_b128 a[136:139], v236 offset:96
v_fma_f32 v193, v193, s56, v247
v_mfma_f32_16x16x32_bf16 v[212:215], a[148:151], a[76:79], v[212:215]
v_exp_f32 v191, v191
v_mfma_f32_16x16x32_bf16 v[212:215], a[144:147], a[72:75], v[212:215]
v_exp_f32 v193, v193
v_mfma_f32_16x16x32_bf16 v[208:211], a[144:147], a[40:43], v[208:211]
v_fma_f32 v194, v194, s56, v247
v_fma_f32 v195, v195, s56, v247
v_mfma_f32_16x16x32_bf16 v[208:211], a[148:151], a[44:47], v[208:211]
ds_read_b128 a[140:143], v236 offset:112
v_fma_f32 v196, v196, s56, v248
v_mfma_f32_16x16x32_bf16 v[204:207], a[148:151], a[12:15], v[204:207]
v_exp_f32 v194, v194
v_mfma_f32_16x16x32_bf16 v[204:207], a[144:147], a[8:11], v[204:207]
s_waitcnt lgkmcnt(2)
v_exp_f32 v195, v195
v_mfma_f32_16x16x32_bf16 v[204:207], a[128:131], a[16:19], v[204:207]
v_exp_f32 v196, v196
v_mfma_f32_16x16x32_bf16 v[204:207], a[132:135], a[20:23], v[204:207]
ds_read_b128 a[144:147], v236 offset:512
v_fma_f32 v197, v197, s56, v248
v_mfma_f32_16x16x32_bf16 v[208:211], a[132:135], a[52:55], v[208:211]
v_exp_f32 v197, v197
v_mfma_f32_16x16x32_bf16 v[208:211], a[128:131], a[48:51], v[208:211]
v_fma_f32 v198, v198, s56, v248
v_fma_f32 v199, v199, s56, v248
v_mfma_f32_16x16x32_bf16 v[212:215], a[128:131], a[80:83], v[212:215]
v_exp_f32 v198, v198
v_mfma_f32_16x16x32_bf16 v[212:215], a[132:135], a[84:87], v[212:215]
ds_read_b128 a[148:151], v236 offset:528
v_fma_f32 v201, v201, s56, v249
v_mfma_f32_16x16x32_bf16 v[216:219], a[132:135], a[116:119], v[216:219]
v_exp_f32 v199, v199
v_mfma_f32_16x16x32_bf16 v[216:219], a[128:131], a[112:115], v[216:219]
s_waitcnt lgkmcnt(2)
v_exp_f32 v201, v201
v_mfma_f32_16x16x32_bf16 v[216:219], a[136:139], a[120:123], v[216:219]
v_fma_f32 v202, v202, s56, v249
v_fma_f32 v203, v203, s56, v249
v_mfma_f32_16x16x32_bf16 v[216:219], a[140:143], a[124:127], v[216:219]
ds_read_b128 a[128:131], v236 offset:544
)"
R"(
v_accvgpr_write_b32 a191, v158
v_mfma_f32_16x16x32_bf16 v[212:215], a[140:143], a[92:95], v[212:215]
v_exp_f32 v202, v202
v_mfma_f32_16x16x32_bf16 v[212:215], a[136:139], a[88:91], v[212:215]
ds_read_b64_tr_b16 a[152:153], v245 offset:37088
v_cvt_pk_bf16_f32 v160, v160, v161
v_mfma_f32_16x16x32_bf16 v[208:211], a[136:139], a[56:59], v[208:211]
ds_read_b64_tr_b16 a[154:155], v245 offset:38144
v_accvgpr_write_b32 a194, v160
v_mfma_f32_16x16x32_bf16 v[208:211], a[140:143], a[60:63], v[208:211]
.Lpad11_%=:
s_cbranch_vccnz .Lret2_%=
ds_read_b128 a[132:135], v236 offset:560
v_cvt_pk_bf16_f32 v162, v162, v163
v_mfma_f32_16x16x32_bf16 v[204:207], a[140:143], a[28:31], v[204:207]
v_exp_f32 v203, v203
v_mfma_f32_16x16x32_bf16 v[204:207], a[136:139], a[24:27], v[204:207]
s_waitcnt lgkmcnt(4)
v_accvgpr_write_b32 a195, v162
v_cvt_pk_bf16_f32 v164, v164, v165
v_mfma_f32_16x16x32_bf16 v[220:223], a[144:147], a[0:3], 0
v_accvgpr_write_b32 a198, v164
v_cvt_pk_bf16_f32 v166, v166, v167
v_mfma_f32_16x16x32_bf16 v[220:223], a[148:151], a[4:7], v[220:223]
ds_read_b128 a[136:139], v236 offset:576
v_accvgpr_write_b32 a199, v166
v_mfma_f32_16x16x32_bf16 v[224:227], a[148:151], a[36:39], 0
v_cvt_pk_bf16_f32 v168, v168, v169
v_accvgpr_write_b32 a202, v168
v_mfma_f32_16x16x32_bf16 v[224:227], a[144:147], a[32:35], v[224:227]
v_cvt_pk_bf16_f32 v170, v170, v171
v_accvgpr_write_b32 a203, v170
v_mfma_f32_16x16x32_bf16 v[228:231], a[144:147], a[64:67], 0
s_waitcnt lgkmcnt(2)
buffer_store_dwordx4 a[152:155], v243, s[48:51], s78 offen offset:384 sc1
v_cvt_pk_bf16_f32 v172, v172, v173
v_accvgpr_write_b32 a204, v172
v_cvt_pk_bf16_f32 v174, v174, v175
v_mfma_f32_16x16x32_bf16 v[228:231], a[148:151], a[68:71], v[228:231]
s_add_u32 s48, s48, 0x2000000
s_addc_u32 s49, s49, 0
ds_read_b128 a[140:143], v236 offset:592
v_accvgpr_write_b32 a205, v174
v_mfma_f32_16x16x32_bf16 v[232:235], a[148:151], a[100:103], 0
v_cvt_pk_bf16_f32 v176, v176, v177
v_accvgpr_write_b32 a208, v176
v_mfma_f32_16x16x32_bf16 v[232:235], a[144:147], a[96:99], v[232:235]
s_waitcnt lgkmcnt(2)
v_cvt_pk_bf16_f32 v178, v178, v179
v_accvgpr_write_b32 a209, v178
v_mfma_f32_16x16x32_bf16 v[232:235], a[128:131], a[104:107], v[232:235]
v_cvt_pk_bf16_f32 v180, v180, v181
v_accvgpr_write_b32 a212, v180
v_mfma_f32_16x16x32_bf16 v[232:235], a[132:135], a[108:111], v[232:235]
ds_read_b128 a[144:147], v236 offset:608
v_cvt_pk_bf16_f32 v182, v182, v183
v_mfma_f32_16x16x32_bf16 v[228:231], a[132:135], a[76:79], v[228:231]
v_accvgpr_write_b32 a213, v182
v_cvt_pk_bf16_f32 v184, v184, v185
v_mfma_f32_16x16x32_bf16 v[228:231], a[128:131], a[72:75], v[228:231]
v_accvgpr_write_b32 a216, v184
v_cvt_pk_bf16_f32 v186, v186, v187
v_mfma_f32_16x16x32_bf16 v[224:227], a[128:131], a[40:43], v[224:227]
v_accvgpr_write_b32 a217, v186
v_cvt_pk_bf16_f32 v188, v188, v189
v_mfma_f32_16x16x32_bf16 v[224:227], a[132:135], a[44:47], v[224:227]
s_add_u32 m0, s60, 124416
ds_read_b128 a[148:151], v236 offset:624
v_accvgpr_write_b32 a206, v188
v_mfma_f32_16x16x32_bf16 v[220:223], a[132:135], a[12:15], v[220:223]
s_waitcnt vmcnt(7) lgkmcnt(2)
v_cvt_pk_bf16_f32 v190, v190, v191
v_accvgpr_write_b32 a207, v190
v_mfma_f32_16x16x32_bf16 v[220:223], a[128:131], a[8:11], v[220:223]
s_barrier
v_mfma_f32_16x16x32_bf16 v[220:223], a[136:139], a[16:19], v[220:223]
buffer_load_dwordx4 v241, s[36:39], s84 offen offset:0  lds
v_mfma_f32_16x16x32_bf16 v[220:223], a[140:143], a[20:23], v[220:223]
ds_read_b128 a[128:131], v236 offset:16896
v_cvt_pk_bf16_f32 v192, v192, v193
v_mfma_f32_16x16x32_bf16 v[224:227], a[140:143], a[52:55], v[224:227]
v_accvgpr_write_b32 a210, v192
v_cvt_pk_bf16_f32 v194, v194, v195
v_mfma_f32_16x16x32_bf16 v[224:227], a[136:139], a[48:51], v[224:227]
v_accvgpr_write_b32 a211, v194
v_cvt_pk_bf16_f32 v196, v196, v197
v_mfma_f32_16x16x32_bf16 v[228:231], a[136:139], a[80:83], v[228:231]
v_accvgpr_write_b32 a214, v196
v_cvt_pk_bf16_f32 v198, v198, v199
v_mfma_f32_16x16x32_bf16 v[228:231], a[140:143], a[84:87], v[228:231]
ds_read_b128 a[132:135], v236 offset:16912
v_accvgpr_write_b32 a215, v198
v_mfma_f32_16x16x32_bf16 v[232:235], a[140:143], a[116:119], v[232:235]
s_add_u32 m0, s60, 124432
v_cvt_pk_bf16_f32 v200, v200, v201
v_accvgpr_write_b32 a218, v200
v_mfma_f32_16x16x32_bf16 v[232:235], a[136:139], a[112:115], v[232:235]
s_waitcnt lgkmcnt(2)
v_cvt_pk_bf16_f32 v202, v202, v203
v_accvgpr_write_b32 a219, v202
v_mfma_f32_16x16x32_bf16 v[232:235], a[144:147], a[120:123], v[232:235]
buffer_load_dwordx4 v241, s[36:39], s84 offen offset:1024  lds
v_mfma_f32_16x16x32_bf16 v[232:235], a[148:151], a[124:127], v[232:235]
ds_read_b128 a[136:139], v236 offset:16928
v_mfma_f32_16x16x32_bf16 v[228:231], a[148:151], a[92:95], v[228:231]
v_mfma_f32_16x16x32_bf16 v[228:231], a[144:147], a[88:91], v[228:231]
v_mfma_f32_16x16x32_bf16 v[224:227], a[144:147], a[56:59], v[224:227]
v_mfma_f32_16x16x32_bf16 v[224:227], a[148:151], a[60:63], v[224:227]
ds_read_b128 a[140:143], v236 offset:16944
v_mfma_f32_16x16x32_bf16 v[220:223], a[148:151], a[28:31], v[220:223]
v_mfma_f32_16x16x32_bf16 v[220:223], a[144:147], a[24:27], v[220:223]
s_waitcnt lgkmcnt(2)
s_branch .LC%=
.LX1%=:
v_mfma_f32_16x16x32_bf16 v[220:223], a[240:243], a[128:131], v[220:223]
.Lpad12_%=:
s_cbranch_vccnz .Lret3_%=
s_add_u32 m0, s61, 132496
ds_read_b64_tr_b16 a[144:145], v238 offset:8464
v_mul_f32_dpp v175, v250, v175 row_newbcast:3 row_mask:0xf bank_mask:0xf
v_mfma_f32_16x16x32_bf16 v[220:223], v[88:91], a[132:135], v[220:223]
buffer_load_dwordx4 v242, s[40:43], s87 offen offset:1408 nt lds
v_mfma_f32_16x16x32_bf16 v[224:227], a[244:247], a[132:135], v[224:227]
ds_read_b64_tr_b16 a[146:147], v238 offset:8592
v_mul_f32_dpp v176, v251, v176 row_newbcast:0 row_mask:0xf bank_mask:0xf
v_mfma_f32_16x16x32_bf16 v[224:227], v[76:79], a[128:131], v[224:227]
ds_read_b64_tr_b16 a[152:153], v245 offset:36992
v_mul_f32_dpp v177, v251, v177 row_newbcast:1 row_mask:0xf bank_mask:0xf
v_mfma_f32_16x16x32_bf16 v[228:231], v[80:83], a[128:131], v[228:231]
ds_read_b64_tr_b16 a[148:149], v238 offset:8976
v_mul_f32_dpp v178, v251, v178 row_newbcast:2 row_mask:0xf bank_mask:0xf
v_mfma_f32_16x16x32_bf16 v[228:231], v[92:95], a[132:135], v[228:231]
ds_read_b64_tr_b16 a[154:155], v245 offset:38048
v_mul_f32_dpp v179, v251, v179 row_newbcast:3 row_mask:0xf bank_mask:0xf
v_mfma_f32_16x16x32_bf16 v[232:235], v[96:99], a[132:135], v[232:235]
s_waitcnt vmcnt(12) lgkmcnt(5)
ds_read_b64_tr_b16 a[150:151], v238 offset:9104
v_mul_f32_dpp v180, v252, v180 row_newbcast:0 row_mask:0xf bank_mask:0xf
v_mfma_f32_16x16x32_bf16 v[232:235], v[84:87], a[128:131], v[232:235]
s_barrier
v_mfma_f32_16x16x32_bf16 v[204:207], v[100:103], a[136:139], v[204:207]
s_add_u32 m0, s61, 140928
ds_read_b64_tr_b16 a[128:129], v238 offset:80
v_mul_f32_dpp v181, v252, v181 row_newbcast:1 row_mask:0xf bank_mask:0xf
v_mfma_f32_16x16x32_bf16 v[204:207], v[112:115], a[140:143], v[204:207]
buffer_load_dwordx4 v242, s[40:43], s89 offen offset:384 nt lds
v_mfma_f32_16x16x32_bf16 v[208:211], v[116:119], a[140:143], v[208:211]
ds_read_b64_tr_b16 a[130:131], v238 offset:208
v_mul_f32_dpp v182, v252, v182 row_newbcast:2 row_mask:0xf bank_mask:0xf
v_mfma_f32_16x16x32_bf16 v[208:211], v[104:107], a[136:139], v[208:211]
v_mul_f32_dpp v183, v252, v183 row_newbcast:3 row_mask:0xf bank_mask:0xf
v_mul_f32_dpp v184, v253, v184 row_newbcast:0 row_mask:0xf bank_mask:0xf
v_mfma_f32_16x16x32_bf16 v[212:215], a[248:251], a[136:139], v[212:215]
ds_read_b64_tr_b16 a[132:133], v238 offset:592
v_mul_f32_dpp v185, v253, v185 row_newbcast:1 row_mask:0xf bank_mask:0xf
v_mfma_f32_16x16x32_bf16 v[212:215], v[120:123], a[140:143], v[212:215]
s_waitcnt lgkmcnt(4)
buffer_store_dwordx4 a[152:155], v243, s[48:51], s75 offen offset:128 sc1
v_mul_f32_dpp v186, v253, v186 row_newbcast:2 row_mask:0xf bank_mask:0xf
v_mul_f32_dpp v187, v253, v187 row_newbcast:3 row_mask:0xf bank_mask:0xf
v_mfma_f32_16x16x32_bf16 v[216:219], a[252:255], a[140:143], v[216:219]
s_add_u32 m0, s61, 140944
ds_read_b64_tr_b16 a[134:135], v238 offset:720
v_mfma_f32_16x16x32_bf16 v[216:219], v[108:111], a[136:139], v[216:219]
s_waitcnt lgkmcnt(4)
buffer_load_dwordx4 v242, s[40:43], s89 offen offset:1408 nt lds
v_mfma_f32_16x16x32_bf16 v[188:191], v[124:127], a[144:147], v[188:191]
ds_read_b64_tr_b16 a[136:137], v238 offset:8512
v_mfma_f32_16x16x32_bf16 v[188:191], v[140:143], a[148:151], v[188:191]
ds_read_b64_tr_b16 a[152:153], v245 offset:37024
v_mfma_f32_16x16x32_bf16 v[192:195], v[144:147], a[148:151], v[192:195]
ds_read_b64_tr_b16 a[138:139], v238 offset:8640
v_mfma_f32_16x16x32_bf16 v[192:195], v[128:131], a[144:147], v[192:195]
ds_read_b64_tr_b16 a[154:155], v245 offset:38080
v_mfma_f32_16x16x32_bf16 v[196:199], v[132:135], a[144:147], v[196:199]
s_add_u32 m0, s61, 149376
ds_read_b64_tr_b16 a[140:141], v238 offset:9024
v_mul_f32_dpp v188, v250, v188 row_newbcast:0 row_mask:0xf bank_mask:0xf
v_mfma_f32_16x16x32_bf16 v[196:199], v[148:151], a[148:151], v[196:199]
buffer_load_dwordx4 v242, s[40:43], s91 offen offset:384 nt lds
v_mfma_f32_16x16x32_bf16 v[200:203], v[152:155], a[148:151], v[200:203]
ds_read_b64_tr_b16 a[142:143], v238 offset:9152
v_mul_f32_dpp v189, v250, v189 row_newbcast:1 row_mask:0xf bank_mask:0xf
v_mfma_f32_16x16x32_bf16 v[200:203], v[136:139], a[144:147], v[200:203]
s_waitcnt lgkmcnt(6)
v_mul_f32_dpp v190, v250, v190 row_newbcast:2 row_mask:0xf bank_mask:0xf
v_mul_f32_dpp v191, v250, v191 row_newbcast:3 row_mask:0xf bank_mask:0xf
v_mfma_f32_16x16x32_bf16 v[220:223], v[100:103], a[128:131], v[220:223]
ds_read_b64_tr_b16 a[144:145], v238 offset:8528
v_cvt_pk_bf16_f32 v188, v172, v188
v_mfma_f32_16x16x32_bf16 v[220:223], v[112:115], a[132:135], v[220:223]
v_cvt_pk_bf16_f32 v189, v173, v189
v_cvt_pk_bf16_f32 v190, v174, v190
v_mfma_f32_16x16x32_bf16 v[224:227], v[116:119], a[132:135], v[224:227]
s_add_u32 m0, s61, 149392
ds_read_b64_tr_b16 a[146:147], v238 offset:8656
v_cvt_pk_bf16_f32 v191, v175, v191
v_mfma_f32_16x16x32_bf16 v[224:227], v[104:107], a[128:131], v[224:227]
buffer_load_dwordx4 v242, s[40:43], s91 offen offset:1408 nt lds
v_mfma_f32_16x16x32_bf16 v[228:231], a[248:251], a[128:131], v[228:231]
ds_read_b64_tr_b16 a[148:149], v238 offset:9040
v_mul_f32_dpp v192, v251, v192 row_newbcast:0 row_mask:0xf bank_mask:0xf
v_mfma_f32_16x16x32_bf16 v[228:231], v[120:123], a[132:135], v[228:231]
s_waitcnt lgkmcnt(5)
buffer_store_dwordx4 a[152:155], v243, s[48:51], s76 offen offset:128 sc1
v_mul_f32_dpp v193, v251, v193 row_newbcast:1 row_mask:0xf bank_mask:0xf
v_mul_f32_dpp v194, v251, v194 row_newbcast:2 row_mask:0xf bank_mask:0xf
v_mul_f32_dpp v195, v251, v195 row_newbcast:3 row_mask:0xf bank_mask:0xf
v_mfma_f32_16x16x32_bf16 v[232:235], a[252:255], a[132:135], v[232:235]
ds_read_b64_tr_b16 a[150:151], v238 offset:9168
v_cvt_pk_bf16_f32 v192, v176, v192
v_mfma_f32_16x16x32_bf16 v[232:235], v[108:111], a[128:131], v[232:235]
s_waitcnt lgkmcnt(4)
ds_read_b64_tr_b16 a[152:153], v245 offset:37056
v_cvt_pk_bf16_f32 v193, v177, v193
v_mfma_f32_16x16x32_bf16 v[204:207], v[124:127], a[136:139], v[204:207]
ds_read_b64_tr_b16 a[128:129], v238 offset:16896
v_cvt_pk_bf16_f32 v194, v178, v194
v_mfma_f32_16x16x32_bf16 v[204:207], v[140:143], a[140:143], v[204:207]
ds_read_b64_tr_b16 a[154:155], v245 offset:38112
v_cvt_pk_bf16_f32 v195, v179, v195
v_mfma_f32_16x16x32_bf16 v[208:211], v[144:147], a[140:143], v[208:211]
ds_read_b64_tr_b16 a[130:131], v238 offset:17024
v_mul_f32_dpp v196, v252, v196 row_newbcast:0 row_mask:0xf bank_mask:0xf
v_mfma_f32_16x16x32_bf16 v[208:211], v[128:131], a[136:139], v[208:211]
v_mul_f32_dpp v197, v252, v197 row_newbcast:1 row_mask:0xf bank_mask:0xf
v_mul_f32_dpp v198, v252, v198 row_newbcast:2 row_mask:0xf bank_mask:0xf
v_mfma_f32_16x16x32_bf16 v[212:215], v[132:135], a[136:139], v[212:215]
ds_read_b64_tr_b16 a[132:133], v238 offset:17408
v_mul_f32_dpp v199, v252, v199 row_newbcast:3 row_mask:0xf bank_mask:0xf
v_mfma_f32_16x16x32_bf16 v[212:215], v[148:151], a[140:143], v[212:215]
v_cvt_pk_bf16_f32 v196, v180, v196
v_cvt_pk_bf16_f32 v197, v181, v197
)"
R"(
v_mfma_f32_16x16x32_bf16 v[216:219], v[152:155], a[140:143], v[216:219]
ds_read_b64_tr_b16 a[134:135], v238 offset:17536
v_cvt_pk_bf16_f32 v198, v182, v198
v_mfma_f32_16x16x32_bf16 v[216:219], v[136:139], a[136:139], v[216:219]
s_waitcnt lgkmcnt(6)
v_cvt_pk_bf16_f32 v199, v183, v199
v_mul_f32_dpp v200, v253, v200 row_newbcast:0 row_mask:0xf bank_mask:0xf
v_mfma_f32_16x16x32_bf16 v[220:223], v[124:127], a[144:147], v[220:223]
ds_read_b64_tr_b16 a[136:137], v238 offset:16912
v_mul_f32_dpp v201, v253, v201 row_newbcast:1 row_mask:0xf bank_mask:0xf
v_mfma_f32_16x16x32_bf16 v[220:223], v[140:143], a[148:151], v[220:223]
s_waitcnt lgkmcnt(4)
buffer_store_dwordx4 a[152:155], v243, s[48:51], s77 offen offset:128 sc1
v_mul_f32_dpp v202, v253, v202 row_newbcast:2 row_mask:0xf bank_mask:0xf
v_mul_f32_dpp v203, v253, v203 row_newbcast:3 row_mask:0xf bank_mask:0xf
v_cvt_pk_bf16_f32 v200, v184, v200
v_mfma_f32_16x16x32_bf16 v[224:227], v[144:147], a[148:151], v[224:227]
ds_read_b64_tr_b16 a[138:139], v238 offset:17040
v_cvt_pk_bf16_f32 v201, v185, v201
v_mfma_f32_16x16x32_bf16 v[224:227], v[128:131], a[144:147], v[224:227]
v_cvt_pk_bf16_f32 v202, v186, v202
v_cvt_pk_bf16_f32 v203, v187, v203
v_mfma_f32_16x16x32_bf16 v[228:231], v[132:135], a[144:147], v[228:231]
ds_read_b64_tr_b16 a[140:141], v238 offset:17424
v_mul_f32_dpp v204, v250, v204 row_newbcast:0 row_mask:0xf bank_mask:0xf
v_mfma_f32_16x16x32_bf16 v[228:231], v[148:151], a[148:151], v[228:231]
ds_write_b64 v244, v[188:189] offset:0
v_mfma_f32_16x16x32_bf16 v[232:235], v[152:155], a[148:151], v[232:235]
ds_read_b64_tr_b16 a[142:143], v238 offset:17552
v_mfma_f32_16x16x32_bf16 v[232:235], v[136:139], a[144:147], v[232:235]
s_waitcnt lgkmcnt(5)
ds_write_b64 v244, v[190:191] offset:8
v_mfma_f32_16x16x32_bf16 v[156:159], a[156:159], a[128:131], 0
ds_read_b64_tr_b16 a[144:145], v238 offset:25344
v_mfma_f32_16x16x32_bf16 v[156:159], a[172:175], a[132:135], v[156:159]
ds_read_b64_tr_b16 a[152:153], v245 offset:37088
v_mul_f32_dpp v205, v250, v205 row_newbcast:1 row_mask:0xf bank_mask:0xf
v_mfma_f32_16x16x32_bf16 v[160:163], a[176:179], a[132:135], 0
ds_read_b64_tr_b16 a[146:147], v238 offset:25472
v_mul_f32_dpp v206, v250, v206 row_newbcast:2 row_mask:0xf bank_mask:0xf
v_mfma_f32_16x16x32_bf16 v[160:163], a[160:163], a[128:131], v[160:163]
ds_read_b64_tr_b16 a[154:155], v245 offset:38144
v_mul_f32_dpp v207, v250, v207 row_newbcast:3 row_mask:0xf bank_mask:0xf
v_mfma_f32_16x16x32_bf16 v[164:167], a[164:167], a[128:131], 0
ds_read_b64_tr_b16 a[148:149], v238 offset:25856
v_mul_f32_dpp v208, v251, v208 row_newbcast:0 row_mask:0xf bank_mask:0xf
v_mfma_f32_16x16x32_bf16 v[164:167], a[180:183], a[132:135], v[164:167]
ds_write_b64 v244, v[192:193] offset:64
v_mfma_f32_16x16x32_bf16 v[168:171], a[184:187], a[132:135], 0
ds_read_b64_tr_b16 a[150:151], v238 offset:25984
v_mfma_f32_16x16x32_bf16 v[168:171], a[168:171], a[128:131], v[168:171]
s_waitcnt lgkmcnt(8)
ds_write_b64 v244, v[194:195] offset:72
v_mfma_f32_16x16x32_bf16 v[172:175], a[156:159], a[136:139], 0
ds_read_b64_tr_b16 a[128:129], v238 offset:25360
v_mfma_f32_16x16x32_bf16 v[172:175], a[172:175], a[140:143], v[172:175]
v_mul_f32_dpp v209, v251, v209 row_newbcast:1 row_mask:0xf bank_mask:0xf
v_mul_f32_dpp v210, v251, v210 row_newbcast:2 row_mask:0xf bank_mask:0xf
v_mfma_f32_16x16x32_bf16 v[176:179], a[176:179], a[140:143], 0
ds_read_b64_tr_b16 a[130:131], v238 offset:25488
v_mul_f32_dpp v211, v251, v211 row_newbcast:3 row_mask:0xf bank_mask:0xf
v_mfma_f32_16x16x32_bf16 v[176:179], a[160:163], a[136:139], v[176:179]
s_waitcnt lgkmcnt(6)
buffer_store_dwordx4 a[152:155], v243, s[48:51], s78 offen offset:128 sc1
v_mul_f32_dpp v212, v252, v212 row_newbcast:0 row_mask:0xf bank_mask:0xf
v_mul_f32_dpp v213, v252, v213 row_newbcast:1 row_mask:0xf bank_mask:0xf
v_mul_f32_dpp v214, v252, v214 row_newbcast:2 row_mask:0xf bank_mask:0xf
v_mfma_f32_16x16x32_bf16 v[180:183], a[164:167], a[136:139], 0
ds_read_b64_tr_b16 a[132:133], v238 offset:25872
v_mul_f32_dpp v215, v252, v215 row_newbcast:3 row_mask:0xf bank_mask:0xf
v_mfma_f32_16x16x32_bf16 v[180:183], a[180:183], a[140:143], v[180:183]
ds_write_b64 v244, v[196:197] offset:128
v_mfma_f32_16x16x32_bf16 v[184:187], a[184:187], a[140:143], 0
s_waitcnt vmcnt(14) lgkmcnt(5)
ds_read_b64_tr_b16 a[134:135], v238 offset:26000
v_mfma_f32_16x16x32_bf16 v[184:187], a[168:171], a[136:139], v[184:187]
s_barrier
v_mfma_f32_16x16x32_bf16 v[156:159], a[188:191], a[144:147], v[156:159]
ds_read_b64_tr_b16 a[136:137], v238 offset:33792
v_mul_f32_dpp v216, v253, v216 row_newbcast:0 row_mask:0xf bank_mask:0xf
v_mfma_f32_16x16x32_bf16 v[156:159], a[204:207], a[148:151], v[156:159]
ds_write_b64 v244, v[198:199] offset:136
v_mfma_f32_16x16x32_bf16 v[160:163], a[208:211], a[148:151], v[160:163]
ds_read_b64_tr_b16 a[138:139], v238 offset:33920
v_mfma_f32_16x16x32_bf16 v[160:163], a[192:195], a[144:147], v[160:163]
ds_write_b64 v244, v[200:201] offset:192
v_mfma_f32_16x16x32_bf16 v[164:167], a[196:199], a[144:147], v[164:167]
ds_read_b64_tr_b16 a[140:141], v238 offset:34304
v_mfma_f32_16x16x32_bf16 v[164:167], a[212:215], a[148:151], v[164:167]
ds_write_b64 v244, v[202:203] offset:200
v_mfma_f32_16x16x32_bf16 v[168:171], a[216:219], a[148:151], v[168:171]
ds_read_b64_tr_b16 a[142:143], v238 offset:34432
v_mfma_f32_16x16x32_bf16 v[168:171], a[200:203], a[144:147], v[168:171]
s_waitcnt lgkmcnt(7)
v_mul_f32_dpp v217, v253, v217 row_newbcast:1 row_mask:0xf bank_mask:0xf
v_mul_f32_dpp v218, v253, v218 row_newbcast:2 row_mask:0xf bank_mask:0xf
v_mfma_f32_16x16x32_bf16 v[172:175], a[188:191], a[128:131], v[172:175]
ds_read_b64_tr_b16 a[144:145], v238 offset:33808
v_mul_f32_dpp v219, v253, v219 row_newbcast:3 row_mask:0xf bank_mask:0xf
v_mfma_f32_16x16x32_bf16 v[172:175], a[204:207], a[132:135], v[172:175]
v_mul_f32_dpp v220, v250, v220 row_newbcast:0 row_mask:0xf bank_mask:0xf
v_mul_f32_dpp v221, v250, v221 row_newbcast:1 row_mask:0xf bank_mask:0xf
v_mfma_f32_16x16x32_bf16 v[176:179], a[208:211], a[132:135], v[176:179]
ds_read_b64_tr_b16 a[146:147], v238 offset:33936
v_mul_f32_dpp v222, v250, v222 row_newbcast:2 row_mask:0xf bank_mask:0xf
v_mfma_f32_16x16x32_bf16 v[176:179], a[192:195], a[128:131], v[176:179]
v_mul_f32_dpp v223, v250, v223 row_newbcast:3 row_mask:0xf bank_mask:0xf
v_mul_f32_dpp v224, v251, v224 row_newbcast:0 row_mask:0xf bank_mask:0xf
v_mfma_f32_16x16x32_bf16 v[180:183], a[196:199], a[128:131], v[180:183]
ds_read_b64_tr_b16 a[148:149], v238 offset:34320
v_mul_f32_dpp v225, v251, v225 row_newbcast:1 row_mask:0xf bank_mask:0xf
v_mfma_f32_16x16x32_bf16 v[180:183], a[212:215], a[132:135], v[180:183]
v_mul_f32_dpp v226, v251, v226 row_newbcast:2 row_mask:0xf bank_mask:0xf
v_mul_f32_dpp v227, v251, v227 row_newbcast:3 row_mask:0xf bank_mask:0xf
v_mfma_f32_16x16x32_bf16 v[184:187], a[216:219], a[132:135], v[184:187]
ds_read_b64_tr_b16 a[150:151], v238 offset:34448
v_mul_f32_dpp v228, v252, v228 row_newbcast:0 row_mask:0xf bank_mask:0xf
v_mfma_f32_16x16x32_bf16 v[184:187], a[200:203], a[128:131], v[184:187]
s_waitcnt lgkmcnt(4)
v_mul_f32_dpp v229, v252, v229 row_newbcast:1 row_mask:0xf bank_mask:0xf
v_mul_f32_dpp v230, v252, v230 row_newbcast:2 row_mask:0xf bank_mask:0xf
v_mfma_f32_16x16x32_bf16 v[156:159], a[220:223], a[136:139], v[156:159]
ds_read_b64_tr_b16 a[128:129], v238 offset:42240
v_mul_f32_dpp v231, v252, v231 row_newbcast:3 row_mask:0xf bank_mask:0xf
v_mfma_f32_16x16x32_bf16 v[156:159], a[224:227], a[140:143], v[156:159]
v_mul_f32_dpp v232, v253, v232 row_newbcast:0 row_mask:0xf bank_mask:0xf
v_mul_f32_dpp v233, v253, v233 row_newbcast:1 row_mask:0xf bank_mask:0xf
v_mfma_f32_16x16x32_bf16 v[160:163], v[12:15], a[140:143], v[160:163]
ds_read_b64_tr_b16 a[130:131], v238 offset:42368
v_mul_f32_dpp v234, v253, v234 row_newbcast:2 row_mask:0xf bank_mask:0xf
v_mfma_f32_16x16x32_bf16 v[160:163], v[0:3], a[136:139], v[160:163]
v_mul_f32_dpp v235, v253, v235 row_newbcast:3 row_mask:0xf bank_mask:0xf
v_cvt_pk_bf16_f32 v220, v204, v220
v_mfma_f32_16x16x32_bf16 v[164:167], v[4:7], a[136:139], v[164:167]
ds_read_b64_tr_b16 a[132:133], v238 offset:42752
v_cvt_pk_bf16_f32 v221, v205, v221
v_mfma_f32_16x16x32_bf16 v[164:167], v[16:19], a[140:143], v[164:167]
ds_write_b64 v244, v[220:221] offset:4224
v_mfma_f32_16x16x32_bf16 v[168:171], v[20:23], a[140:143], v[168:171]
ds_read_b64_tr_b16 a[134:135], v238 offset:42880
v_mfma_f32_16x16x32_bf16 v[168:171], v[8:11], a[136:139], v[168:171]
s_waitcnt lgkmcnt(5)
v_cvt_pk_bf16_f32 v222, v206, v222
v_cvt_pk_bf16_f32 v223, v207, v223
v_mfma_f32_16x16x32_bf16 v[172:175], a[220:223], a[144:147], v[172:175]
ds_read_b64_tr_b16 a[136:137], v238 offset:42256
v_cvt_pk_bf16_f32 v224, v208, v224
v_mfma_f32_16x16x32_bf16 v[172:175], a[224:227], a[148:151], v[172:175]
ds_write_b64 v244, v[222:223] offset:4232
v_mfma_f32_16x16x32_bf16 v[176:179], v[12:15], a[148:151], v[176:179]
ds_read_b64_tr_b16 a[138:139], v238 offset:42384
v_mfma_f32_16x16x32_bf16 v[176:179], v[0:3], a[144:147], v[176:179]
ds_read_b64_tr_b16 a[152:153], v245 offset:0
v_cvt_pk_bf16_f32 v225, v209, v225
v_mfma_f32_16x16x32_bf16 v[180:183], v[4:7], a[144:147], v[180:183]
ds_read_b64_tr_b16 a[140:141], v238 offset:42768
v_cvt_pk_bf16_f32 v226, v210, v226
v_mfma_f32_16x16x32_bf16 v[180:183], v[16:19], a[148:151], v[180:183]
ds_write_b64 v244, v[224:225] offset:4288
v_mfma_f32_16x16x32_bf16 v[184:187], v[20:23], a[148:151], v[184:187]
s_waitcnt vmcnt(8) lgkmcnt(6)
ds_read_b64_tr_b16 a[142:143], v238 offset:42896
v_cvt_pk_bf16_f32 v227, v211, v227
v_mfma_f32_16x16x32_bf16 v[184:187], v[8:11], a[144:147], v[184:187]
s_barrier
v_mfma_f32_16x16x32_bf16 v[156:159], v[24:27], a[128:131], v[156:159]
ds_read_b64_tr_b16 a[144:145], v238 offset:50688
v_cvt_pk_bf16_f32 v228, v212, v228
v_mfma_f32_16x16x32_bf16 v[156:159], v[36:39], a[132:135], v[156:159]
ds_write_b64 v244, v[226:227] offset:4296
v_mfma_f32_16x16x32_bf16 v[160:163], v[40:43], a[132:135], v[160:163]
ds_read_b64_tr_b16 a[146:147], v238 offset:50816
v_cvt_pk_bf16_f32 v229, v213, v229
v_mfma_f32_16x16x32_bf16 v[160:163], a[228:231], a[128:131], v[160:163]
ds_write_b64 v244, v[228:229] offset:4352
v_mfma_f32_16x16x32_bf16 v[164:167], v[28:31], a[128:131], v[164:167]
ds_read_b64_tr_b16 a[148:149], v238 offset:51200
v_cvt_pk_bf16_f32 v230, v214, v230
v_mfma_f32_16x16x32_bf16 v[164:167], a[232:235], a[132:135], v[164:167]
ds_read_b64_tr_b16 a[154:155], v245 offset:1056
v_cvt_pk_bf16_f32 v231, v215, v231
v_cvt_pk_bf16_f32 v232, v216, v232
v_mfma_f32_16x16x32_bf16 v[168:171], v[44:47], a[132:135], v[168:171]
ds_read_b64_tr_b16 a[150:151], v238 offset:51328
v_cvt_pk_bf16_f32 v233, v217, v233
v_mfma_f32_16x16x32_bf16 v[168:171], v[32:35], a[128:131], v[168:171]
s_waitcnt lgkmcnt(7)
ds_write_b64 v244, v[230:231] offset:4360
v_mfma_f32_16x16x32_bf16 v[172:175], v[24:27], a[136:139], v[172:175]
ds_read_b64_tr_b16 a[128:129], v238 offset:50704
v_cvt_pk_bf16_f32 v234, v218, v234
v_mfma_f32_16x16x32_bf16 v[172:175], v[36:39], a[140:143], v[172:175]
ds_write_b64 v244, v[232:233] offset:4416
v_mfma_f32_16x16x32_bf16 v[176:179], v[40:43], a[140:143], v[176:179]
ds_read_b64_tr_b16 a[130:131], v238 offset:50832
v_cvt_pk_bf16_f32 v235, v219, v235
v_mfma_f32_16x16x32_bf16 v[176:179], a[228:231], a[136:139], v[176:179]
ds_write_b64 v244, v[234:235] offset:4424
v_mfma_f32_16x16x32_bf16 v[180:183], v[28:31], a[136:139], v[180:183]
ds_read_b64_tr_b16 a[132:133], v238 offset:51216
v_mfma_f32_16x16x32_bf16 v[180:183], a[232:235], a[140:143], v[180:183]
s_waitcnt lgkmcnt(7)
buffer_store_dwordx4 a[152:155], v243, s[48:51], 0 offen offset:256 sc1
v_mfma_f32_16x16x32_bf16 v[184:187], v[44:47], a[140:143], v[184:187]
ds_read_b64_tr_b16 a[134:135], v238 offset:51344
v_mfma_f32_16x16x32_bf16 v[184:187], v[32:35], a[136:139], v[184:187]
s_waitcnt lgkmcnt(7)
ds_read_b64_tr_b16 a[152:153], v245 offset:32
v_mfma_f32_16x16x32_bf16 v[156:159], v[48:51], a[144:147], v[156:159]
ds_read_b64_tr_b16 a[136:137], v238 offset:59136
v_mfma_f32_16x16x32_bf16 v[156:159], v[60:63], a[148:151], v[156:159]
)"
R"(
ds_read_b64_tr_b16 a[154:155], v245 offset:1088
v_mfma_f32_16x16x32_bf16 v[160:163], v[64:67], a[148:151], v[160:163]
ds_read_b64_tr_b16 a[138:139], v238 offset:59264
v_mfma_f32_16x16x32_bf16 v[160:163], v[52:55], a[144:147], v[160:163]
ds_read_b64_tr_b16 a[0:1], v238 offset:16960
v_mfma_f32_16x16x32_bf16 v[164:167], v[56:59], a[144:147], v[164:167]
ds_read_b64_tr_b16 a[140:141], v238 offset:59648
v_mfma_f32_16x16x32_bf16 v[164:167], v[68:71], a[148:151], v[164:167]
ds_read_b64_tr_b16 a[2:3], v238 offset:17088
v_mfma_f32_16x16x32_bf16 v[168:171], v[72:75], a[148:151], v[168:171]
ds_read_b64_tr_b16 a[142:143], v238 offset:59776
v_mfma_f32_16x16x32_bf16 v[168:171], a[236:239], a[144:147], v[168:171]
s_waitcnt lgkmcnt(8)
ds_read_b64_tr_b16 a[4:5], v238 offset:17472
v_mfma_f32_16x16x32_bf16 v[172:175], v[48:51], a[128:131], v[172:175]
ds_read_b64_tr_b16 a[144:145], v238 offset:59152
v_mfma_f32_16x16x32_bf16 v[172:175], v[60:63], a[132:135], v[172:175]
s_waitcnt lgkmcnt(7)
buffer_store_dwordx4 a[152:155], v243, s[48:51], s72 offen offset:256 sc1
v_mfma_f32_16x16x32_bf16 v[176:179], v[64:67], a[132:135], v[176:179]
ds_read_b64_tr_b16 a[146:147], v238 offset:59280
v_mfma_f32_16x16x32_bf16 v[176:179], v[52:55], a[128:131], v[176:179]
ds_read_b64_tr_b16 a[152:153], v245 offset:64
v_mfma_f32_16x16x32_bf16 v[180:183], v[56:59], a[128:131], v[180:183]
ds_read_b64_tr_b16 a[148:149], v238 offset:59664
v_mfma_f32_16x16x32_bf16 v[180:183], v[68:71], a[132:135], v[180:183]
ds_read_b64_tr_b16 a[154:155], v245 offset:1120
v_mfma_f32_16x16x32_bf16 v[184:187], v[72:75], a[132:135], v[184:187]
s_waitcnt vmcnt(5) lgkmcnt(6)
ds_read_b64_tr_b16 a[150:151], v238 offset:59792
v_mfma_f32_16x16x32_bf16 v[184:187], a[236:239], a[128:131], v[184:187]
s_barrier
v_mfma_f32_16x16x32_bf16 v[156:159], a[240:243], a[136:139], v[156:159]
ds_read_b64_tr_b16 a[128:129], v239 offset:0
v_mfma_f32_16x16x32_bf16 v[156:159], v[88:91], a[140:143], v[156:159]
ds_read_b64_tr_b16 a[6:7], v238 offset:17600
v_mfma_f32_16x16x32_bf16 v[160:163], a[244:247], a[140:143], v[160:163]
ds_read_b64_tr_b16 a[130:131], v239 offset:128
v_mfma_f32_16x16x32_bf16 v[160:163], v[76:79], a[136:139], v[160:163]
ds_read_b64_tr_b16 a[8:9], v238 offset:25408
v_mfma_f32_16x16x32_bf16 v[164:167], v[80:83], a[136:139], v[164:167]
ds_read_b64_tr_b16 a[132:133], v239 offset:512
v_mfma_f32_16x16x32_bf16 v[164:167], v[92:95], a[140:143], v[164:167]
s_waitcnt lgkmcnt(6)
buffer_store_dwordx4 a[152:155], v243, s[48:51], s73 offen offset:256 sc1
v_mfma_f32_16x16x32_bf16 v[168:171], v[96:99], a[140:143], v[168:171]
ds_read_b64_tr_b16 a[134:135], v239 offset:640
v_mfma_f32_16x16x32_bf16 v[168:171], v[84:87], a[136:139], v[168:171]
s_waitcnt lgkmcnt(6)
ds_read_b64_tr_b16 a[10:11], v238 offset:25536
v_mfma_f32_16x16x32_bf16 v[172:175], a[240:243], a[144:147], v[172:175]
ds_read_b64_tr_b16 a[136:137], v239 offset:16
v_mfma_f32_16x16x32_bf16 v[172:175], v[88:91], a[148:151], v[172:175]
ds_read_b64_tr_b16 a[152:153], v245 offset:96
v_mfma_f32_16x16x32_bf16 v[176:179], a[244:247], a[148:151], v[176:179]
ds_read_b64_tr_b16 a[138:139], v239 offset:144
v_mfma_f32_16x16x32_bf16 v[176:179], v[76:79], a[144:147], v[176:179]
ds_read_b64_tr_b16 a[154:155], v245 offset:1152
v_mfma_f32_16x16x32_bf16 v[180:183], v[80:83], a[144:147], v[180:183]
ds_read_b64_tr_b16 a[140:141], v239 offset:528
v_mfma_f32_16x16x32_bf16 v[180:183], v[92:95], a[148:151], v[180:183]
ds_read_b64_tr_b16 a[12:13], v238 offset:25920
v_mfma_f32_16x16x32_bf16 v[184:187], v[96:99], a[148:151], v[184:187]
ds_read_b64_tr_b16 a[142:143], v239 offset:656
v_mfma_f32_16x16x32_bf16 v[184:187], v[84:87], a[144:147], v[184:187]
s_waitcnt lgkmcnt(8)
ds_read_b64_tr_b16 a[14:15], v238 offset:26048
v_mfma_f32_16x16x32_bf16 v[156:159], v[100:103], a[128:131], v[156:159]
ds_read_b64_tr_b16 a[144:145], v239 offset:8448
v_mfma_f32_16x16x32_bf16 v[156:159], v[112:115], a[132:135], v[156:159]
ds_read_b64_tr_b16 a[16:17], v238 offset:33856
v_mfma_f32_16x16x32_bf16 v[160:163], v[116:119], a[132:135], v[160:163]
ds_read_b64_tr_b16 a[146:147], v239 offset:8576
v_mfma_f32_16x16x32_bf16 v[160:163], v[104:107], a[128:131], v[160:163]
s_waitcnt lgkmcnt(7)
buffer_store_dwordx4 a[152:155], v243, s[48:51], s74 offen offset:256 sc1
v_mfma_f32_16x16x32_bf16 v[164:167], a[248:251], a[128:131], v[164:167]
ds_read_b64_tr_b16 a[148:149], v239 offset:8960
v_mfma_f32_16x16x32_bf16 v[164:167], v[120:123], a[132:135], v[164:167]
ds_read_b64_tr_b16 a[18:19], v238 offset:33984
v_mfma_f32_16x16x32_bf16 v[168:171], a[252:255], a[132:135], v[168:171]
ds_read_b64_tr_b16 a[150:151], v239 offset:9088
v_mfma_f32_16x16x32_bf16 v[168:171], v[108:111], a[128:131], v[168:171]
s_waitcnt lgkmcnt(7)
ds_read_b64_tr_b16 a[20:21], v238 offset:34368
v_mfma_f32_16x16x32_bf16 v[172:175], v[100:103], a[136:139], v[172:175]
ds_read_b64_tr_b16 a[128:129], v239 offset:8464
v_mfma_f32_16x16x32_bf16 v[172:175], v[112:115], a[140:143], v[172:175]
ds_read_b64_tr_b16 a[22:23], v238 offset:34496
v_mfma_f32_16x16x32_bf16 v[176:179], v[116:119], a[140:143], v[176:179]
ds_read_b64_tr_b16 a[130:131], v239 offset:8592
v_mfma_f32_16x16x32_bf16 v[176:179], v[104:107], a[136:139], v[176:179]
ds_read_b64_tr_b16 a[152:153], v245 offset:128
v_mfma_f32_16x16x32_bf16 v[180:183], a[248:251], a[136:139], v[180:183]
ds_read_b64_tr_b16 a[132:133], v239 offset:8976
v_mfma_f32_16x16x32_bf16 v[180:183], v[120:123], a[140:143], v[180:183]
ds_read_b64_tr_b16 a[154:155], v245 offset:1184
v_mfma_f32_16x16x32_bf16 v[184:187], a[252:255], a[140:143], v[184:187]
ds_read_b64_tr_b16 a[134:135], v239 offset:9104
v_mfma_f32_16x16x32_bf16 v[184:187], v[108:111], a[136:139], v[184:187]
s_waitcnt lgkmcnt(8)
ds_read_b64_tr_b16 a[24:25], v238 offset:42304
v_mfma_f32_16x16x32_bf16 v[156:159], v[124:127], a[144:147], v[156:159]
.Lpad13_%=:
s_cbranch_vccnz .Lret3_%=
ds_read_b64_tr_b16 a[26:27], v238 offset:42432
v_mfma_f32_16x16x32_bf16 v[156:159], v[140:143], a[148:151], v[156:159]
ds_read_b64_tr_b16 a[28:29], v238 offset:42816
v_mfma_f32_16x16x32_bf16 v[160:163], v[144:147], a[148:151], v[160:163]
ds_read_b64_tr_b16 a[30:31], v238 offset:42944
v_mfma_f32_16x16x32_bf16 v[160:163], v[128:131], a[144:147], v[160:163]
ds_read_b64_tr_b16 a[32:33], v238 offset:50752
v_mfma_f32_16x16x32_bf16 v[164:167], v[132:135], a[144:147], v[164:167]
ds_read_b64_tr_b16 a[34:35], v238 offset:50880
v_mfma_f32_16x16x32_bf16 v[164:167], v[148:151], a[148:151], v[164:167]
s_waitcnt lgkmcnt(7)
buffer_store_dwordx4 a[152:155], v243, s[48:51], s75 offen offset:256 sc1
v_mul_f32_dpp v156, v250, v156 row_newbcast:0 row_mask:0xf bank_mask:0xf
v_mul_f32_dpp v157, v250, v157 row_newbcast:1 row_mask:0xf bank_mask:0xf
v_mul_f32_dpp v158, v250, v158 row_newbcast:2 row_mask:0xf bank_mask:0xf
v_mfma_f32_16x16x32_bf16 v[168:171], v[152:155], a[148:151], v[168:171]
ds_read_b64_tr_b16 a[36:37], v238 offset:51264
v_mul_f32_dpp v159, v250, v159 row_newbcast:3 row_mask:0xf bank_mask:0xf
v_mfma_f32_16x16x32_bf16 v[168:171], v[136:139], a[144:147], v[168:171]
s_waitcnt lgkmcnt(7)
ds_read_b64_tr_b16 a[38:39], v238 offset:51392
v_mul_f32_dpp v160, v251, v160 row_newbcast:0 row_mask:0xf bank_mask:0xf
v_mfma_f32_16x16x32_bf16 v[172:175], v[124:127], a[128:131], v[172:175]
ds_read_b64_tr_b16 a[40:41], v238 offset:59200
v_mul_f32_dpp v161, v251, v161 row_newbcast:1 row_mask:0xf bank_mask:0xf
v_mfma_f32_16x16x32_bf16 v[172:175], v[140:143], a[132:135], v[172:175]
ds_read_b64_tr_b16 a[42:43], v238 offset:59328
v_mul_f32_dpp v162, v251, v162 row_newbcast:2 row_mask:0xf bank_mask:0xf
v_mfma_f32_16x16x32_bf16 v[176:179], v[144:147], a[132:135], v[176:179]
ds_read_b64_tr_b16 a[44:45], v238 offset:59712
v_mul_f32_dpp v163, v251, v163 row_newbcast:3 row_mask:0xf bank_mask:0xf
v_mfma_f32_16x16x32_bf16 v[176:179], v[128:131], a[128:131], v[176:179]
ds_read_b64_tr_b16 a[152:153], v245 offset:160
v_mul_f32_dpp v164, v252, v164 row_newbcast:0 row_mask:0xf bank_mask:0xf
v_mfma_f32_16x16x32_bf16 v[180:183], v[132:135], a[128:131], v[180:183]
ds_read_b64_tr_b16 a[154:155], v245 offset:1216
v_mul_f32_dpp v165, v252, v165 row_newbcast:1 row_mask:0xf bank_mask:0xf
v_mfma_f32_16x16x32_bf16 v[180:183], v[148:151], a[132:135], v[180:183]
ds_read_b64_tr_b16 a[46:47], v238 offset:59840
v_mul_f32_dpp v166, v252, v166 row_newbcast:2 row_mask:0xf bank_mask:0xf
v_mfma_f32_16x16x32_bf16 v[184:187], v[152:155], a[132:135], v[184:187]
ds_read_b64_tr_b16 a[48:49], v239 offset:64
v_mul_f32_dpp v167, v252, v167 row_newbcast:3 row_mask:0xf bank_mask:0xf
v_mfma_f32_16x16x32_bf16 v[184:187], v[136:139], a[128:131], v[184:187]
ds_read_b64_tr_b16 a[50:51], v239 offset:192
v_mul_f32_dpp v168, v253, v168 row_newbcast:0 row_mask:0xf bank_mask:0xf
v_mfma_f32_16x16x32_bf16 v[188:191], a[156:159], a[0:3], 0
ds_read_b64_tr_b16 a[52:53], v239 offset:576
v_mul_f32_dpp v169, v253, v169 row_newbcast:1 row_mask:0xf bank_mask:0xf
v_mfma_f32_16x16x32_bf16 v[188:191], a[172:175], a[4:7], v[188:191]
ds_read_b64_tr_b16 a[54:55], v239 offset:704
v_mul_f32_dpp v170, v253, v170 row_newbcast:2 row_mask:0xf bank_mask:0xf
v_mfma_f32_16x16x32_bf16 v[188:191], a[188:191], a[8:11], v[188:191]
ds_read_b64_tr_b16 a[56:57], v239 offset:8512
v_mul_f32_dpp v171, v253, v171 row_newbcast:3 row_mask:0xf bank_mask:0xf
v_mfma_f32_16x16x32_bf16 v[188:191], a[204:207], a[12:15], v[188:191]
ds_read_b64_tr_b16 a[58:59], v239 offset:8640
v_mul_f32_dpp v172, v250, v172 row_newbcast:0 row_mask:0xf bank_mask:0xf
v_mfma_f32_16x16x32_bf16 v[192:195], a[160:163], a[0:3], 0
s_waitcnt lgkmcnt(7)
buffer_store_dwordx4 a[152:155], v243, s[48:51], s76 offen offset:256 sc1
v_mul_f32_dpp v173, v250, v173 row_newbcast:1 row_mask:0xf bank_mask:0xf
v_mul_f32_dpp v174, v250, v174 row_newbcast:2 row_mask:0xf bank_mask:0xf
v_mul_f32_dpp v175, v250, v175 row_newbcast:3 row_mask:0xf bank_mask:0xf
v_mfma_f32_16x16x32_bf16 v[192:195], a[176:179], a[4:7], v[192:195]
ds_read_b64_tr_b16 a[60:61], v239 offset:9024
v_mul_f32_dpp v176, v251, v176 row_newbcast:0 row_mask:0xf bank_mask:0xf
v_mfma_f32_16x16x32_bf16 v[192:195], a[192:195], a[8:11], v[192:195]
ds_read_b64_tr_b16 a[62:63], v239 offset:9152
v_mul_f32_dpp v177, v251, v177 row_newbcast:1 row_mask:0xf bank_mask:0xf
v_mfma_f32_16x16x32_bf16 v[192:195], a[208:211], a[12:15], v[192:195]
ds_read_b64_tr_b16 a[64:65], v238 offset:16976
v_mul_f32_dpp v178, v251, v178 row_newbcast:2 row_mask:0xf bank_mask:0xf
v_mfma_f32_16x16x32_bf16 v[188:191], a[220:223], a[16:19], v[188:191]
ds_read_b64_tr_b16 a[66:67], v238 offset:17104
v_mul_f32_dpp v179, v251, v179 row_newbcast:3 row_mask:0xf bank_mask:0xf
v_mfma_f32_16x16x32_bf16 v[188:191], a[224:227], a[20:23], v[188:191]
ds_read_b64_tr_b16 a[68:69], v238 offset:17488
v_mul_f32_dpp v180, v252, v180 row_newbcast:0 row_mask:0xf bank_mask:0xf
v_mfma_f32_16x16x32_bf16 v[188:191], v[24:27], a[24:27], v[188:191]
ds_read_b64_tr_b16 a[70:71], v238 offset:17616
v_mul_f32_dpp v181, v252, v181 row_newbcast:1 row_mask:0xf bank_mask:0xf
v_mfma_f32_16x16x32_bf16 v[188:191], v[36:39], a[28:31], v[188:191]
ds_read_b64_tr_b16 a[72:73], v238 offset:25424
v_mul_f32_dpp v182, v252, v182 row_newbcast:2 row_mask:0xf bank_mask:0xf
v_mfma_f32_16x16x32_bf16 v[192:195], v[0:3], a[16:19], v[192:195]
ds_read_b64_tr_b16 a[152:153], v245 offset:192
v_mul_f32_dpp v183, v252, v183 row_newbcast:3 row_mask:0xf bank_mask:0xf
v_mfma_f32_16x16x32_bf16 v[192:195], v[12:15], a[20:23], v[192:195]
ds_read_b64_tr_b16 a[154:155], v245 offset:1248
v_mul_f32_dpp v184, v253, v184 row_newbcast:0 row_mask:0xf bank_mask:0xf
v_mfma_f32_16x16x32_bf16 v[192:195], a[228:231], a[24:27], v[192:195]
ds_read_b64_tr_b16 a[74:75], v238 offset:25552
v_mul_f32_dpp v185, v253, v185 row_newbcast:1 row_mask:0xf bank_mask:0xf
v_mfma_f32_16x16x32_bf16 v[192:195], v[40:43], a[28:31], v[192:195]
s_waitcnt lgkmcnt(14)
ds_read_b64_tr_b16 a[76:77], v238 offset:25936
v_mul_f32_dpp v186, v253, v186 row_newbcast:2 row_mask:0xf bank_mask:0xf
v_mfma_f32_16x16x32_bf16 v[188:191], v[48:51], a[32:35], v[188:191]
ds_read_b64_tr_b16 a[78:79], v238 offset:26064
)"
R"(
v_mul_f32_dpp v187, v253, v187 row_newbcast:3 row_mask:0xf bank_mask:0xf
v_mfma_f32_16x16x32_bf16 v[188:191], v[60:63], a[36:39], v[188:191]
ds_read_b64_tr_b16 a[80:81], v238 offset:33872
v_cvt_pk_bf16_f32 v172, v156, v172
v_mfma_f32_16x16x32_bf16 v[188:191], a[240:243], a[40:43], v[188:191]
ds_read_b64_tr_b16 a[82:83], v238 offset:34000
v_cvt_pk_bf16_f32 v173, v157, v173
v_mfma_f32_16x16x32_bf16 v[188:191], v[88:91], a[44:47], v[188:191]
ds_read_b64_tr_b16 a[84:85], v238 offset:34384
v_cvt_pk_bf16_f32 v174, v158, v174
v_mfma_f32_16x16x32_bf16 v[192:195], v[52:55], a[32:35], v[192:195]
ds_read_b64_tr_b16 a[86:87], v238 offset:34512
v_cvt_pk_bf16_f32 v175, v159, v175
v_mfma_f32_16x16x32_bf16 v[192:195], v[64:67], a[36:39], v[192:195]
s_waitcnt lgkmcnt(7)
buffer_store_dwordx4 a[152:155], v243, s[48:51], s77 offen offset:256 sc1
v_cvt_pk_bf16_f32 v176, v160, v176
v_cvt_pk_bf16_f32 v177, v161, v177
v_cvt_pk_bf16_f32 v178, v162, v178
v_mfma_f32_16x16x32_bf16 v[192:195], v[76:79], a[40:43], v[192:195]
ds_read_b64_tr_b16 a[88:89], v238 offset:42320
v_cvt_pk_bf16_f32 v179, v163, v179
v_mfma_f32_16x16x32_bf16 v[192:195], a[244:247], a[44:47], v[192:195]
ds_read_b64_tr_b16 a[90:91], v238 offset:42448
v_cvt_pk_bf16_f32 v180, v164, v180
v_mfma_f32_16x16x32_bf16 v[188:191], v[100:103], a[48:51], v[188:191]
ds_read_b64_tr_b16 a[92:93], v238 offset:42832
v_cvt_pk_bf16_f32 v181, v165, v181
v_mfma_f32_16x16x32_bf16 v[188:191], v[112:115], a[52:55], v[188:191]
ds_read_b64_tr_b16 a[94:95], v238 offset:42960
v_cvt_pk_bf16_f32 v182, v166, v182
v_mfma_f32_16x16x32_bf16 v[188:191], v[124:127], a[56:59], v[188:191]
ds_read_b64_tr_b16 a[96:97], v238 offset:50768
v_cvt_pk_bf16_f32 v183, v167, v183
v_mfma_f32_16x16x32_bf16 v[188:191], v[140:143], a[60:63], v[188:191]
ds_read_b64_tr_b16 a[98:99], v238 offset:50896
v_cvt_pk_bf16_f32 v184, v168, v184
v_mfma_f32_16x16x32_bf16 v[192:195], v[104:107], a[48:51], v[192:195]
ds_read_b64_tr_b16 a[100:101], v238 offset:51280
v_cvt_pk_bf16_f32 v185, v169, v185
v_mfma_f32_16x16x32_bf16 v[192:195], v[116:119], a[52:55], v[192:195]
ds_read_b64_tr_b16 a[102:103], v238 offset:51408
v_cvt_pk_bf16_f32 v186, v170, v186
v_mfma_f32_16x16x32_bf16 v[192:195], v[128:131], a[56:59], v[192:195]
ds_read_b64_tr_b16 a[104:105], v238 offset:59216
ds_read_b64_tr_b16 a[110:111], v238 offset:59856
ds_read_b64_tr_b16 a[108:109], v238 offset:59728
ds_read_b64_tr_b16 a[106:107], v238 offset:59344
v_mul_f32_dpp v188, v250, v188 row_newbcast:0 row_mask:0xf bank_mask:0xf
v_mfma_f32_16x16x32_bf16 v[192:195], v[144:147], a[60:63], v[192:195]
s_waitcnt lgkmcnt(14)
ds_read_b64_tr_b16 a[152:153], v245 offset:224
v_mul_f32_dpp v189, v250, v189 row_newbcast:1 row_mask:0xf bank_mask:0xf
v_mfma_f32_16x16x32_bf16 v[204:207], a[156:159], a[64:67], 0
ds_read_b64_tr_b16 a[154:155], v245 offset:1280
v_mul_f32_dpp v190, v250, v190 row_newbcast:2 row_mask:0xf bank_mask:0xf
v_mfma_f32_16x16x32_bf16 v[204:207], a[172:175], a[68:71], v[204:207]
ds_read_b64_tr_b16 a[112:113], v239 offset:80
v_mul_f32_dpp v191, v250, v191 row_newbcast:3 row_mask:0xf bank_mask:0xf
v_mfma_f32_16x16x32_bf16 v[204:207], a[188:191], a[72:75], v[204:207]
ds_read_b64_tr_b16 a[114:115], v239 offset:208
ds_read_b64_tr_b16 a[126:127], v239 offset:9168
ds_read_b64_tr_b16 a[124:125], v239 offset:9040
ds_read_b64_tr_b16 a[122:123], v239 offset:8656
ds_read_b64_tr_b16 a[120:121], v239 offset:8528
ds_read_b64_tr_b16 a[118:119], v239 offset:720
ds_read_b64_tr_b16 a[116:117], v239 offset:592
v_mul_f32_dpp v192, v251, v192 row_newbcast:0 row_mask:0xf bank_mask:0xf
v_mfma_f32_16x16x32_bf16 v[204:207], a[204:207], a[76:79], v[204:207]
s_waitcnt lgkmcnt(14)
ds_write_b64 v244, v[172:173] offset:36864
v_mfma_f32_16x16x32_bf16 v[204:207], a[220:223], a[80:83], v[204:207]
ds_write_b64 v244, v[174:175] offset:36872
v_mfma_f32_16x16x32_bf16 v[204:207], a[224:227], a[84:87], v[204:207]
ds_write_b64 v244, v[176:177] offset:36928
v_mfma_f32_16x16x32_bf16 v[204:207], v[24:27], a[88:91], v[204:207]
ds_write_b64 v244, v[178:179] offset:36936
v_mfma_f32_16x16x32_bf16 v[204:207], v[36:39], a[92:95], v[204:207]
s_waitcnt lgkmcnt(14)
v_mul_f32_dpp v193, v251, v193 row_newbcast:1 row_mask:0xf bank_mask:0xf
v_mul_f32_dpp v194, v251, v194 row_newbcast:2 row_mask:0xf bank_mask:0xf
v_mfma_f32_16x16x32_bf16 v[204:207], v[48:51], a[96:99], v[204:207]
s_waitcnt lgkmcnt(12)
buffer_store_dwordx4 a[152:155], v243, s[48:51], s78 offen offset:256 sc1
v_mul_f32_dpp v195, v251, v195 row_newbcast:3 row_mask:0xf bank_mask:0xf
v_cvt_pk_bf16_f32 v187, v171, v187
v_mfma_f32_16x16x32_bf16 v[204:207], v[60:63], a[100:103], v[204:207]
ds_write_b64 v244, v[180:181] offset:36992
v_mfma_f32_16x16x32_bf16 v[204:207], a[240:243], a[104:107], v[204:207]
ds_write_b64 v244, v[182:183] offset:37000
v_mfma_f32_16x16x32_bf16 v[204:207], v[88:91], a[108:111], v[204:207]
s_waitcnt lgkmcnt(6)
ds_write_b64 v244, v[184:185] offset:37056
v_mfma_f32_16x16x32_bf16 v[204:207], v[100:103], a[112:115], v[204:207]
ds_write_b64 v244, v[186:187] offset:37064
v_mfma_f32_16x16x32_bf16 v[204:207], v[112:115], a[116:119], v[204:207]
v_mfma_f32_16x16x32_bf16 v[204:207], v[124:127], a[120:123], v[204:207]
v_mfma_f32_16x16x32_bf16 v[204:207], v[140:143], a[124:127], v[204:207]
v_mfma_f32_16x16x32_bf16 v[208:211], a[160:163], a[64:67], 0
v_mfma_f32_16x16x32_bf16 v[208:211], a[176:179], a[68:71], v[208:211]
v_mfma_f32_16x16x32_bf16 v[208:211], a[192:195], a[72:75], v[208:211]
v_mfma_f32_16x16x32_bf16 v[208:211], a[208:211], a[76:79], v[208:211]
v_mfma_f32_16x16x32_bf16 v[208:211], v[0:3], a[80:83], v[208:211]
v_mfma_f32_16x16x32_bf16 v[208:211], v[12:15], a[84:87], v[208:211]
v_mfma_f32_16x16x32_bf16 v[208:211], a[228:231], a[88:91], v[208:211]
v_mfma_f32_16x16x32_bf16 v[208:211], v[40:43], a[92:95], v[208:211]
v_mul_f32_dpp v204, v250, v204 row_newbcast:0 row_mask:0xf bank_mask:0xf
v_mul_f32_dpp v205, v250, v205 row_newbcast:1 row_mask:0xf bank_mask:0xf
v_mfma_f32_16x16x32_bf16 v[208:211], v[52:55], a[96:99], v[208:211]
v_mul_f32_dpp v206, v250, v206 row_newbcast:2 row_mask:0xf bank_mask:0xf
v_mul_f32_dpp v207, v250, v207 row_newbcast:3 row_mask:0xf bank_mask:0xf
v_mfma_f32_16x16x32_bf16 v[208:211], v[64:67], a[100:103], v[208:211]
v_cvt_pk_bf16_f32 v204, v188, v204
v_cvt_pk_bf16_f32 v205, v189, v205
v_mfma_f32_16x16x32_bf16 v[208:211], v[76:79], a[104:107], v[208:211]
ds_write_b64 v244, v[204:205] offset:41088
v_mfma_f32_16x16x32_bf16 v[208:211], a[244:247], a[108:111], v[208:211]
v_cvt_pk_bf16_f32 v206, v190, v206
v_cvt_pk_bf16_f32 v207, v191, v207
v_mfma_f32_16x16x32_bf16 v[208:211], v[104:107], a[112:115], v[208:211]
ds_write_b64 v244, v[206:207] offset:41096
v_mfma_f32_16x16x32_bf16 v[208:211], v[116:119], a[116:119], v[208:211]
ds_read_b64_tr_b16 a[128:129], v245 offset:36864
v_mfma_f32_16x16x32_bf16 v[208:211], v[128:131], a[120:123], v[208:211]
ds_read_b64_tr_b16 a[132:133], v245 offset:36896
v_mfma_f32_16x16x32_bf16 v[208:211], v[144:147], a[124:127], v[208:211]
ds_read_b64_tr_b16 a[130:131], v245 offset:37920
v_mfma_f32_16x16x32_bf16 v[196:199], a[164:167], a[0:3], 0
ds_read_b64_tr_b16 a[134:135], v245 offset:37952
v_mfma_f32_16x16x32_bf16 v[196:199], a[180:183], a[4:7], v[196:199]
v_mfma_f32_16x16x32_bf16 v[196:199], a[196:199], a[8:11], v[196:199]
v_mfma_f32_16x16x32_bf16 v[196:199], a[212:215], a[12:15], v[196:199]
v_mfma_f32_16x16x32_bf16 v[196:199], v[4:7], a[16:19], v[196:199]
v_mfma_f32_16x16x32_bf16 v[196:199], v[16:19], a[20:23], v[196:199]
v_mul_f32_dpp v208, v251, v208 row_newbcast:0 row_mask:0xf bank_mask:0xf
v_mul_f32_dpp v209, v251, v209 row_newbcast:1 row_mask:0xf bank_mask:0xf
v_mfma_f32_16x16x32_bf16 v[196:199], v[28:31], a[24:27], v[196:199]
v_mul_f32_dpp v210, v251, v210 row_newbcast:2 row_mask:0xf bank_mask:0xf
v_mul_f32_dpp v211, v251, v211 row_newbcast:3 row_mask:0xf bank_mask:0xf
v_mfma_f32_16x16x32_bf16 v[196:199], a[232:235], a[28:31], v[196:199]
s_waitcnt lgkmcnt(1)
buffer_store_dwordx4 a[128:131], v243, s[48:51], 0 offen offset:384 sc1
v_cvt_pk_bf16_f32 v208, v192, v208
v_cvt_pk_bf16_f32 v209, v193, v209
v_cvt_pk_bf16_f32 v210, v194, v210
v_mfma_f32_16x16x32_bf16 v[196:199], v[56:59], a[32:35], v[196:199]
ds_write_b64 v244, v[208:209] offset:41152
v_mfma_f32_16x16x32_bf16 v[196:199], v[68:71], a[36:39], v[196:199]
v_cvt_pk_bf16_f32 v211, v195, v211
v_mfma_f32_16x16x32_bf16 v[196:199], v[80:83], a[40:43], v[196:199]
ds_write_b64 v244, v[210:211] offset:41160
v_mfma_f32_16x16x32_bf16 v[196:199], v[92:95], a[44:47], v[196:199]
ds_read_b64_tr_b16 a[136:137], v245 offset:36928
v_mfma_f32_16x16x32_bf16 v[196:199], a[248:251], a[48:51], v[196:199]
ds_read_b64_tr_b16 a[140:141], v245 offset:36960
v_mfma_f32_16x16x32_bf16 v[196:199], v[120:123], a[52:55], v[196:199]
ds_read_b64_tr_b16 a[138:139], v245 offset:37984
v_mfma_f32_16x16x32_bf16 v[196:199], v[132:135], a[56:59], v[196:199]
ds_read_b64_tr_b16 a[142:143], v245 offset:38016
v_mfma_f32_16x16x32_bf16 v[196:199], v[148:151], a[60:63], v[196:199]
v_mfma_f32_16x16x32_bf16 v[212:215], a[164:167], a[64:67], 0
v_mfma_f32_16x16x32_bf16 v[212:215], a[180:183], a[68:71], v[212:215]
s_waitcnt lgkmcnt(6)
buffer_store_dwordx4 a[132:135], v243, s[48:51], s72 offen offset:384 sc1
v_mfma_f32_16x16x32_bf16 v[212:215], a[196:199], a[72:75], v[212:215]
v_mfma_f32_16x16x32_bf16 v[212:215], a[212:215], a[76:79], v[212:215]
v_mfma_f32_16x16x32_bf16 v[212:215], v[4:7], a[80:83], v[212:215]
v_mfma_f32_16x16x32_bf16 v[212:215], v[16:19], a[84:87], v[212:215]
v_mfma_f32_16x16x32_bf16 v[212:215], v[28:31], a[88:91], v[212:215]
v_mul_f32_dpp v196, v252, v196 row_newbcast:0 row_mask:0xf bank_mask:0xf
v_mul_f32_dpp v197, v252, v197 row_newbcast:1 row_mask:0xf bank_mask:0xf
v_mfma_f32_16x16x32_bf16 v[212:215], a[232:235], a[92:95], v[212:215]
v_mul_f32_dpp v198, v252, v198 row_newbcast:2 row_mask:0xf bank_mask:0xf
v_mul_f32_dpp v199, v252, v199 row_newbcast:3 row_mask:0xf bank_mask:0xf
v_mfma_f32_16x16x32_bf16 v[212:215], v[56:59], a[96:99], v[212:215]
v_mfma_f32_16x16x32_bf16 v[212:215], v[68:71], a[100:103], v[212:215]
v_mfma_f32_16x16x32_bf16 v[212:215], v[80:83], a[104:107], v[212:215]
v_mfma_f32_16x16x32_bf16 v[212:215], v[92:95], a[108:111], v[212:215]
s_waitcnt lgkmcnt(1)
buffer_store_dwordx4 a[136:139], v243, s[48:51], s73 offen offset:384 sc1
v_mfma_f32_16x16x32_bf16 v[212:215], a[248:251], a[112:115], v[212:215]
v_mfma_f32_16x16x32_bf16 v[212:215], v[120:123], a[116:119], v[212:215]
v_mfma_f32_16x16x32_bf16 v[212:215], v[132:135], a[120:123], v[212:215]
v_mfma_f32_16x16x32_bf16 v[212:215], v[148:151], a[124:127], v[212:215]
v_mfma_f32_16x16x32_bf16 v[200:203], a[168:171], a[0:3], 0
v_mfma_f32_16x16x32_bf16 v[200:203], a[184:187], a[4:7], v[200:203]
v_mfma_f32_16x16x32_bf16 v[200:203], a[200:203], a[8:11], v[200:203]
v_mfma_f32_16x16x32_bf16 v[200:203], a[216:219], a[12:15], v[200:203]
v_mfma_f32_16x16x32_bf16 v[200:203], v[8:11], a[16:19], v[200:203]
v_mfma_f32_16x16x32_bf16 v[200:203], v[20:23], a[20:23], v[200:203]
s_waitcnt lgkmcnt(0)
buffer_store_dwordx4 a[140:143], v243, s[48:51], s74 offen offset:384 sc1
v_mfma_f32_16x16x32_bf16 v[200:203], v[32:35], a[24:27], v[200:203]
v_mul_f32_dpp v212, v252, v212 row_newbcast:0 row_mask:0xf bank_mask:0xf
v_mul_f32_dpp v213, v252, v213 row_newbcast:1 row_mask:0xf bank_mask:0xf
v_mfma_f32_16x16x32_bf16 v[200:203], v[44:47], a[28:31], v[200:203]
v_mul_f32_dpp v214, v252, v214 row_newbcast:2 row_mask:0xf bank_mask:0xf
v_mul_f32_dpp v215, v252, v215 row_newbcast:3 row_mask:0xf bank_mask:0xf
v_mfma_f32_16x16x32_bf16 v[200:203], a[236:239], a[32:35], v[200:203]
v_cvt_pk_bf16_f32 v212, v196, v212
v_cvt_pk_bf16_f32 v213, v197, v213
v_mfma_f32_16x16x32_bf16 v[200:203], v[72:75], a[36:39], v[200:203]
ds_write_b64 v244, v[212:213] offset:41216
v_mfma_f32_16x16x32_bf16 v[200:203], v[84:87], a[40:43], v[200:203]
v_cvt_pk_bf16_f32 v214, v198, v214
v_cvt_pk_bf16_f32 v215, v199, v215
v_mfma_f32_16x16x32_bf16 v[200:203], v[96:99], a[44:47], v[200:203]
ds_write_b64 v244, v[214:215] offset:41224
v_mfma_f32_16x16x32_bf16 v[200:203], v[108:111], a[48:51], v[200:203]
)"
R"(
ds_read_b64_tr_b16 a[144:145], v245 offset:36992
v_mfma_f32_16x16x32_bf16 v[200:203], a[252:255], a[52:55], v[200:203]
ds_read_b64_tr_b16 a[148:149], v245 offset:37024
v_mfma_f32_16x16x32_bf16 v[200:203], v[136:139], a[56:59], v[200:203]
ds_read_b64_tr_b16 a[146:147], v245 offset:38048
v_mfma_f32_16x16x32_bf16 v[200:203], v[152:155], a[60:63], v[200:203]
ds_read_b64_tr_b16 a[150:151], v245 offset:38080
v_mfma_f32_16x16x32_bf16 v[216:219], a[168:171], a[64:67], 0
v_mfma_f32_16x16x32_bf16 v[216:219], a[184:187], a[68:71], v[216:219]
v_mfma_f32_16x16x32_bf16 v[216:219], a[200:203], a[72:75], v[216:219]
v_mfma_f32_16x16x32_bf16 v[216:219], a[216:219], a[76:79], v[216:219]
v_mfma_f32_16x16x32_bf16 v[216:219], v[8:11], a[80:83], v[216:219]
v_mfma_f32_16x16x32_bf16 v[216:219], v[20:23], a[84:87], v[216:219]
v_mfma_f32_16x16x32_bf16 v[216:219], v[32:35], a[88:91], v[216:219]
s_waitcnt lgkmcnt(1)
buffer_store_dwordx4 a[144:147], v243, s[48:51], s75 offen offset:384 sc1
v_mul_f32_dpp v200, v253, v200 row_newbcast:0 row_mask:0xf bank_mask:0xf
v_mul_f32_dpp v201, v253, v201 row_newbcast:1 row_mask:0xf bank_mask:0xf
v_mul_f32_dpp v202, v253, v202 row_newbcast:2 row_mask:0xf bank_mask:0xf
v_mfma_f32_16x16x32_bf16 v[216:219], v[44:47], a[92:95], v[216:219]
v_mul_f32_dpp v203, v253, v203 row_newbcast:3 row_mask:0xf bank_mask:0xf
v_mfma_f32_16x16x32_bf16 v[216:219], a[236:239], a[96:99], v[216:219]
v_mfma_f32_16x16x32_bf16 v[216:219], v[72:75], a[100:103], v[216:219]
v_mfma_f32_16x16x32_bf16 v[216:219], v[84:87], a[104:107], v[216:219]
v_mfma_f32_16x16x32_bf16 v[216:219], v[96:99], a[108:111], v[216:219]
v_mfma_f32_16x16x32_bf16 v[216:219], v[108:111], a[112:115], v[216:219]
v_mfma_f32_16x16x32_bf16 v[216:219], a[252:255], a[116:119], v[216:219]
v_mfma_f32_16x16x32_bf16 v[216:219], v[136:139], a[120:123], v[216:219]
v_mfma_f32_16x16x32_bf16 v[216:219], v[152:155], a[124:127], v[216:219]
s_waitcnt lgkmcnt(0)
buffer_store_dwordx4 a[148:151], v243, s[48:51], s76 offen offset:384 sc1
s_nop 5
v_mul_f32_dpp v216, v253, v216 row_newbcast:0 row_mask:0xf bank_mask:0xf
v_mul_f32_dpp v217, v253, v217 row_newbcast:1 row_mask:0xf bank_mask:0xf
v_mul_f32_dpp v218, v253, v218 row_newbcast:2 row_mask:0xf bank_mask:0xf
v_mul_f32_dpp v219, v253, v219 row_newbcast:3 row_mask:0xf bank_mask:0xf
v_cvt_pk_bf16_f32 v216, v200, v216
v_cvt_pk_bf16_f32 v217, v201, v217
ds_write_b64 v244, v[216:217] offset:41280
v_cvt_pk_bf16_f32 v218, v202, v218
v_cvt_pk_bf16_f32 v219, v203, v219
ds_write_b64 v244, v[218:219] offset:41288
ds_read_b64_tr_b16 a[128:129], v245 offset:37056
ds_read_b64_tr_b16 a[132:133], v245 offset:37088
ds_read_b64_tr_b16 a[130:131], v245 offset:38112
ds_read_b64_tr_b16 a[134:135], v245 offset:38144
s_waitcnt lgkmcnt(1)
buffer_store_dwordx4 a[128:131], v243, s[48:51], s77 offen offset:384 sc1
s_waitcnt lgkmcnt(0)
buffer_store_dwordx4 a[132:135], v243, s[48:51], s78 offen offset:384 sc1
s_cmp_lg_u64 s[64:65], 0
s_cselect_b32 %[fl], 2, 0
s_or_b32 %[fl], %[fl], s70
s_cmp_lg_u32 %[fl], 0
s_cbranch_scc1 .Lend%=
s_endpgm
.Lend%=:
)"
    : [fl] "=&s"(fl)
    : [ka] "s"(__builtin_amdgcn_kernarg_segment_ptr()), [w] "s"(w), [b] "s"(b)
    : "v0","v1","v2","v3","v4","v5","v6","v7","v8","v9","v10","v11","v12","v13","v14","v15","v16","v17","v18","v19","v20","v21","v22","v23","v24","v25","v26","v27","v28","v29","v30","v31","v32","v33","v34","v35","v36","v37","v38","v39","v40","v41","v42","v43","v44","v45","v46","v47","v48","v49","v50","v51","v52","v53","v54","v55","v56","v57","v58","v59","v60","v61","v62","v63","v64","v65","v66","v67","v68","v69","v70","v71","v72","v73","v74","v75","v76","v77","v78","v79","v80","v81","v82","v83","v84","v85","v86","v87","v88","v89","v90","v91","v92","v93","v94","v95","v96","v97","v98","v99","v100","v101","v102","v103","v104","v105","v106","v107","v108","v109","v110","v111","v112","v113","v114","v115","v116","v117","v118","v119","v120","v121","v122","v123","v124","v125","v126","v127","v128","v129","v130","v131","v132","v133","v134","v135","v136","v137","v138","v139","v140","v141","v142","v143","v144","v145","v146","v147","v148","v149","v150","v151","v152","v153","v154","v155","v156","v157","v158","v159","v160","v161","v162","v163","v164","v165","v166","v167","v168","v169","v170","v171","v172","v173","v174","v175","v176","v177","v178","v179","v180","v181","v182","v183","v184","v185","v186","v187","v188","v189","v190","v191","v192","v193","v194","v195","v196","v197","v198","v199","v200","v201","v202","v203","v204","v205","v206","v207","v208","v209","v210","v211","v212","v213","v214","v215","v216","v217","v218","v219","v220","v221","v222","v223","v224","v225","v226","v227","v228","v229","v230","v231","v232","v233","v234","v235","v236","v237","v238","v239","v240","v241","v242","v243","v244","v245","v246","v247","v248","v249","v250","v251","v252","v253","v254","v255","a0","a1","a2","a3","a4","a5","a6","a7","a8","a9","a10","a11","a12","a13","a14","a15","a16","a17","a18","a19","a20","a21","a22","a23","a24","a25","a26","a27","a28","a29","a30","a31","a32","a33","a34","a35","a36","a37","a38","a39","a40","a41","a42","a43","a44","a45","a46","a47","a48","a49","a50","a51","a52","a53","a54","a55","a56","a57","a58","a59","a60","a61","a62","a63","a64","a65","a66","a67","a68","a69","a70","a71","a72","a73","a74","a75","a76","a77","a78","a79","a80","a81","a82","a83","a84","a85","a86","a87","a88","a89","a90","a91","a92","a93","a94","a95","a96","a97","a98","a99","a100","a101","a102","a103","a104","a105","a106","a107","a108","a109","a110","a111","a112","a113","a114","a115","a116","a117","a118","a119","a120","a121","a122","a123","a124","a125","a126","a127","a128","a129","a130","a131","a132","a133","a134","a135","a136","a137","a138","a139","a140","a141","a142","a143","a144","a145","a146","a147","a148","a149","a150","a151","a152","a153","a154","a155","a156","a157","a158","a159","a160","a161","a162","a163","a164","a165","a166","a167","a168","a169","a170","a171","a172","a173","a174","a175","a176","a177","a178","a179","a180","a181","a182","a183","a184","a185","a186","a187","a188","a189","a190","a191","a192","a193","a194","a195","a196","a197","a198","a199","a200","a201","a202","a203","a204","a205","a206","a207","a208","a209","a210","a211","a212","a213","a214","a215","a216","a217","a218","a219","a220","a221","a222","a223","a224","a225","a226","a227","a228","a229","a230","a231","a232","a233","a234","a235","a236","a237","a238","a239","a240","a241","a242","a243","a244","a245","a246","a247","a248","a249","a250","a251","a252","a253","a254","a255","s20","s21","s22","s23","s24","s25","s26","s27","s28","s29","s30","s31","s34","s35","s36","s37","s38","s39","s40","s41","s42","s43","s44","s45","s46","s47","s48","s49","s50","s51","s52","s53","s54","s55","s56","s57","s58","s59","s60","s61","s62","s63","s64","s65","s66","s67","s68","s69","s70","s71","s72","s73","s74","s75","s76","s77","s78","s79","s80","s81","s82","s83","s84","s85","s86","s87","s88","s89","s90","s91","s92","s93","s94","vcc","scc","memory");
  if (fl) {
    asm volatile("s_waitcnt vmcnt(0) lgkmcnt(0)" ::: "memory");
    const unsigned j = b >> 3;
    const size_t hoff = (size_t)((b & 7u) * 16u + (j >> 1)) * 262144u, qoff = hoff + (j & 1u) * 131072u;
    const unsigned lane = __builtin_amdgcn_mbcnt_hi(-1, __builtin_amdgcn_mbcnt_lo(-1, 0));
    for (unsigned it = 0; it < 2; ++it)
      if ((fl >> it) & 1u) {
        const size_t x = (size_t)it * 33554432u;
        slow_rows(Q + qoff + x + w * 32768u, K + hoff + x, V + hoff + x, O + qoff + x + w * 32768u, lds + w * 9216u, lds + 36864u + w * 9216u, lane);
      }
  }
}

extern "C" void kernel_launch(void* const* d_in, const int* in_sizes, int n_in, void* d_out, int out_size, void* d_ws, size_t ws_size, hipStream_t stream) {
  mha_tp<<<256, 256, 0, stream>>>((const char*)d_in[0], (const char*)d_in[1], (const char*)d_in[2], (char*)d_out);
}
```
